```python
import torch

def _softmax(x, axis=-1):
    return torch.softmax(x, dim=axis)
def _gelu(x):
    return torch.nn.functional.gelu(x, approximate="tanh")
def _silu(x):
    return torch.nn.functional.silu(x)
def _sigmoid(x):
    return torch.sigmoid(x)
def _layernorm(x, axis=-1, eps=1e-5):
    m = x.mean(dim=axis, keepdim=True)
    v = x.var(dim=axis, keepdim=True, unbiased=False)
    return (x - m) / torch.sqrt(v + eps)
def _scatter_add(tgt, idx, src, axis=0):
    return tgt.clone().index_add_(axis, idx, src)
def _segment_sum(src, seg, n):
    out = torch.zeros((n,) + src.shape[1:], dtype=src.dtype)
    return out.index_add_(0, seg, src)
def _pad(x, pw):
    flat = tuple(v for pair in reversed(pw) for v in pair)
    return torch.nn.functional.pad(x, flat)

kernel_name = 'gated_mix__compressed_kv_cla__linear_kernel_hedgehog__prompt'

def setup_inputs():

    _w = (lambda a: (lambda t: t.to(torch.bfloat16) if t.is_floating_point() else t)(torch.as_tensor(a)))
    def _A_setup():
        B, H, Sq, Sk, D, M = 1, 32, 2048, 2048, 128, 512
        Dc = 4 * D
        return {
            "x": _w((lambda *s: torch.randn(*s, dtype=torch.bfloat16) * 0.05)(B, Sq, M)),
            "c_kv": _w((lambda *s: torch.randn(*s, dtype=torch.bfloat16) * 0.8)(B, Sk, Dc)),
            "w_q": _w((lambda *s: torch.randn(*s, dtype=torch.bfloat16) * 6.4)(M, H, D) * (M ** -0.5)),
            "w_uk": _w((lambda *s: torch.randn(*s, dtype=torch.bfloat16) * 6.4)(Dc, H, D) * (Dc ** -0.5)),
            "w_uv": _w((lambda *s: torch.randn(*s, dtype=torch.bfloat16) * 0.8)(Dc, H, D) * (Dc ** -0.5)),
            "w_o": _w((lambda *s: torch.randn(*s, dtype=torch.bfloat16) * 0.8)(H, D, M) * ((H * D) ** -0.5)),
        }
    _A_in = _A_setup()

    def _B_setup():
        B, S, D, H = 1, 2048, 512, 32
        Dh = D // H
        return {
            "x": _w((lambda *s: torch.randn(*s, dtype=torch.bfloat16) * 0.05)(B, S, D)),
            "wqkv": _w((lambda *s: torch.randn(*s, dtype=torch.bfloat16) * 1.6)(D, 3 * D)),
            "w_phi": _w((lambda *s: torch.randn(*s, dtype=torch.bfloat16) * 1.6)(H, Dh, Dh) * (Dh ** -0.5)),
            "wo": _w((lambda *s: torch.randn(*s, dtype=torch.bfloat16) * 0.05)(D, D)),
        }
    _B_in = _B_setup()

    return {
        "A_x": _A_in["x"],
        "A_c_kv": _A_in["c_kv"],
        "A_w_q": _A_in["w_q"],
        "A_w_uk": _A_in["w_uk"],
        "A_w_uv": _A_in["w_uv"],
        "A_w_o": _A_in["w_o"],
        "B_x": _B_in["x"],
        "B_wqkv": _B_in["wqkv"],
        "B_w_phi": _B_in["w_phi"],
        "B_wo": _B_in["wo"],
        "gate_w": _w((lambda *s: torch.randn(*s, dtype=torch.bfloat16) * 0.05)(512) * 16.0),
    }

def reference(A_x, A_c_kv, A_w_q, A_w_uk, A_w_uv, A_w_o, B_x, B_wqkv, B_w_phi, B_wo, gate_w):
    def _A_ref(x, c_kv, w_q, w_uk, w_uv, w_o):
        B, Sq, M = x.shape[0], x.shape[1], x.shape[2]
        Sk = c_kv.shape[1]
        H, D = w_q.shape[1], w_q.shape[2]
        q = torch.einsum("bqm,mhd->bhqd", x, w_q)
        k = torch.einsum("bkc,chd->bhkd", c_kv, w_uk)
        v = torch.einsum("bkc,chd->bhkd", c_kv, w_uv)
        scores = torch.einsum("bhqd,bhkd->bhqk", q, k) * (D ** -0.5)
        off = Sk - Sq
        qi = torch.arange(Sq)[:, None]
        kj = torch.arange(Sk)[None, :]
        cmask = kj <= (qi + off)
        scores = torch.where(cmask[None, None], scores, -1e9)
        p = _softmax(scores, axis=-1)
        ctx = torch.einsum("bhqk,bhkd->bhqd", p, v)
        return torch.einsum("bhqd,hdm->bqm", ctx, w_o)

    def _B_ref(x, wqkv, w_phi, wo):
        B, S, D = x.shape
        H = 32; Dh = D // H
        qkv = (lambda x, *d: x.permute(*d))((x @ wqkv).reshape(B, S, 3, H, Dh), 2, 0, 3, 1, 4)
        q, k, v = qkv[0], qkv[1], qkv[2]
        pq = torch.einsum("bhsd,hdf->bhsf", q, w_phi)
        pk = torch.einsum("bhsd,hdf->bhsf", k, w_phi)
        phi_q = torch.concatenate([_softmax(pq, axis=-1), _softmax(-pq, axis=-1)], axis=-1)
        phi_k = torch.concatenate([_softmax(pk, axis=-1), _softmax(-pk, axis=-1)], axis=-1)
        kv = torch.einsum("bhsf,bhsd->bhfd", phi_k, v)
        z  = torch.einsum("bhsf->bhf", phi_k)
        num = torch.einsum("bhqf,bhfd->bhqd", phi_q, kv)
        den = torch.einsum("bhqf,bhf->bhq", phi_q, z)[..., None] + 1e-6
        o = num / den
        return (lambda x, *d: x.permute(*d))(o, 0, 2, 1, 3).reshape(B, S, D) @ wo

    x = A_x
    g = _sigmoid((x * gate_w).sum(-1, keepdims=True))
    a = _A_ref(x, A_c_kv, A_w_q, A_w_uk, A_w_uv, A_w_o)
    b = _B_ref(x, B_wqkv, B_w_phi, B_wo)
    return g * a + (1 - g) * b
```

```cpp
#include <hip/hip_runtime.h>
#include <hip/hip_bf16.h>
#include <stdint.h>

typedef unsigned short u16;
typedef short bf16x8 __attribute__((ext_vector_type(8)));
typedef short s16x4 __attribute__((ext_vector_type(4)));
typedef float f32x2 __attribute__((ext_vector_type(2)));
typedef float f32x4 __attribute__((ext_vector_type(4)));
typedef float f32x16 __attribute__((ext_vector_type(16)));
typedef unsigned u32x4 __attribute__((ext_vector_type(4)));
typedef unsigned u32x2 __attribute__((ext_vector_type(2)));
typedef __bf16 bf16x2_t __attribute__((ext_vector_type(2)));
#define LAS __attribute__((address_space(3)))
#define DI __device__ __forceinline__
typedef LAS const char* lds_cptr;
typedef LAS char* lds_ptr;

constexpr int SEQ = 2048, DM = 512, NH = 32, HD = 128;
constexpr size_t WS_KV = 0;
constexpr size_t WS_A3 = WS_KV + (size_t)NH * 32 * 32768;
constexpr size_t WS_KVP = WS_A3 + (size_t)576 * SEQ * 16;
constexpr size_t WS_END = WS_KVP + (size_t)NH * 8 * 4096;
constexpr int LDS_BYTES = 163840;

#define MFMA32(a, b, c) __builtin_amdgcn_mfma_f32_32x32x16_bf16((a), (b), (c), 0, 0, 0)
#define MFMA16(a, b, c) __builtin_amdgcn_mfma_f32_16x16x32_bf16((a), (b), (c), 0, 0, 0)
#define SBAR() __builtin_amdgcn_sched_barrier(0)
#define WAIT_V(n) asm volatile("s_waitcnt vmcnt(" #n ")" ::: "memory")
#define WAIT_L(n) asm volatile("s_waitcnt lgkmcnt(" #n ")" ::: "memory")
#define BAR() __builtin_amdgcn_s_barrier()
#define WAIT_BAR(N) asm volatile("s_waitcnt vmcnt(" #N ") lgkmcnt(0)\n\ts_barrier" ::: "memory")

DI unsigned rfl(unsigned x) { return __builtin_amdgcn_readfirstlane(x); }
DI unsigned cvtpk(float lo, float hi) { f32x2 v = {lo, hi}; bf16x2_t b = __builtin_convertvector(v, bf16x2_t); return __builtin_bit_cast(unsigned, b); }
DI float bfround(float x) { return __uint_as_float(cvtpk(x, x) << 16); }
DI void glds16(const void* sbase, unsigned voff, unsigned lds_dst) {
    unsigned sv;
    asm volatile("s_mov_b32 %0, m0\n\ts_mov_b32 m0, %3\n\ts_nop 0\n\tglobal_load_lds_dwordx4 %1, %2\n\ts_mov_b32 m0, %0"
                 : "=&s"(sv) : "v"(voff), "s"(sbase), "s"(lds_dst) : "memory");
}
typedef short v4i16_t __attribute__((ext_vector_type(4)));
DI s16x4 trrd(lds_cptr p) { return __builtin_bit_cast(s16x4, __builtin_amdgcn_ds_read_tr16_b64_v4i16((LAS v4i16_t*)p)); }
DI bf16x8 cat8(s16x4 a, s16x4 b) { return __builtin_shufflevector(a, b, 0, 1, 2, 3, 4, 5, 6, 7); }
DI bf16x8 ldsr128(lds_cptr p) { return *(const LAS bf16x8*)p; }
DI bf16x8 pack8(float a0, float a1, float a2, float a3, float a4, float a5, float a6, float a7) {
    u32x4 w = {cvtpk(a0, a1), cvtpk(a2, a3), cvtpk(a4, a5), cvtpk(a6, a7)}; return __builtin_bit_cast(bf16x8, w); }
#define PACK8(X, B) pack8((X)[(B)], (X)[(B) + 1], (X)[(B) + 2], (X)[(B) + 3], (X)[(B) + 4], (X)[(B) + 5], (X)[(B) + 6], (X)[(B) + 7])
DI float swap32_max(float v) { auto rr = __builtin_amdgcn_permlane32_swap(__float_as_uint(v), __float_as_uint(v), false, false); return fmaxf(__uint_as_float(rr[0]), __uint_as_float(rr[1])); }
DI float swap32_add(float v) { auto rr = __builtin_amdgcn_permlane32_swap(__float_as_uint(v), __float_as_uint(v), false, false); return __uint_as_float(rr[0]) + __uint_as_float(rr[1]); }
DI float lo32_bcast(float v) { auto rr = __builtin_amdgcn_permlane32_swap(__float_as_uint(v), __float_as_uint(v), false, false); return __uint_as_float(rr[0]); }
template <int CTRL> DI float dpp(float v) { return __uint_as_float(__builtin_amdgcn_update_dpp(0u, __float_as_uint(v), CTRL, 0xF, 0xF, true)); }
DI float row16_max(float v) { v = fmaxf(v, dpp<0xB1>(v)); v = fmaxf(v, dpp<0x4E>(v)); v = fmaxf(v, dpp<0x141>(v)); v = fmaxf(v, dpp<0x140>(v)); return v; }
DI float row16_add(float v) { v += dpp<0xB1>(v); v += dpp<0x4E>(v); v += dpp<0x141>(v); v += dpp<0x140>(v); return v; }

__global__ __launch_bounds__(512, 2) void k1_proj(const u16* __restrict__ x, const u16* __restrict__ ckv, const u16* __restrict__ wuk,
                                                  const u16* __restrict__ wuv, const u16* __restrict__ wqkv, const u16* __restrict__ wphi,
                                                  char* __restrict__ KV, float* __restrict__ KVP) {
    extern __shared__ __attribute__((aligned(16))) char shm[];
    const lds_ptr lds = (lds_ptr)shm; const unsigned lds0 = (unsigned)(uintptr_t)shm;
    const int tid = threadIdx.x, wid = rfl(tid >> 6), lane = tid & 63, wr = wid >> 2, wc = wid & 3, fr = lane & 15, fq = lane >> 4;
    const int bid = blockIdx.x, hh_ = 4 * (bid & 7) + (bid >> 6), it = (bid >> 3) & 7;
    constexpr int HTB = 16384;
#define SA(b, h) (((b) * 2 + (h)) * HTB)
#define SB(b, h) ((4 + (b) * 2 + (h)) * HTB)
    {
        const int row = 64 * wid + (lane >> 2), sl = lane & 3;
        const unsigned vo = (unsigned)(row * 1536 + 512 + 512 * (sl >> 1) + 16 * hh_ + 8 * (sl & 1)) * 2u;
#pragma unroll
        for (int e = 0; e < 4; ++e) glds16((const char*)wqkv + (size_t)e * 16 * 3072, vo, rfl(lds0 + 131072 + wid * 4096 + e * 1024));
    }
    unsigned voffA, voffB;
    { const int row = 8 * wid + (lane >> 3), ch = (lane & 7) ^ ((row >> 1) & 7); voffA = (unsigned)(row * 1024 + ch * 16); }
    { const int row = 4 * wid + (lane >> 4), j = lane & 15, s = ((row >> 3) & 1) * 4 + (row & 3), c = (j >> 1) ^ s; voffB = (unsigned)(row * 8192 + c * 32 + (j & 1) * 16); }
    const char* gA = (const char*)ckv + (size_t)it * 256 * 1024;
    const char* gB0 = (const char*)wuk + hh_ * 256; const char* gB1 = (const char*)wuv + hh_ * 256;
    const unsigned ldsw = rfl(lds0 + wid * 1024);
#define STAGE_A(b, h, kt) do { const char* g_ = gA + (h) * 131072 + (kt) * 128; glds16(g_, voffA, ldsw + SA(b, h)); glds16(g_ + 65536, voffA, ldsw + SA(b, h) + 8192); } while (0)
#define STAGE_B(b, h, kt) do { const char* g_ = ((h) ? gB1 : gB0) + (size_t)(kt) * 524288; glds16(g_, voffB, ldsw + SB(b, h)); glds16(g_ + 262144, voffB, ldsw + SB(b, h) + 8192); } while (0)
    lds_cptr pa[2], pb[2];
#pragma unroll
    for (int ks = 0; ks < 2; ++ks) pa[ks] = lds + (wr * 64 + fr) * 128 + 16 * ((4 * ks + fq) ^ ((fr >> 1) & 7));
    { const int q = fr >> 2, p = fr & 3, s = (fq & 1) * 4 + q;
#pragma unroll
      for (int n = 0; n < 2; ++n) pb[n] = lds + (8 * fq + q) * 256 + 32 * ((2 * wc + n) ^ s) + 8 * p; }
#define LDA(dst, b, h) do { _Pragma("unroll") for (int m = 0; m < 4; ++m) _Pragma("unroll") for (int k = 0; k < 2; ++k) dst[m][k] = ldsr128(pa[k] + SA(b, h) + m * 2048); } while (0)
#define LDB(dst, b, h) do { _Pragma("unroll") for (int n = 0; n < 2; ++n) _Pragma("unroll") for (int k = 0; k < 2; ++k) \
        dst[n][k] = cat8(trrd(pb[n] + SB(b, h) + k * 8192), trrd(pb[n] + SB(b, h) + k * 8192 + 1024)); } while (0)
#define MMA(ai, bj, At, Bt) do { __builtin_amdgcn_s_setprio(1); _Pragma("unroll") for (int m = 0; m < 4; ++m) _Pragma("unroll") for (int n = 0; n < 2; ++n) _Pragma("unroll") for (int k = 0; k < 2; ++k) \
        acc[ai][bj][m][n] = (bj) ? MFMA16(At[m][k], Bt[n][k], acc[ai][bj][m][n]) : MFMA16(Bt[n][k], At[m][k], acc[ai][bj][m][n]); __builtin_amdgcn_s_setprio(0); } while (0)
    f32x4 acc[2][2][4][2] = {};
    bf16x8 At[4][2], B0[2][2], B1[2][2];
    constexpr int nt = 8;
    STAGE_B(0, 0, 0); STAGE_A(0, 0, 0); STAGE_B(0, 1, 0); STAGE_A(0, 1, 0);
    if (wr == 1) BAR();
    WAIT_V(4); BAR();
    STAGE_B(1, 0, 1); STAGE_A(1, 0, 1); STAGE_B(1, 1, 1);
    WAIT_V(6); BAR();
    for (int t = 0; t < nt - 2; t += 2) {
        LDB(B0, 0, 0); SBAR(); LDA(At, 0, 0); STAGE_A(1, 1, t + 1);
        WAIT_L(8); BAR(); WAIT_L(0); MMA(0, 0, At, B0); BAR(); SBAR();
        LDB(B1, 0, 1); STAGE_B(0, 0, t + 2);
        BAR(); WAIT_L(0); MMA(0, 1, At, B1); BAR();
        LDA(At, 0, 1); STAGE_A(0, 0, t + 2);
        BAR(); WAIT_L(0); MMA(1, 0, At, B0); BAR(); SBAR();
        STAGE_B(0, 1, t + 2);
        WAIT_V(6); BAR(); MMA(1, 1, At, B1); BAR();
        LDB(B0, 1, 0); SBAR(); LDA(At, 1, 0); STAGE_A(0, 1, t + 2);
        WAIT_L(8); BAR(); WAIT_L(0); MMA(0, 0, At, B0); BAR(); SBAR();
        LDB(B1, 1, 1); STAGE_B(1, 0, t + 3);
        BAR(); WAIT_L(0); MMA(0, 1, At, B1); BAR();
        LDA(At, 1, 1); STAGE_A(1, 0, t + 3);
        BAR(); WAIT_L(0); MMA(1, 0, At, B0); BAR(); SBAR();
        STAGE_B(1, 1, t + 3);
        WAIT_V(6); BAR(); MMA(1, 1, At, B1); BAR();
    }
    { LDB(B0, 0, 0); WAIT_V(0); LDA(At, 0, 0); STAGE_A(1, 1, nt - 1);
      BAR(); WAIT_L(0); MMA(0, 0, At, B0); BAR();
      LDB(B1, 0, 1); BAR(); WAIT_L(0); MMA(0, 1, At, B1); BAR();
      LDA(At, 0, 1); BAR(); WAIT_L(0); MMA(1, 0, At, B0); MMA(1, 1, At, B1); BAR(); }
    { LDB(B0, 1, 0); LDA(At, 1, 0); BAR(); WAIT_L(0); MMA(0, 0, At, B0); BAR();
      LDB(B1, 1, 1); WAIT_V(0); BAR(); WAIT_L(0); MMA(0, 1, At, B1); BAR();
      LDA(At, 1, 1); BAR(); WAIT_L(0); MMA(1, 0, At, B0); MMA(1, 1, At, B1); BAR(); }
    if (wr == 0) BAR();
#pragma unroll
    for (int ai = 0; ai < 2; ++ai) {
        char* tb = KV + (size_t)(hh_ * 32 + 4 * it + 2 * ai + wr) * 32768;
#pragma unroll
        for (int m = 0; m < 4; ++m) {
            const f32x4 a = acc[ai][0][m][0], b = acc[ai][0][m][1];
            u32x4 w = {cvtpk(a[0], a[1]), cvtpk(a[2], a[3]), cvtpk(b[0], b[1]), cvtpk(b[2], b[3])};
            *(u32x4*)(tb + (4 * wc + fq) * 1024 + (m * 16 + fr) * 16) = w;
        }
#pragma unroll
        for (int u = 0; u < 2; ++u)
#pragma unroll
            for (int n = 0; n < 2; ++n) {
                const f32x4 a = acc[ai][1][2 * u][n], b = acc[ai][1][2 * u + 1][n];
                u32x4 w = {cvtpk(a[0], a[1]), cvtpk(a[2], a[3]), cvtpk(b[0], b[1]), cvtpk(b[2], b[3])};
                *(u32x4*)(tb + 16384 + (4 * u + fq) * 2048 + (wc * 32 + n * 16 + fr) * 16) = w;
            }
    }
#undef SA
#undef SB
#undef STAGE_A
#undef STAGE_B
#undef LDA
#undef LDB
#undef MMA
    {
        const int r32 = lane & 31, hi = lane >> 5, blk = (lane >> 4) & 1, q = fr >> 2, p = fr & 3;
        const u16* xr = x + (size_t)(256 * it + 32 * wid + r32) * 512 + 32 * hi;
        const lds_cptr wp = lds + 131072 + (32 * hi + q) * 64 + 32 * blk + 8 * p;
        f32x16 d1 = {}, d2 = {};
#pragma unroll
        for (int kap = 0; kap < 8; ++kap) {
            bf16x8 xf[4];
#pragma unroll
            for (int e = 0; e < 4; ++e) xf[e] = *(const bf16x8*)(xr + 64 * kap + 8 * e);
#pragma unroll
            for (int e = 0; e < 4; ++e) {
                const bf16x8 wf = cat8(trrd(wp + kap * 4096 + e * 512), trrd(wp + kap * 4096 + e * 512 + 256));
                d1 = MFMA32(xf[e], wf, d1); d2 = MFMA32(wf, xf[e], d2);
            }
        }
        bf16x8 wpf;
#pragma unroll
        for (int j = 0; j < 8; ++j) { const int d = 8 * (j >> 2) + 4 * hi + (j & 3); const u16 v = wphi[hh_ * 256 + d * 16 + fr]; wpf[j] = (short)(blk ? (v ^ 0x8000) : v); }
        const bf16x8 kp = PACK8(d2, 0);
        f32x16 d3 = MFMA32(kp, wpf, f32x16{});
#pragma unroll
        for (int r = 0; r < 16; ++r) {
            const float v = bfround(d3[r]); const float mx = row16_max(v);
            const float e = __builtin_amdgcn_exp2f((v - mx) * 1.4426950408889634f); const float s = row16_add(e);
            d3[r] = e * __builtin_amdgcn_rcpf(s);
        }
        const bf16x8 ones = {0x3F80, 0x3F80, 0x3F80, 0x3F80, 0x3F80, 0x3F80, 0x3F80, 0x3F80};
        const bf16x8 v0 = PACK8(d1, 0), v1 = PACK8(d1, 8);
        f32x16 d4 = MFMA32(PACK8(d3, 0), blk ? v0 : ones, f32x16{});
        d4 = MFMA32(PACK8(d3, 8), blk ? v1 : ones, d4);
        LAS float* red = (LAS float*)(lds + wid * 4096);
#pragma unroll
        for (int r = 0; r < 16; ++r) red[((r & 3) + 8 * (r >> 2) + 4 * hi) * 32 + r32] = d4[r];
        WAIT_L(0); BAR(); asm volatile("" ::: "memory");
        f32x2 s = {0.f, 0.f};
#pragma unroll
        for (int w = 0; w < 8; ++w) s += *(const LAS f32x2*)(lds + w * 4096 + tid * 8);
        *(f32x2*)(KVP + (size_t)(hh_ * 8 + it) * 1024 + tid * 2) = s;
    }
}

constexpr float QSCALE = 0.08838834764831845f * 1.4426950408889634f;
constexpr float THR = 8.f;
constexpr int L_QB = 0, L_V = 32768, L_K1 = 98304, L_K0 = 131072;
constexpr int L_GATE = 102400, L_KVS = 103424;

struct AttnState { f32x16 o[4]; float m, l; };

template <int G>
DI void qk_tile(f32x16& s0, f32x16& s1, lds_cptr kp, const bf16x8* qf, const f32x16& negm) {
#pragma unroll
    for (int s = 0; s < 8; ++s) {
        const bf16x8 a0 = ldsr128(kp + s * 2048), a1 = ldsr128(kp + s * 2048 + 512);
        s0 = MFMA32(a0, qf[s], s == 0 ? negm : s0);
        s1 = MFMA32(a1, qf[s], s == 0 ? negm : s1);
    }
}
DI void pv_tile(f32x16* o, lds_cptr vp, const bf16x8* pf) {
#pragma unroll
    for (int ks = 0; ks < 4; ++ks)
#pragma unroll
        for (int c = 0; c < 4; ++c) o[c] = MFMA32(ldsr128(vp + ks * 4096 + c * 512), pf[ks], o[c]);
}
#define MX3(a, b, c) __builtin_fmaxf(__builtin_fmaxf((a), (b)), (c))
DI void mask_tile(f32x16& s0, f32x16& s1, int dq) {
#pragma unroll
    for (int r = 0; r < 16; ++r) { const int kk = 16 * ((r >> 2) & 1) + 8 * (r >> 3) + (r & 3);
        if (kk > dq) s0[r] = -INFINITY; if (kk + 32 > dq) s1[r] = -INFINITY; }
}
DI void softmax_tile(f32x16& s0, f32x16& s1, f32x16* o, f32x16& negm, float& m, float& l, bf16x8* pf, bool first) {
    float a = MX3(s0[0], s0[1], s1[0]), b = MX3(s0[2], s0[3], s1[1]); a = MX3(a, s1[2], s1[3]);
#pragma unroll
    for (int r = 4; r < 16; r += 4) { a = MX3(a, s0[r], s0[r + 1]); b = MX3(b, s0[r + 2], s0[r + 3]); a = MX3(a, s1[r], s1[r + 1]); b = MX3(b, s1[r + 2], s1[r + 3]); }
    const float rm = swap32_max(fmaxf(a, b));
    if (first || __builtin_expect(__any(rm > THR), 0)) {
        const float dl = first ? rm : fmaxf(rm, 0.f);
        m += dl;
#pragma unroll
        for (int r = 0; r < 16; ++r) { s0[r] -= dl; s1[r] -= dl; negm[r] = -m; }
        if (!first) {
            const float f = __builtin_amdgcn_exp2f(-dl);
            l *= f;
#pragma unroll
            for (int c = 0; c < 4; ++c)
#pragma unroll
                for (int r = 0; r < 16; ++r) o[c][r] *= f;
        }
    }
    float sa = 0.f, sb = 0.f;
#pragma unroll
    for (int r = 0; r < 16; ++r) { s0[r] = __builtin_amdgcn_exp2f(s0[r]); sa += s0[r]; }
#pragma unroll
    for (int r = 0; r < 16; ++r) { s1[r] = __builtin_amdgcn_exp2f(s1[r]); sb += s1[r]; }
    l += sa + sb;
    pf[0] = PACK8(s0, 0); pf[1] = PACK8(s0, 8); pf[2] = PACK8(s1, 0); pf[3] = PACK8(s1, 8);
}

template <int G>
DI void attn_passes(const lds_ptr lds, const unsigned lds0, const char* kvh, char* A3, bf16x8* qf, const float gate, const int hh_, const int bA, const int bB,
                    const int w4, const int lane) {
    const int r32 = lane & 31, hi = lane >> 5, wid = G * 4 + w4;
    const unsigned vlin = (unsigned)lane * 16u;
    const int prow = (r32 & 7) | ((r32 & 8) << 1) | ((r32 & 16) >> 1);
    const unsigned kro = (unsigned)(prow * 16 + hi * 1024), vro = (unsigned)(r32 * 16 + hi * 2048);
    const char* kvw = kvh + w4 * 4096;
    const unsigned ldw = lds0 + w4 * 4096;
    auto dmaK = [&](int tile, int par) {
        const char* g = kvw + (size_t)tile * 32768; const unsigned d = ldw + (par ? L_K1 : L_K0) + G * 16384;
#pragma unroll
        for (int e = 0; e < 4; ++e) glds16(g + e * 1024, vlin, d + e * 1024); };
    auto dmaV = [&](int tile, int par) {
        const char* g = kvw + (size_t)tile * 32768 + 16384; const unsigned d = ldw + L_V + (G * 2 + par) * 16384;
#pragma unroll
        for (int e = 0; e < 4; ++e) glds16(g + e * 1024, vlin, d + e * 1024); };
    for (int pass = 0; pass < 2; ++pass) {
        const int b = pass ? bB : bA;
        if (pass) {
#pragma unroll
            for (int s = 0; s < 8; ++s) qf[s] = ldsr128(lds + L_QB + w4 * 8192 + lane * 16 + s * 1024);
            dmaK(G, 0);
        }
        if (G == 0) dmaV(0, 0);
        f32x16 o[4] = {}; f32x16 negm = {}; float m = 0.f, l = 0.f; bf16x8 pf[4]; f32x16 s0, s1;
        const int dq = 32 * w4 + r32 - 64 * G - 4 * hi;
        WAIT_BAR(0);
        if (G == 0) {
            for (int r = 0; r <= b; ++r) {
                const int par = r & 1;
                if (r < b) { dmaK(2 * r + 2, par ^ 1); dmaV(2 * r + 2, par ^ 1); }
                qk_tile<0>(s0, s1, lds + (par ? L_K1 : L_K0) + kro, qf, negm);
                if (r == b && w4 < 2) mask_tile(s0, s1, dq);
                softmax_tile(s0, s1, o, negm, m, l, pf, r == 0);
                pv_tile(o, lds + L_V + par * 16384 + vro, pf);
                WAIT_BAR(0);
            }
        } else {
            const bool last_act = w4 >= 2;
            for (int r = 0; r <= b; ++r) {
                const int par = r & 1;
                if (r < b) dmaK(2 * r + 3, par ^ 1);
                dmaV(2 * r + 1, par);
                if (r > 0) pv_tile(o, lds + L_V + (2 + (par ^ 1)) * 16384 + vro, pf);
                if (r < b || last_act) {
                    qk_tile<1>(s0, s1, lds + (par ? L_K1 : L_K0) + 16384 + kro, qf, negm);
                    if (r == b) mask_tile(s0, s1, dq);
                    softmax_tile(s0, s1, o, negm, m, l, pf, r == 0);
                }
                WAIT_BAR(0);
            }
            if (last_act) pv_tile(o, lds + L_V + (2 + (b & 1)) * 16384 + vro, pf);
            else if (b == 0) m = -1e30f;
        }
        l = swap32_add(l);
        WAIT_L(0); BAR(); asm volatile("" ::: "memory");
        { const lds_ptr xp = lds + L_V + wid * 8192 + lane * 16;
#pragma unroll
          for (int c = 0; c < 2; ++c)
#pragma unroll
              for (int g = 0; g < 4; ++g) { const f32x16 src = o[G ? c : 2 + c]; *(LAS f32x4*)(xp + (c * 4 + g) * 1024) = (f32x4){src[4 * g], src[4 * g + 1], src[4 * g + 2], src[4 * g + 3]}; }
          *(LAS f32x4*)(lds + L_K1 + wid * 1024 + lane * 16) = (f32x4){m, l, gate, 0.f}; }
        WAIT_L(0); BAR(); asm volatile("" ::: "memory");
        { const int ow = wid ^ 4;
          const f32x4 st = *(const LAS f32x4*)(lds + L_K1 + ow * 1024 + lane * 16);
          const float mn = fmaxf(m, st[0]), f0 = __builtin_amdgcn_exp2f(m - mn), f1 = __builtin_amdgcn_exp2f(st[0] - mn);
          const float gg = (G == pass) ? gate : st[2];
          const float inv = gg / (l * f0 + st[1] * f1); const float c0 = f0 * inv, c1 = f1 * inv;
          const lds_cptr xp = lds + L_V + ow * 8192 + lane * 16;
          char* dst = A3 + ((size_t)(hh_ * 16 + G * 8 + hi) * SEQ + b * 128 + 32 * w4 + r32) * 16;
#pragma unroll
          for (int c = 0; c < 2; ++c) {
              const f32x16 mine = o[G ? 2 + c : c]; float v[16];
#pragma unroll
              for (int g = 0; g < 4; ++g) { const f32x4 t = *(const LAS f32x4*)(xp + (c * 4 + g) * 1024);
#pragma unroll
                  for (int e = 0; e < 4; ++e) v[4 * g + e] = mine[4 * g + e] * c0 + t[e] * c1; }
              *(bf16x8*)(dst + (size_t)(4 * c) * SEQ * 16) = PACK8(v, 0);
              *(bf16x8*)(dst + (size_t)(4 * c + 2) * SEQ * 16) = PACK8(v, 8);
          } }
        WAIT_L(0); BAR(); asm volatile("" ::: "memory");
    }
}

__global__ __launch_bounds__(512, 2) void k2_attn(const u16* __restrict__ x, const u16* __restrict__ wq, const u16* __restrict__ wqkv,
                                                  const u16* __restrict__ wphi, const u16* __restrict__ gatew, const char* __restrict__ KV,
                                                  const float* __restrict__ KVP, char* __restrict__ A3) {
    extern __shared__ __attribute__((aligned(16))) char shm[];
    const lds_ptr lds = (lds_ptr)shm; const unsigned lds0 = (unsigned)(uintptr_t)shm;
    const int tid = threadIdx.x, wid = rfl(tid >> 6), lane = tid & 63, G = wid >> 2, w4 = wid & 3;
    const int r32 = lane & 31, hi = lane >> 5, blk = (lane >> 4) & 1, q4 = (lane & 15) >> 2, p4 = lane & 3;
    const int bid = blockIdx.x, hh_ = 4 * (bid & 7) + (bid >> 6), jj = (bid >> 3) & 7;
    const int bA = 15 - jj, bB = jj;
    const int myrow0 = (G ? bB : bA) * 128 + 32 * w4;
    const char* kvh = KV + (size_t)hh_ * 32 * 32768;
    const unsigned vlin = (unsigned)lane * 16u;
    bf16x8 qf[8]; float gate;
    {
        f32x2 ps[8];
#pragma unroll
        for (int w = 0; w < 8; ++w) ps[w] = *(const f32x2*)(KVP + (size_t)(hh_ * 8 + w) * 1024 + tid * 2);
        if (wid == 0) glds16(gatew, vlin, rfl(lds0 + L_GATE));
        unsigned voffX, voffW, voffE;
        { const int row = lane >> 3, ch = (lane & 7) ^ ((row >> 1) & 3); voffX = (unsigned)(row * 1024 + ch * 16); }
        { const int row = 4 * wid + (lane >> 4), j = lane & 15, c = (j >> 2) ^ (row & 3); voffW = (unsigned)(row * 8192 + c * 64 + (j & 3) * 16); }
        { const int row = 8 * wid + (lane >> 1); voffE = (unsigned)(row * 3072 + (lane & 1) * 16); }
        const char* gX = (const char*)x + (size_t)myrow0 * 1024; const char* gW = (const char*)wq + hh_ * 256; const char* gE = (const char*)wqkv + hh_ * 32;
        auto slot_of = [&](int c) -> unsigned { const int s = c % 3; return s == 0 ? 0u : (s == 1 ? 51200u : 112640u); };
        auto issue = [&](int c) {
            const unsigned sb = lds0 + slot_of(c);
#pragma unroll
            for (int e = 0; e < 4; ++e) glds16(gX + e * 8192 + c * 128, voffX ^ ((e & 1) << 6), rfl(sb + wid * 4096 + e * 1024));
            glds16(gW + (size_t)c * 524288, voffW, rfl(sb + 32768 + wid * 1024));
            glds16(gW + (size_t)c * 524288 + 262144, voffW, rfl(sb + 32768 + 8192 + wid * 1024));
            if (lane < 16) glds16(gE + (size_t)c * 196608, voffE, rfl(sb + 49152 + wid * 256));
        };
        issue(0);
        { f32x2 s = ps[0];
#pragma unroll
          for (int w = 1; w < 8; ++w) s += ps[w];
          *(LAS f32x2*)(lds + L_KVS + tid * 8) = s; }
        issue(1);
        f32x16 qa[5] = {};
        const int rx = 32 * wid + r32;
        unsigned xo[2], wo_[4];
#pragma unroll
        for (int s = 0; s < 2; ++s) xo[s] = (unsigned)(rx * 128 + 16 * ((2 * s + hi) ^ ((r32 >> 1) & 7)));
#pragma unroll
        for (int c = 0; c < 4; ++c) wo_[c] = (unsigned)(32768 + (8 * hi + q4) * 256 + 64 * (c ^ q4) + 32 * (p4 >> 1) + 16 * blk + 8 * (p4 & 1));
        const unsigned eo = (unsigned)(49152 + (8 * hi + q4) * 32 + 8 * p4);
        for (int c = 0; c < 8; ++c) {
            if (c < 7) { WAIT_BAR(7); } else { WAIT_BAR(4); }
            if (c + 2 < 8) issue(c + 2);
            if (c == 6) {
#pragma unroll
                for (int e = 0; e < 4; ++e) glds16(kvh + (size_t)G * 32768 + (w4 * 4 + e) * 1024, vlin, rfl(lds0 + L_K0 + G * 16384 + (w4 * 4 + e) * 1024));
            }
            const lds_cptr sb = lds + slot_of(c);
#pragma unroll
            for (int ks = 0; ks < 4; ++ks) {
                const bf16x8 xf = ldsr128(sb + (xo[ks & 1] ^ ((ks >> 1) << 6)));
#pragma unroll
                for (int cc = 0; cc < 4; ++cc) {
                    const bf16x8 wf = cat8(trrd(sb + wo_[cc] + ks * 4096), trrd(sb + wo_[cc] + ks * 4096 + 1024));
                    qa[cc] = MFMA32(wf, xf, qa[cc]);
                }
                bf16x8 ef = cat8(trrd(sb + eo + ks * 512), trrd(sb + eo + ks * 512 + 128));
                const bf16x8 gf = ldsr128(lds + L_GATE + (c * 64 + ks * 16 + 8 * hi) * 2);
                if (r32 == 16) ef = gf;
                qa[4] = MFMA32(ef, xf, qa[4]);
            }
        }
#pragma unroll
        for (int cc = 0; cc < 4; ++cc) {
#pragma unroll
            for (int r = 0; r < 16; ++r) qa[cc][r] *= QSCALE;
            qf[2 * cc] = PACK8(qa[cc], 0); qf[2 * cc + 1] = PACK8(qa[cc], 8);
        }
        { const float gz = bfround(lo32_bcast(qa[4][8])); gate = bfround(1.f / (1.f + __expf(-gz))); }
        bf16x8 wpf;
#pragma unroll
        for (int j = 0; j < 8; ++j) { const int d = 8 * (j >> 2) + 4 * hi + (j & 3); const u16 v = wphi[hh_ * 256 + d * 16 + (lane & 15)]; wpf[j] = (short)(blk ? (v ^ 0x8000) : v); }
        f32x16 pq = MFMA32(wpf, PACK8(qa[4], 0), f32x16{});
#pragma unroll
        for (int hf = 0; hf < 2; ++hf) {
            float mx = -INFINITY;
#pragma unroll
            for (int r = 0; r < 8; ++r) { pq[8 * hf + r] = bfround(pq[8 * hf + r]); mx = fmaxf(mx, pq[8 * hf + r]); }
            mx = swap32_max(mx); float s = 0.f;
#pragma unroll
            for (int r = 0; r < 8; ++r) { pq[8 * hf + r] = __builtin_amdgcn_exp2f((pq[8 * hf + r] - mx) * 1.4426950408889634f); s += pq[8 * hf + r]; }
            s = __builtin_amdgcn_rcpf(swap32_add(s));
#pragma unroll
            for (int r = 0; r < 8; ++r) pq[8 * hf + r] *= s;
        }
        f32x16 nm = {};
#pragma unroll
        for (int s = 0; s < 2; ++s) {
            float kvv[8];
            const int col = r32 < 16 ? 16 + r32 : 0;
#pragma unroll
            for (int j = 0; j < 8; ++j) { const int f = 16 * s + 8 * (j >> 2) + 4 * hi + (j & 3); const float v = *(const LAS float*)(lds + L_KVS + (f * 32 + col) * 4); kvv[j] = r32 <= 16 ? v : 0.f; }
            nm = MFMA32(PACK8(kvv, 0), PACK8(pq, 8 * s), nm);
        }
        { const float den = bfround(bfround(lo32_bcast(nm[8])) + 1e-6f); const float sc = (1.f - gate) / den;
          float ov[8];
#pragma unroll
          for (int r = 0; r < 8; ++r) ov[r] = bfround(nm[r]) * sc;
          *(bf16x8*)(A3 + ((size_t)(512 + 2 * hh_ + hi) * SEQ + myrow0 + r32) * 16) = PACK8(ov, 0); }
        WAIT_L(0); BAR(); asm volatile("" ::: "memory");
        { const lds_ptr qp = lds + (G ? L_QB : L_V) + w4 * 8192 + lane * 16;
#pragma unroll
          for (int s = 0; s < 8; ++s) *(LAS bf16x8*)(qp + s * 1024) = qf[s]; }
        WAIT_L(0); BAR(); asm volatile("" ::: "memory");
        if (G) {
#pragma unroll
            for (int s = 0; s < 8; ++s) qf[s] = ldsr128(lds + L_V + w4 * 8192 + lane * 16 + s * 1024);
        }
        WAIT_L(0); BAR(); asm volatile("" ::: "memory");
    }
    if (G == 0) attn_passes<0>(lds, lds0, kvh, A3, qf, gate, hh_, bA, bB, w4, lane);
    else        attn_passes<1>(lds, lds0, kvh, A3, qf, gate, hh_, bA, bB, w4, lane);
}

__global__ __launch_bounds__(512, 2) void k3_out(const char* __restrict__ A3, const u16* __restrict__ wo_a, const u16* __restrict__ wo_b, u16* __restrict__ out) {
    extern __shared__ __attribute__((aligned(16))) char shm[];
    const lds_ptr lds = (lds_ptr)shm; const unsigned lds0 = (unsigned)(uintptr_t)shm;
    const int tid = threadIdx.x, wid = rfl(tid >> 6), lane = tid & 63;
    const int r32 = lane & 31, hi = lane >> 5, blk = (lane >> 4) & 1, q4 = (lane & 15) >> 2, p4 = lane & 3;
    const int bid = blockIdx.x, rt = 4 * (bid & 7) + (bid >> 6), ct = (bid >> 3) & 7;
    const unsigned vlin = (unsigned)lane * 16u;
    unsigned voffW; { const int row = lane >> 3, sl = lane & 7, nb = (sl >> 2) ^ ((row >> 1) & 1); voffW = (unsigned)(row * 1024 + nb * 64 + (sl & 3) * 16); }
    const char* gA = A3 + (size_t)rt * 1024;
    const unsigned wb = lds0 + wid * 16384;
    auto issue = [&](int i) {
        const int kap = wid + 8 * i; const unsigned sb = wb + (i & 1) * 8192;
        const char* gw = (kap < 128 ? (const char*)wo_a + (size_t)kap * 32768 : (const char*)wo_b + (size_t)(kap - 128) * 32768) + ct * 128;
#pragma unroll
        for (int e = 0; e < 4; ++e) glds16(gA + (size_t)(4 * kap + e) * 32768, vlin, rfl(sb + e * 1024));
#pragma unroll
        for (int e = 0; e < 4; ++e) glds16(gw + e * 8192, voffW, rfl(sb + 4096 + e * 1024));
    };
    f32x16 acc[2][2] = {};
    const unsigned ao = (unsigned)(wid * 16384 + hi * 1024 + r32 * 16);
    unsigned bo[2];
#pragma unroll
    for (int nb = 0; nb < 2; ++nb) bo[nb] = (unsigned)(wid * 16384 + 4096 + (4 * hi + q4) * 128 + 64 * (nb ^ ((q4 >> 1) & 1)) + 32 * blk + 8 * p4);
    issue(0);
    for (int i = 0; i < 18; ++i) {
        if (i + 1 < 18) { issue(i + 1); WAIT_V(8); } else { WAIT_V(0); }
        const lds_cptr sb = lds + (i & 1) * 8192;
#pragma unroll
        for (int s = 0; s < 2; ++s) {
            bf16x8 af[2], bf[2];
#pragma unroll
            for (int mi = 0; mi < 2; ++mi) af[mi] = ldsr128(sb + ao + s * 2048 + mi * 512);
#pragma unroll
            for (int nb = 0; nb < 2; ++nb) bf[nb] = cat8(trrd(sb + bo[nb] + s * 2048), trrd(sb + bo[nb] + s * 2048 + 1024));
#pragma unroll
            for (int mi = 0; mi < 2; ++mi)
#pragma unroll
                for (int nb = 0; nb < 2; ++nb) acc[mi][nb] = MFMA32(bf[nb], af[mi], acc[mi][nb]);
        }
        WAIT_L(0);
    }
    BAR(); asm volatile("" ::: "memory");
#pragma unroll
    for (int mi = 0; mi < 2; ++mi)
#pragma unroll
        for (int nb = 0; nb < 2; ++nb)
#pragma unroll
            for (int g = 0; g < 4; ++g) { const int row = 32 * mi + r32, ch = 8 * nb + 2 * g + hi;
                *(LAS f32x4*)(lds + wid * 16384 + row * 256 + 16 * (ch ^ (row & 15))) = (f32x4){acc[mi][nb][4 * g], acc[mi][nb][4 * g + 1], acc[mi][nb][4 * g + 2], acc[mi][nb][4 * g + 3]}; }
    WAIT_L(0); BAR(); asm volatile("" ::: "memory");
    { const int row = tid >> 3, c8 = tid & 7; f32x4 s0 = {}, s1 = {};
#pragma unroll
      for (int w = 0; w < 8; ++w) { s0 += *(const LAS f32x4*)(lds + w * 16384 + row * 256 + 16 * ((2 * c8) ^ (row & 15)));
                                    s1 += *(const LAS f32x4*)(lds + w * 16384 + row * 256 + 16 * ((2 * c8 + 1) ^ (row & 15))); }
      u32x4 w = {cvtpk(s0[0], s0[1]), cvtpk(s0[2], s0[3]), cvtpk(s1[0], s1[1]), cvtpk(s1[2], s1[3])};
      *(u32x4*)(out + (size_t)(rt * 64 + row) * 512 + ct * 64 + c8 * 8) = w; }
}

extern "C" void kernel_launch(void* const* d_in, const int* in_sizes, int n_in, void* d_out, int out_size, void* d_ws, size_t ws_size,
                              hipStream_t stream) {
    (void)in_sizes; (void)n_in; (void)out_size; (void)ws_size;
    const u16* x = (const u16*)d_in[0]; const u16* ckv = (const u16*)d_in[1]; const u16* wq = (const u16*)d_in[2];
    const u16* wuk = (const u16*)d_in[3]; const u16* wuv = (const u16*)d_in[4]; const u16* wo_a = (const u16*)d_in[5];
    const u16* wqkv = (const u16*)d_in[7]; const u16* wphi = (const u16*)d_in[8]; const u16* wo_b = (const u16*)d_in[9]; const u16* gatew = (const u16*)d_in[10];
    char* ws = (char*)d_ws;
    (void)hipFuncSetAttribute((const void*)k1_proj, hipFuncAttributeMaxDynamicSharedMemorySize, LDS_BYTES);
    (void)hipFuncSetAttribute((const void*)k2_attn, hipFuncAttributeMaxDynamicSharedMemorySize, LDS_BYTES);
    (void)hipFuncSetAttribute((const void*)k3_out, hipFuncAttributeMaxDynamicSharedMemorySize, 131072);
    k1_proj<<<256, 512, LDS_BYTES, stream>>>(x, ckv, wuk, wuv, wqkv, wphi, ws + WS_KV, (float*)(ws + WS_KVP));
    k2_attn<<<256, 512, LDS_BYTES, stream>>>(x, wq, wqkv, wphi, gatew, ws + WS_KV, (const float*)(ws + WS_KVP), ws + WS_A3);
    k3_out<<<256, 512, 131072, stream>>>(ws + WS_A3, wo_a, wo_b, (u16*)d_out);
}
```

```cpp
#include <hip/hip_runtime.h>
#include <hip/hip_bf16.h>
#include <stdint.h>

typedef unsigned short u16;
typedef short bf16x8 __attribute__((ext_vector_type(8)));
typedef short s16x4 __attribute__((ext_vector_type(4)));
typedef float f32x2 __attribute__((ext_vector_type(2)));
typedef float f32x4 __attribute__((ext_vector_type(4)));
typedef float f32x16 __attribute__((ext_vector_type(16)));
typedef unsigned u32x4 __attribute__((ext_vector_type(4)));
typedef unsigned u32x2 __attribute__((ext_vector_type(2)));
typedef __bf16 bf16x2_t __attribute__((ext_vector_type(2)));
#define LAS __attribute__((address_space(3)))
#define DI __device__ __forceinline__
typedef LAS const char* lds_cptr;
typedef LAS char* lds_ptr;

constexpr int SEQ = 2048, DM = 512, NH = 32, HD = 128;
constexpr size_t WS_KV = 0;
constexpr size_t WS_A3 = WS_KV + (size_t)NH * 32 * 32768;
constexpr size_t WS_KVP = WS_A3 + (size_t)576 * SEQ * 16;
constexpr size_t WS_QB = WS_KVP + (size_t)NH * 8 * 4096;
constexpr size_t WS_END = WS_QB + (size_t)256 * 32768;
constexpr int LDS_BYTES = 163840;

#define MFMA32(a, b, c) __builtin_amdgcn_mfma_f32_32x32x16_bf16((a), (b), (c), 0, 0, 0)
#define MFMA16(a, b, c) __builtin_amdgcn_mfma_f32_16x16x32_bf16((a), (b), (c), 0, 0, 0)
#define SBAR() __builtin_amdgcn_sched_barrier(0)
#define WAIT_V(n) asm volatile("s_waitcnt vmcnt(" #n ")" ::: "memory")
#define WAIT_L(n) asm volatile("s_waitcnt lgkmcnt(" #n ")" ::: "memory")
#define BAR() __builtin_amdgcn_s_barrier()
#define WAIT_BAR(N) asm volatile("s_waitcnt vmcnt(" #N ") lgkmcnt(0)\n\ts_barrier" ::: "memory")

#define STAMP_DECL()
#define STAMP()
#define STAMP_RT()
#define STAMP_OUT(base)
#define STAMP_PARAM
#define STAMP_ARG
DI unsigned rfl(unsigned x) { return __builtin_amdgcn_readfirstlane(x); }
DI unsigned cvtpk(float lo, float hi) { f32x2 v = {lo, hi}; bf16x2_t b = __builtin_convertvector(v, bf16x2_t); return __builtin_bit_cast(unsigned, b); }
DI float bfround(float x) { return __uint_as_float(cvtpk(x, x) << 16); }
DI void glds16(const void* sbase, unsigned voff, unsigned lds_dst) {
    unsigned sv;
    asm volatile("s_mov_b32 %0, m0\n\ts_mov_b32 m0, %3\n\ts_nop 0\n\tglobal_load_lds_dwordx4 %1, %2\n\ts_mov_b32 m0, %0"
                 : "=&s"(sv) : "v"(voff), "s"(sbase), "s"(lds_dst) : "memory");
}
typedef short v4i16_t __attribute__((ext_vector_type(4)));
DI s16x4 trrd(lds_cptr p) { return __builtin_bit_cast(s16x4, __builtin_amdgcn_ds_read_tr16_b64_v4i16((LAS v4i16_t*)p)); }
DI bf16x8 cat8(s16x4 a, s16x4 b) { return __builtin_shufflevector(a, b, 0, 1, 2, 3, 4, 5, 6, 7); }
DI bf16x8 ldsr128(lds_cptr p) { return *(const LAS bf16x8*)p; }
DI bf16x8 pack8(float a0, float a1, float a2, float a3, float a4, float a5, float a6, float a7) {
    u32x4 w = {cvtpk(a0, a1), cvtpk(a2, a3), cvtpk(a4, a5), cvtpk(a6, a7)}; return __builtin_bit_cast(bf16x8, w); }
#define PACK8(X, B) pack8((X)[(B)], (X)[(B) + 1], (X)[(B) + 2], (X)[(B) + 3], (X)[(B) + 4], (X)[(B) + 5], (X)[(B) + 6], (X)[(B) + 7])
DI float swap32_max(float v) { auto rr = __builtin_amdgcn_permlane32_swap(__float_as_uint(v), __float_as_uint(v), false, false); return fmaxf(__uint_as_float(rr[0]), __uint_as_float(rr[1])); }
DI float swap32_add(float v) { auto rr = __builtin_amdgcn_permlane32_swap(__float_as_uint(v), __float_as_uint(v), false, false); return __uint_as_float(rr[0]) + __uint_as_float(rr[1]); }
DI float lo32_bcast(float v) { auto rr = __builtin_amdgcn_permlane32_swap(__float_as_uint(v), __float_as_uint(v), false, false); return __uint_as_float(rr[0]); }
template <int CTRL> DI float dpp(float v) { return __uint_as_float(__builtin_amdgcn_update_dpp(0u, __float_as_uint(v), CTRL, 0xF, 0xF, true)); }
DI float row16_max(float v) { v = fmaxf(v, dpp<0xB1>(v)); v = fmaxf(v, dpp<0x4E>(v)); v = fmaxf(v, dpp<0x141>(v)); v = fmaxf(v, dpp<0x140>(v)); return v; }
DI float row16_add(float v) { v += dpp<0xB1>(v); v += dpp<0x4E>(v); v += dpp<0x141>(v); v += dpp<0x140>(v); return v; }

__global__ __launch_bounds__(512, 2) void k1_proj(const u16* __restrict__ x, const u16* __restrict__ ckv, const u16* __restrict__ wuk,
                                                  const u16* __restrict__ wuv, const u16* __restrict__ wqkv, const u16* __restrict__ wphi,
                                                  char* __restrict__ KV, float* __restrict__ KVP) {
    extern __shared__ __attribute__((aligned(16))) char shm[];
    const lds_ptr lds = (lds_ptr)shm; const unsigned lds0 = (unsigned)(uintptr_t)shm;
    const int tid = threadIdx.x, wid = rfl(tid >> 6), lane = tid & 63, wr = wid >> 2, wc = wid & 3, fr = lane & 15, fq = lane >> 4;
    const int bid = blockIdx.x, hh_ = 4 * (bid & 7) + (bid >> 6), it = (bid >> 3) & 7;
    constexpr int HTB = 16384;
    STAMP_DECL(); STAMP_RT(); STAMP();
#define SA(b, h) (((b) * 2 + (h)) * HTB)
#define SB(b, h) ((4 + (b) * 2 + (h)) * HTB)
    unsigned voffA, voffB;
    { const int row = 8 * wid + (lane >> 3), ch = (lane & 7) ^ ((row >> 1) & 7); voffA = (unsigned)(row * 1024 + ch * 16); }
    { const int row = 4 * wid + (lane >> 4), j = lane & 15, s = ((row >> 3) & 1) * 4 + (row & 3), c = (j >> 1) ^ s; voffB = (unsigned)(row * 8192 + c * 32 + (j & 1) * 16); }
    const char* gA = (const char*)ckv + (size_t)it * 256 * 1024;
    const char* gB0 = (const char*)wuk + hh_ * 256; const char* gB1 = (const char*)wuv + hh_ * 256;
    const unsigned ldsw = rfl(lds0 + wid * 1024);
#define STAGE_A(b, h, kt) do { const char* g_ = gA + (h) * 131072 + (kt) * 128; glds16(g_, voffA, ldsw + SA(b, h)); glds16(g_ + 65536, voffA, ldsw + SA(b, h) + 8192); } while (0)
#define STAGE_B(b, h, kt) do { const char* g_ = ((h) ? gB1 : gB0) + (size_t)(kt) * 524288; glds16(g_, voffB, ldsw + SB(b, h)); glds16(g_ + 262144, voffB, ldsw + SB(b, h) + 8192); } while (0)
    lds_cptr pa[2], pb[2];
#pragma unroll
    for (int ks = 0; ks < 2; ++ks) pa[ks] = lds + (wr * 64 + fr) * 128 + 16 * ((4 * ks + fq) ^ ((fr >> 1) & 7));
    { const int q = fr >> 2, p = fr & 3, s = (fq & 1) * 4 + q;
#pragma unroll
      for (int n = 0; n < 2; ++n) pb[n] = lds + (8 * fq + q) * 256 + 32 * ((2 * wc + n) ^ s) + 8 * p; }
#define LDA(dst, b, h) do { _Pragma("unroll") for (int m = 0; m < 4; ++m) _Pragma("unroll") for (int k = 0; k < 2; ++k) dst[m][k] = ldsr128(pa[k] + SA(b, h) + m * 2048); } while (0)
#define LDB(dst, b, h) do { _Pragma("unroll") for (int n = 0; n < 2; ++n) _Pragma("unroll") for (int k = 0; k < 2; ++k) \
        dst[n][k] = cat8(trrd(pb[n] + SB(b, h) + k * 8192), trrd(pb[n] + SB(b, h) + k * 8192 + 1024)); } while (0)
#define MMA(ai, bj, At, Bt) do { __builtin_amdgcn_s_setprio(1); _Pragma("unroll") for (int m = 0; m < 4; ++m) _Pragma("unroll") for (int n = 0; n < 2; ++n) _Pragma("unroll") for (int k = 0; k < 2; ++k) \
        acc[ai][bj][m][n] = (bj) ? MFMA16(At[m][k], Bt[n][k], acc[ai][bj][m][n]) : MFMA16(Bt[n][k], At[m][k], acc[ai][bj][m][n]); __builtin_amdgcn_s_setprio(0); } while (0)
    f32x4 acc[2][2][4][2] = {};
    bf16x8 At[4][2], B0[2][2], B1[2][2];
    constexpr int nt = 8;
    STAGE_B(0, 0, 0); STAGE_A(0, 0, 0); STAGE_B(0, 1, 0); STAGE_A(0, 1, 0);
    if (wr == 1) BAR();
    WAIT_V(4); BAR();
    STAGE_B(1, 0, 1); STAGE_A(1, 0, 1); STAGE_B(1, 1, 1);
    WAIT_V(6); BAR();
    STAMP();
    for (int t = 0; t < nt - 2; t += 2) {
        LDB(B0, 0, 0); SBAR(); LDA(At, 0, 0); STAGE_A(1, 1, t + 1);
        WAIT_L(8); BAR(); WAIT_L(0); MMA(0, 0, At, B0); BAR(); SBAR();
        LDB(B1, 0, 1); STAGE_B(0, 0, t + 2);
        BAR(); WAIT_L(0); MMA(0, 1, At, B1); BAR();
        LDA(At, 0, 1); STAGE_A(0, 0, t + 2);
        BAR(); WAIT_L(0); MMA(1, 0, At, B0); BAR(); SBAR();
        STAGE_B(0, 1, t + 2);
        WAIT_V(6); BAR(); MMA(1, 1, At, B1); BAR();
        LDB(B0, 1, 0); SBAR(); LDA(At, 1, 0); STAGE_A(0, 1, t + 2);
        WAIT_L(8); BAR(); WAIT_L(0); MMA(0, 0, At, B0); BAR(); SBAR();
        LDB(B1, 1, 1); STAGE_B(1, 0, t + 3);
        BAR(); WAIT_L(0); MMA(0, 1, At, B1); BAR();
        LDA(At, 1, 1); STAGE_A(1, 0, t + 3);
        BAR(); WAIT_L(0); MMA(1, 0, At, B0); BAR(); SBAR();
        STAGE_B(1, 1, t + 3);
        WAIT_V(6); BAR(); MMA(1, 1, At, B1); BAR();
    }
    { LDB(B0, 0, 0); WAIT_V(0); LDA(At, 0, 0); STAGE_A(1, 1, nt - 1);
      {
        const int row = 64 * wid + (lane >> 2), sl = lane & 3;
        const unsigned vo = (unsigned)(row * 1536 + 512 + 512 * (sl >> 1) + 16 * hh_ + 8 * (sl & 1)) * 2u;
#pragma unroll
        for (int e = 0; e < 4; ++e) glds16((const char*)wqkv + (size_t)e * 16 * 3072, vo, rfl(lds0 + 131072 + wid * 4096 + e * 1024));
      }
      BAR(); WAIT_L(0); MMA(0, 0, At, B0); BAR();
      LDB(B1, 0, 1); BAR(); WAIT_L(0); MMA(0, 1, At, B1); BAR();
      LDA(At, 0, 1); BAR(); WAIT_L(0); MMA(1, 0, At, B0); MMA(1, 1, At, B1); BAR(); }
    { LDB(B0, 1, 0); LDA(At, 1, 0); BAR(); WAIT_L(0); MMA(0, 0, At, B0); BAR();
      LDB(B1, 1, 1); WAIT_V(0); BAR(); WAIT_L(0); MMA(0, 1, At, B1); BAR();
      LDA(At, 1, 1); BAR(); WAIT_L(0); MMA(1, 0, At, B0); MMA(1, 1, At, B1); BAR(); }
    if (wr == 0) BAR();
    STAMP();
    u32x4 pk[2][4], pv[2][2][2];
#pragma unroll
    for (int ai = 0; ai < 2; ++ai) {
#pragma unroll
        for (int m = 0; m < 4; ++m) { const f32x4 a = acc[ai][0][m][0], b = acc[ai][0][m][1];
            pk[ai][m] = (u32x4){cvtpk(a[0], a[1]), cvtpk(a[2], a[3]), cvtpk(b[0], b[1]), cvtpk(b[2], b[3])}; }
#pragma unroll
        for (int u = 0; u < 2; ++u)
#pragma unroll
            for (int n = 0; n < 2; ++n) { const f32x4 a = acc[ai][1][2 * u][n], b = acc[ai][1][2 * u + 1][n];
                pv[ai][u][n] = (u32x4){cvtpk(a[0], a[1]), cvtpk(a[2], a[3]), cvtpk(b[0], b[1]), cvtpk(b[2], b[3])}; }
    }
    SBAR();
    const int r32 = lane & 31, hi = lane >> 5, blk = (lane >> 4) & 1;
    bf16x8 xf[32]; u16 wph[8];
    { const u16* xr = x + (size_t)(256 * it + 32 * wid + r32) * 512 + 32 * hi;
#pragma unroll
      for (int e = 0; e < 32; ++e) xf[e] = *(const bf16x8*)(xr + 64 * (e >> 2) + 8 * (e & 3));
#pragma unroll
      for (int j = 0; j < 8; ++j) wph[j] = wphi[hh_ * 256 + (8 * (j >> 2) + 4 * hi + (j & 3)) * 16 + fr]; }
    asm volatile("" ::: "memory"); SBAR();
#pragma unroll
    for (int ai = 0; ai < 2; ++ai) {
        char* tb = KV + (size_t)(hh_ * 32 + 4 * it + 2 * ai + wr) * 32768;
#pragma unroll
        for (int m = 0; m < 4; ++m) *(u32x4*)(tb + (4 * wc + fq) * 1024 + (m * 16 + fr) * 16) = pk[ai][m];
#pragma unroll
        for (int u = 0; u < 2; ++u)
#pragma unroll
            for (int n = 0; n < 2; ++n) *(u32x4*)(tb + 16384 + (4 * u + fq) * 2048 + (wc * 32 + n * 16 + fr) * 16) = pv[ai][u][n];
    }
    asm volatile("" ::: "memory"); SBAR();
#undef SA
#undef SB
#undef STAGE_A
#undef STAGE_B
#undef LDA
#undef LDB
#undef MMA
    STAMP();
    {
        const int q = fr >> 2, p = fr & 3;
        const lds_cptr wp = lds + 131072 + (32 * hi + q) * 64 + 32 * blk + 8 * p;
        f32x16 d1 = {}, d2 = {};
        bf16x8 wf[4];
#define WRD(e) cat8(trrd(wp + ((e) >> 2) * 4096 + ((e) & 3) * 512), trrd(wp + ((e) >> 2) * 4096 + ((e) & 3) * 512 + 256))
        wf[0] = WRD(0); wf[1] = WRD(1); wf[2] = WRD(2);
#pragma unroll
        for (int e = 0; e < 32; ++e) {
            if (e + 3 < 32) wf[(e + 3) & 3] = WRD(e + 3);
            SBAR();
            d1 = MFMA32(xf[e], wf[e & 3], d1); d2 = MFMA32(wf[e & 3], xf[e], d2);
            SBAR();
        }
#undef WRD
        STAMP();
        bf16x8 wpf;
#pragma unroll
        for (int j = 0; j < 8; ++j) wpf[j] = (short)(blk ? (wph[j] ^ 0x8000) : wph[j]);
        const bf16x8 kp = PACK8(d2, 0);
        f32x16 d3 = MFMA32(kp, wpf, f32x16{});
#pragma unroll
        for (int r = 0; r < 16; ++r) {
            const float v = bfround(d3[r]); const float mx = row16_max(v);
            const float e = __builtin_amdgcn_exp2f((v - mx) * 1.4426950408889634f); const float s = row16_add(e);
            d3[r] = e * __builtin_amdgcn_rcpf(s);
        }
        const bf16x8 ones = {0x3F80, 0x3F80, 0x3F80, 0x3F80, 0x3F80, 0x3F80, 0x3F80, 0x3F80};
        const bf16x8 v0 = PACK8(d1, 0), v1 = PACK8(d1, 8);
        f32x16 d4 = MFMA32(PACK8(d3, 0), blk ? v0 : ones, f32x16{});
        d4 = MFMA32(PACK8(d3, 8), blk ? v1 : ones, d4);
        LAS float* red = (LAS float*)(lds + wid * 4096);
#pragma unroll
        for (int r = 0; r < 16; ++r) red[((r & 3) + 8 * (r >> 2) + 4 * hi) * 32 + r32] = d4[r];
        WAIT_L(0); BAR(); asm volatile("" ::: "memory");
        f32x2 s = {0.f, 0.f};
#pragma unroll
        for (int w = 0; w < 8; ++w) s += *(const LAS f32x2*)(lds + w * 4096 + tid * 8);
        *(f32x2*)(KVP + (size_t)(hh_ * 8 + it) * 1024 + tid * 2) = s;
    }
    STAMP(); WAIT_V(0); STAMP(); STAMP_RT();
    STAMP_OUT(KV + WS_END);
}

constexpr float QSCALE = 0.08838834764831845f * 1.4426950408889634f;
constexpr float THR = 8.f;
constexpr int L_V = 98304, L_GATE = 102400, L_KVS = 103424;
#define PIN(x) asm volatile("" : "+v"(x))
#define MX3(a, b, c) __builtin_fmaxf(__builtin_fmaxf((a), (b)), (c))
#define EX2(v) __builtin_amdgcn_exp2f(v)
DI void mask_tile(f32x16& s0, f32x16& s1, int dq) {
    asm volatile("" : "+v"(dq));
#pragma unroll
    for (int r = 0; r < 16; ++r) { const int kk = 16 * ((r >> 2) & 1) + 8 * (r >> 3) + (r & 3);
        if (kk > dq) s0[r] = -INFINITY; if (kk + 32 > dq) s1[r] = -INFINITY; }
}

__global__ __launch_bounds__(512, 2) void k2_attn(const u16* __restrict__ x, const u16* __restrict__ wq, const u16* __restrict__ wqkv,
                                                  const u16* __restrict__ wphi, const u16* __restrict__ gatew, const char* __restrict__ KV,
                                                  const float* __restrict__ KVP, char* __restrict__ A3, char* __restrict__ QB) {
    extern __shared__ __attribute__((aligned(16))) char shm[];
    const lds_ptr lds = (lds_ptr)shm; const unsigned lds0 = (unsigned)(uintptr_t)shm;
    const int tid = threadIdx.x, wid = rfl(tid >> 6), lane = tid & 63, G = wid >> 2, w4 = wid & 3;
    const int r32 = lane & 31, hi = lane >> 5, blk = (lane >> 4) & 1, q4 = (lane & 15) >> 2, p4 = lane & 3;
    const int bid = blockIdx.x, hh_ = 4 * (bid & 7) + (bid >> 6), jj = (bid >> 3) & 7;
    const int bA = 15 - jj, bB = jj;
    const int myrow0 = (G ? bB : bA) * 128 + 32 * w4;
    const char* kvw = KV + (size_t)hh_ * 32 * 32768 + w4 * 4096;
    const unsigned vlin = (unsigned)lane * 16u;
    const unsigned ldw = lds0 + w4 * 4096;
    char* qbp = QB + (size_t)(bid * 4 + w4) * 8192 + lane * 16;
    bf16x8 qf[8]; float gate;
    STAMP_DECL(); STAMP_RT(); STAMP();
    {
        f32x2 ps[8]; u16 wph[8];
#pragma unroll
        for (int w = 0; w < 8; ++w) ps[w] = *(const f32x2*)(KVP + (size_t)(hh_ * 8 + w) * 1024 + tid * 2);
#pragma unroll
        for (int j = 0; j < 8; ++j) wph[j] = wphi[hh_ * 256 + (8 * (j >> 2) + 4 * hi + (j & 3)) * 16 + (lane & 15)];
        if (wid == 0) glds16(gatew, vlin, rfl(lds0 + L_GATE));
        unsigned voffX, voffW, voffE;
        { const int row = lane >> 3, ch = (lane & 7) ^ ((row >> 1) & 3); voffX = (unsigned)(row * 1024 + ch * 16); }
        { const int row = 4 * wid + (lane >> 4), j = lane & 15, c = (j >> 2) ^ (row & 3); voffW = (unsigned)(row * 8192 + c * 64 + (j & 3) * 16); }
        { const int row = 8 * wid + (lane >> 1); voffE = (unsigned)(row * 3072 + (lane & 1) * 16); }
        const char* gX = (const char*)x + (size_t)myrow0 * 1024; const char* gW = (const char*)wq + hh_ * 256; const char* gE = (const char*)wqkv + hh_ * 32;
        auto issue = [&](int c, unsigned slot) {
            const unsigned sb = lds0 + slot;
#pragma unroll
            for (int e = 0; e < 4; ++e) glds16(gX + e * 8192 + c * 128, voffX ^ ((e & 1) << 6), rfl(sb + wid * 4096 + e * 1024));
            glds16(gW + (size_t)c * 524288, voffW, rfl(sb + 32768 + wid * 1024));
            glds16(gW + (size_t)c * 524288 + 262144, voffW, rfl(sb + 32768 + 8192 + wid * 1024));
            if (lane < 16) glds16(gE + (size_t)c * 196608, voffE, rfl(sb + 49152 + wid * 256));
        };
        issue(0, 51200u);
        { f32x2 s = ps[0];
#pragma unroll
          for (int w = 1; w < 8; ++w) s += ps[w];
          *(LAS f32x2*)(lds + L_KVS + tid * 8) = s; }
        bf16x8 wpf;
#pragma unroll
        for (int j = 0; j < 8; ++j) wpf[j] = (short)(blk ? (wph[j] ^ 0x8000) : wph[j]);
        PIN(wpf);
        issue(1, 112640u);
        f32x16 qa[5] = {};
        unsigned xo[2], wo_[4];
#pragma unroll
        for (int s = 0; s < 2; ++s) xo[s] = (unsigned)((32 * wid + r32) * 128 + 16 * ((2 * s + hi) ^ ((r32 >> 1) & 7)));
#pragma unroll
        for (int c = 0; c < 4; ++c) wo_[c] = (unsigned)(32768 + (8 * hi + q4) * 256 + 64 * (c ^ q4) + 32 * (p4 >> 1) + 16 * blk + 8 * (p4 & 1));
        const unsigned eo = (unsigned)(49152 + (8 * hi + q4) * 32 + 8 * p4);
        bf16x8 fx[2], fw[2][5], fg[2];
#define QRD(buf, sb, c, ks) do { fx[buf] = ldsr128((sb) + (xo[(ks) & 1] ^ (((ks) >> 1) << 6))); \
        _Pragma("unroll") for (int cc = 0; cc < 4; ++cc) fw[buf][cc] = cat8(trrd((sb) + wo_[cc] + (ks) * 4096), trrd((sb) + wo_[cc] + (ks) * 4096 + 1024)); \
        fw[buf][4] = cat8(trrd((sb) + eo + (ks) * 512), trrd((sb) + eo + (ks) * 512 + 128)); \
        fg[buf] = ldsr128(lds + L_GATE + ((c) * 64 + (ks) * 16 + 8 * hi) * 2); } while (0)
#define QMM(buf) do { _Pragma("unroll") for (int cc = 0; cc < 4; ++cc) qa[cc] = MFMA32(fw[buf][cc], fx[buf], qa[cc]); \
        qa[4] = MFMA32(r32 == 16 ? fg[buf] : fw[buf][4], fx[buf], qa[4]); } while (0)
        unsigned s0_ = 51200u, s1_ = 112640u, s2_ = 0u;
        WAIT_BAR(7);
        STAMP();
        QRD(0, lds + s0_, 0, 0);
        for (int c = 0; c < 8; ++c) {
            const lds_cptr sb = lds + s0_; const lds_cptr sn = lds + s1_;
            SBAR(); QRD(1, sb, c, 1); SBAR(); QMM(0); SBAR();
            QRD(0, sb, c, 2); SBAR(); QMM(1); SBAR();
            if (c < 7) { asm volatile("s_waitcnt vmcnt(0)\n\ts_barrier" ::: "memory"); } else { BAR(); asm volatile("" ::: "memory"); }
            if (c < 6) issue(c + 2, s2_);
            else if (c - 6 == G) {
#pragma unroll
                for (int e = 0; e < 8; ++e) glds16(kvw + (size_t)(G + 2 * (e >> 2)) * 32768 + (e & 3) * 1024, vlin, ldw + G * 49152 + (e >> 2) * 16384 + (e & 3) * 1024);
            }
            SBAR(); QRD(1, sb, c, 3); SBAR(); QMM(0); SBAR();
            if (c < 7) QRD(0, sn, c + 1, 0);
            SBAR(); QMM(1); SBAR();
            const unsigned t_ = s0_; s0_ = s1_; s1_ = s2_; s2_ = t_;
        }
#undef QRD
#undef QMM
        STAMP();
#pragma unroll
        for (int cc = 0; cc < 4; ++cc) {
#pragma unroll
            for (int r = 0; r < 16; ++r) qa[cc][r] *= QSCALE;
            qf[2 * cc] = PACK8(qa[cc], 0); qf[2 * cc + 1] = PACK8(qa[cc], 8);
        }
        if (G) {
#pragma unroll
            for (int s = 0; s < 8; ++s) *(bf16x8*)(qbp + s * 1024) = qf[s];
        }
        { const float gz = bfround(lo32_bcast(qa[4][8])); gate = bfround(1.f / (1.f + __expf(-gz))); }
        float kvv[2][8];
        { const int col = r32 < 16 ? 16 + r32 : 0;
#pragma unroll
          for (int s = 0; s < 2; ++s)
#pragma unroll
              for (int j = 0; j < 8; ++j) { const int f = 16 * s + 8 * (j >> 2) + 4 * hi + (j & 3); const float v = *(const LAS float*)(lds + L_KVS + (f * 32 + col) * 4); kvv[s][j] = r32 <= 16 ? v : 0.f; } }
        f32x16 pq = MFMA32(wpf, PACK8(qa[4], 0), f32x16{});
#pragma unroll
        for (int hf = 0; hf < 2; ++hf) {
            float mx = -INFINITY;
#pragma unroll
            for (int r = 0; r < 8; ++r) { pq[8 * hf + r] = bfround(pq[8 * hf + r]); mx = fmaxf(mx, pq[8 * hf + r]); }
            mx = swap32_max(mx); float s = 0.f;
#pragma unroll
            for (int r = 0; r < 8; ++r) { pq[8 * hf + r] = EX2((pq[8 * hf + r] - mx) * 1.4426950408889634f); s += pq[8 * hf + r]; }
            s = __builtin_amdgcn_rcpf(swap32_add(s));
#pragma unroll
            for (int r = 0; r < 8; ++r) pq[8 * hf + r] *= s;
        }
        f32x16 nm = MFMA32(PACK8(kvv[0], 0), PACK8(pq, 0), f32x16{});
        nm = MFMA32(PACK8(kvv[1], 0), PACK8(pq, 8), nm);
        { const float den = bfround(bfround(lo32_bcast(nm[8])) + 1e-6f); const float sc = (1.f - gate) / den;
          float ov[8];
#pragma unroll
          for (int r = 0; r < 8; ++r) ov[r] = bfround(nm[r]) * sc;
          *(bf16x8*)(A3 + ((size_t)(512 + 2 * hh_ + hi) * SEQ + myrow0 + r32) * 16) = PACK8(ov, 0); }
        WAIT_L(0); BAR(); asm volatile("" ::: "memory");
        const lds_ptr qp = lds + L_V + 16384 + (w4 >> 1) * 32768 + (w4 & 1) * 8192 + lane * 16;
        if (G == 0) {
#pragma unroll
            for (int s = 0; s < 8; ++s) *(LAS bf16x8*)(qp + s * 1024) = qf[s];
        }
        WAIT_BAR(0);
        if (G) {
#pragma unroll
            for (int s = 0; s < 8; ++s) qf[s] = ldsr128(qp + s * 1024);
        }
    }
    STAMP();
    const int prow = (r32 & 7) | ((r32 & 8) << 1) | ((r32 & 16) >> 1);
    const unsigned kro = (unsigned)(prow * 16 + hi * 1024), vro = (unsigned)(L_V + G * 32768 + r32 * 16 + hi * 2048);
    const int dq = 32 * w4 + r32 - 64 * G - 4 * hi;
    unsigned ks0 = G * 49152u, ks1 = ks0 + 16384u, ks2 = ks0 + 32768u;
    f32x16 o[4], negm, sA0, sA1, sB0, sB1; bf16x8 kf[16], vf[16]; u32x4 pw[4]; float m, l;
#define KRD(i, kp) kf[i] = ldsr128((kp) + ((i) >> 1) * 2048 + ((i) & 1) * 512)
#define VRD(i, vp) vf[i] = ldsr128((vp) + ((i) >> 2) * 4096 + ((i) & 3) * 512)
#define PF(k) __builtin_bit_cast(bf16x8, pw[k])
#define DMA1(i) do { if ((i) < 4) glds16(gk_ + (i) * 1024, vlin, dk_ + (i) * 1024); else glds16(gv_ + ((i) - 4) * 1024, vlin, dv_ + ((i) - 4) * 1024); } while (0)
#define ROT() do { const unsigned t_ = ks0; ks0 = ks1; ks1 = ks2; ks2 = t_; } while (0)
#define DMA_SETUP(r) const int tk_ = (r) + 2 <= R ? (r) + 2 : (pass == 0 ? (r) + 1 - R : 0); \
        const char* gk_ = kvw + (size_t)(2 * tk_ + G) * 32768; const char* gv_ = kvw + (size_t)(2 * (r) + G) * 32768 + 16384; \
        const unsigned dk_ = ldw + ks2, dv_ = ldw + L_V + G * 32768 + ((r) & 1) * 16384;
#define GAPA(i, C0, C1, P0, P1) do { \
        if ((i) + 4 < 16) { KRD((i) + 4, kp_); } else { VRD((i) - 12, vp_); } \
        if ((i) < 8) DMA1(i); \
        if ((i) & 1) C1 = MFMA32(kf[i], qf[(i) >> 1], (i) < 2 ? negm : C1); else C0 = MFMA32(kf[i], qf[(i) >> 1], (i) < 2 ? negm : C0); \
        if ((i) < 8) { P1[2 * (i)] = EX2(P1[2 * (i)]); P1[2 * (i) + 1] = EX2(P1[2 * (i) + 1]); pw[(i) >> 2][(i) & 3] = cvtpk(P0[2 * (i)], P0[2 * (i) + 1]); PIN(P1); PIN(pw[(i) >> 2]); } \
        else { l += P1[2 * (i) - 16]; l2 += P1[2 * (i) - 15]; pw[(i) >> 2][(i) & 3] = cvtpk(P1[2 * (i) - 16], P1[2 * (i) - 15]); PIN(l); PIN(l2); PIN(pw[(i) >> 2]); } \
        SBAR(); } while (0)
#define PVM(i) o[(i) & 3] = MFMA32(vf[i], PF((i) >> 2), o[(i) & 3])
#define RDB(i) do { if ((i) + 4 < 16) { VRD((i) + 4, vp_); } else { KRD((i) - 12, kn_); } } while (0)
#define ROUND(C0, C1, P0, P1, r) do { \
        const lds_cptr kp_ = lds + ks0 + kro; const lds_cptr kn_ = lds + ks1 + kro; const lds_cptr vp_ = lds + vro + (((r) - 1) & 1) * 16384; \
        DMA_SETUP(r) float l2 = 0.f; SBAR(); \
        GAPA(0, C0, C1, P0, P1); GAPA(1, C0, C1, P0, P1); GAPA(2, C0, C1, P0, P1); GAPA(3, C0, C1, P0, P1); \
        GAPA(4, C0, C1, P0, P1); GAPA(5, C0, C1, P0, P1); GAPA(6, C0, C1, P0, P1); GAPA(7, C0, C1, P0, P1); \
        GAPA(8, C0, C1, P0, P1); GAPA(9, C0, C1, P0, P1); GAPA(10, C0, C1, P0, P1); GAPA(11, C0, C1, P0, P1); \
        GAPA(12, C0, C1, P0, P1); GAPA(13, C0, C1, P0, P1); GAPA(14, C0, C1, P0, P1); GAPA(15, C0, C1, P0, P1); \
        l += l2; \
        RDB(0); PVM(0); SBAR(); RDB(1); PVM(1); SBAR(); \
        if ((r) == R) mask_tile(C0, C1, dq); \
        float ma, mb; \
        RDB(2); PVM(2); ma = MX3(C0[0], C0[1], C1[0]); mb = MX3(C0[2], C0[3], C1[1]); ma = MX3(ma, C1[2], C1[3]); mb = MX3(mb, C0[4], C0[5]); PIN(ma); PIN(mb); SBAR(); \
        RDB(3); PVM(3); ma = MX3(ma, C0[6], C0[7]); mb = MX3(mb, C1[4], C1[5]); ma = MX3(ma, C1[6], C1[7]); mb = MX3(mb, C0[8], C0[9]); PIN(ma); PIN(mb); SBAR(); \
        RDB(4); PVM(4); ma = MX3(ma, C0[10], C0[11]); mb = MX3(mb, C1[8], C1[9]); ma = MX3(ma, C1[10], C1[11]); mb = MX3(mb, C0[12], C0[13]); PIN(ma); PIN(mb); SBAR(); \
        RDB(5); PVM(5); ma = MX3(ma, C0[14], C0[15]); mb = MX3(mb, C1[12], C1[13]); ma = MX3(ma, C1[14], C1[15]); \
        const float rm = swap32_max(fmaxf(ma, mb)); bool pend = false; float fsc = 1.f; \
        if (__builtin_expect(__any(rm > THR), 0)) { const float dl = fmaxf(rm, 0.f); m += dl; fsc = EX2(-dl); l *= fsc; pend = true; \
            _Pragma("unroll") for (int e = 0; e < 16; ++e) { C0[e] -= dl; C1[e] -= dl; negm[e] = -m; } } \
        SBAR(); \
        RDB(6); PVM(6); C0[0] = EX2(C0[0]); C0[1] = EX2(C0[1]); PIN(C0); SBAR(); \
        RDB(7); PVM(7); C0[2] = EX2(C0[2]); C0[3] = EX2(C0[3]); l += C0[0]; l2 = C0[1]; PIN(C0); PIN(l); SBAR(); \
        RDB(8); PVM(8); C0[4] = EX2(C0[4]); C0[5] = EX2(C0[5]); l += C0[2]; l2 += C0[3]; PIN(C0); PIN(l); PIN(l2); SBAR(); \
        RDB(9); PVM(9); C0[6] = EX2(C0[6]); C0[7] = EX2(C0[7]); l += C0[4]; l2 += C0[5]; PIN(C0); PIN(l); PIN(l2); SBAR(); \
        RDB(10); PVM(10); C0[8] = EX2(C0[8]); C0[9] = EX2(C0[9]); l += C0[6]; l2 += C0[7]; PIN(C0); PIN(l); PIN(l2); SBAR(); \
        RDB(11); PVM(11); C0[10] = EX2(C0[10]); C0[11] = EX2(C0[11]); l += C0[8]; l2 += C0[9]; PIN(C0); PIN(l); PIN(l2); SBAR(); \
        RDB(12); PVM(12); C0[12] = EX2(C0[12]); C0[13] = EX2(C0[13]); l += C0[10]; l2 += C0[11]; PIN(C0); PIN(l); PIN(l2); SBAR(); \
        RDB(13); PVM(13); C0[14] = EX2(C0[14]); C0[15] = EX2(C0[15]); l += C0[12]; l2 += C0[13]; PIN(C0); PIN(l); PIN(l2); SBAR(); \
        RDB(14); PVM(14); l += C0[14]; l2 += C0[15]; PIN(l); PIN(l2); SBAR(); \
        RDB(15); PVM(15); l += l2; PIN(l); SBAR(); \
        if (__builtin_expect(pend, 0)) { _Pragma("unroll") for (int c = 0; c < 4; ++c) _Pragma("unroll") for (int e = 0; e < 16; ++e) o[c][e] *= fsc; } \
        asm volatile("s_waitcnt vmcnt(0)\n\ts_barrier" ::: "memory"); ROT(); } while (0)

    for (int pass = 0; pass < 2; ++pass) {
        const int R = pass ? bB : bA;
        {
            const lds_cptr kp_ = lds + ks0 + kro; const lds_cptr kn_ = lds + ks1 + kro;
            DMA_SETUP(0)
#pragma unroll
            for (int i = 0; i < 6; ++i) KRD(i, kp_);
#pragma unroll
            for (int i = 0; i < 8; ++i) DMA1(i);
#pragma unroll
            for (int c = 0; c < 4; ++c) o[c] = f32x16{};
            SBAR();
#pragma unroll
            for (int i = 0; i < 16; ++i) {
                if (i + 6 < 16) KRD(i + 6, kp_);
                if (i & 1) sA1 = MFMA32(kf[i], qf[i >> 1], i < 2 ? f32x16{} : sA1); else sA0 = MFMA32(kf[i], qf[i >> 1], i < 2 ? f32x16{} : sA0);
                SBAR();
            }
            if (R == 0) mask_tile(sA0, sA1, dq);
            float ma = MX3(sA0[0], sA0[1], sA1[0]), mb = MX3(sA0[2], sA0[3], sA1[1]); ma = MX3(ma, sA1[2], sA1[3]);
#pragma unroll
            for (int r = 4; r < 16; r += 4) { ma = MX3(ma, sA0[r], sA0[r + 1]); mb = MX3(mb, sA0[r + 2], sA0[r + 3]); ma = MX3(ma, sA1[r], sA1[r + 1]); mb = MX3(mb, sA1[r + 2], sA1[r + 3]); }
            m = fmaxf(swap32_max(fmaxf(ma, mb)), -1e30f); l = 0.f;
#pragma unroll
            for (int i = 0; i < 4; ++i) KRD(i, kn_);
#pragma unroll
            for (int e = 0; e < 16; ++e) { sA0[e] = EX2(sA0[e] - m); l += sA0[e]; sA1[e] -= m; negm[e] = -m; }
            asm volatile("s_waitcnt vmcnt(0)\n\ts_barrier" ::: "memory"); ROT();
        }
        STAMP();
        int r = 1;
        for (; r + 1 <= R; r += 2) { ROUND(sB0, sB1, sA0, sA1, r); ROUND(sA0, sA1, sB0, sB1, r + 1); }
        if (r <= R) { ROUND(sB0, sB1, sA0, sA1, r); sA0 = sB0; sA1 = sB1; }
        STAMP();
        {
            const lds_cptr vp_ = lds + vro + (R & 1) * 16384;
#pragma unroll
            for (int i = 0; i < 8; ++i) VRD(i, vp_);
            if (pass == 0) {
#pragma unroll
                for (int s = 0; s < 8; ++s) qf[s] = *(const bf16x8*)(qbp + s * 1024);
            }
#pragma unroll
            for (int e = 0; e < 16; ++e) { sA1[e] = EX2(sA1[e]); l += sA1[e]; }
#pragma unroll
            for (int i = 0; i < 8; ++i) { pw[i >> 2][i & 3] = cvtpk(sA0[2 * i], sA0[2 * i + 1]); pw[2 + (i >> 2)][i & 3] = cvtpk(sA1[2 * i], sA1[2 * i + 1]); }
            SBAR();
#pragma unroll
            for (int i = 0; i < 16; ++i) { if (i + 8 < 16) VRD(i + 8, vp_); PVM(i); SBAR(); }
        }
        l = swap32_add(l);
        BAR(); asm volatile("" ::: "memory");
        const unsigned st0 = G ? ks2 - 49152u : ks2;
        { const lds_ptr xp = lds + L_V + wid * 8192 + lane * 16;
#pragma unroll
          for (int c = 0; c < 2; ++c)
#pragma unroll
              for (int g = 0; g < 4; ++g) { f32x4 t;
#pragma unroll
                  for (int e = 0; e < 4; ++e) t[e] = G ? o[c][4 * g + e] : o[2 + c][4 * g + e];
                  *(LAS f32x4*)(xp + (c * 4 + g) * 1024) = t; }
          *(LAS f32x4*)(lds + st0 + wid * 1024 + lane * 16) = (f32x4){m, l, gate, 0.f}; }
        WAIT_L(0); BAR(); asm volatile("" ::: "memory");
        { const int ow = wid ^ 4;
          const f32x4 st = *(const LAS f32x4*)(lds + st0 + ow * 1024 + lane * 16);
          const float mn = fmaxf(m, st[0]), f0 = EX2(m - mn), f1 = EX2(st[0] - mn);
          const float gg = (G == pass) ? gate : st[2];
          const float inv = gg / (l * f0 + st[1] * f1); const float c0 = f0 * inv, c1 = f1 * inv;
          const lds_cptr xp = lds + L_V + ow * 8192 + lane * 16;
          char* dst = A3 + ((size_t)(hh_ * 16 + G * 8 + hi) * SEQ + R * 128 + 32 * w4 + r32) * 16;
#pragma unroll
          for (int c = 0; c < 2; ++c) {
              float v[16];
#pragma unroll
              for (int g = 0; g < 4; ++g) { const f32x4 t = *(const LAS f32x4*)(xp + (c * 4 + g) * 1024);
#pragma unroll
                  for (int e = 0; e < 4; ++e) v[4 * g + e] = (G ? o[2 + c][4 * g + e] : o[c][4 * g + e]) * c0 + t[e] * c1; }
              *(bf16x8*)(dst + (size_t)(4 * c) * SEQ * 16) = PACK8(v, 0);
              *(bf16x8*)(dst + (size_t)(4 * c + 2) * SEQ * 16) = PACK8(v, 8);
          } }
        WAIT_L(0); BAR(); asm volatile("" ::: "memory");
        STAMP();
    }
    WAIT_V(0); STAMP(); STAMP_RT();
    STAMP_OUT(KV + WS_END + 524288);
#undef KRD
#undef VRD
#undef PF
#undef DMA1
#undef ROT
#undef DMA_SETUP
#undef GAPA
#undef PVM
#undef RDB
#undef ROUND
}

__global__ __launch_bounds__(512, 2) void k3_out(const char* __restrict__ A3, const u16* __restrict__ wo_a, const u16* __restrict__ wo_b, u16* __restrict__ out) {
    extern __shared__ __attribute__((aligned(16))) char shm[];
    const lds_ptr lds = (lds_ptr)shm; const unsigned lds0 = (unsigned)(uintptr_t)shm;
    const int tid = threadIdx.x, wid = rfl(tid >> 6), lane = tid & 63;
    const int r32 = lane & 31, hi = lane >> 5, blk = (lane >> 4) & 1, q4 = (lane & 15) >> 2, p4 = lane & 3;
    const int bid = blockIdx.x, rt = 4 * (bid & 7) + (bid >> 6), ct = (bid >> 3) & 7;
    const unsigned vlin = (unsigned)lane * 16u;
    unsigned voffW; { const int row = lane >> 3, sl = lane & 7, nb = (sl >> 2) ^ ((row >> 1) & 1); voffW = (unsigned)(row * 1024 + nb * 64 + (sl & 3) * 16); }
    const char* gA = A3 + (size_t)rt * 1024;
    const unsigned wb = lds0 + wid * 16384;
    auto issue = [&](int i) {
        const int kap = wid + 8 * i; const unsigned sb = wb + (i & 1) * 8192;
        const char* gw = (kap < 128 ? (const char*)wo_a + (size_t)kap * 32768 : (const char*)wo_b + (size_t)(kap - 128) * 32768) + ct * 128;
#pragma unroll
        for (int e = 0; e < 4; ++e) glds16(gA + (size_t)(4 * kap + e) * 32768, vlin, rfl(sb + e * 1024));
#pragma unroll
        for (int e = 0; e < 4; ++e) glds16(gw + e * 8192, voffW, rfl(sb + 4096 + e * 1024));
    };
    f32x16 acc[2][2] = {};
    const unsigned ao = (unsigned)(wid * 16384 + hi * 1024 + r32 * 16);
    unsigned bo[2];
#pragma unroll
    for (int nb = 0; nb < 2; ++nb) bo[nb] = (unsigned)(wid * 16384 + 4096 + (4 * hi + q4) * 128 + 64 * (nb ^ ((q4 >> 1) & 1)) + 32 * blk + 8 * p4);
    issue(0);
    for (int i = 0; i < 18; ++i) {
        if (i + 1 < 18) { issue(i + 1); WAIT_V(8); } else { WAIT_V(0); }
        const lds_cptr sb = lds + (i & 1) * 8192;
#pragma unroll
        for (int s = 0; s < 2; ++s) {
            bf16x8 af[2], bf[2];
#pragma unroll
            for (int mi = 0; mi < 2; ++mi) af[mi] = ldsr128(sb + ao + s * 2048 + mi * 512);
#pragma unroll
            for (int nb = 0; nb < 2; ++nb) bf[nb] = cat8(trrd(sb + bo[nb] + s * 2048), trrd(sb + bo[nb] + s * 2048 + 1024));
#pragma unroll
            for (int mi = 0; mi < 2; ++mi)
#pragma unroll
                for (int nb = 0; nb < 2; ++nb) acc[mi][nb] = MFMA32(bf[nb], af[mi], acc[mi][nb]);
        }
        WAIT_L(0);
    }
    BAR(); asm volatile("" ::: "memory");
#pragma unroll
    for (int mi = 0; mi < 2; ++mi)
#pragma unroll
        for (int nb = 0; nb < 2; ++nb)
#pragma unroll
            for (int g = 0; g < 4; ++g) { const int row = 32 * mi + r32, ch = 8 * nb + 2 * g + hi;
                *(LAS f32x4*)(lds + wid * 16384 + row * 256 + 16 * (ch ^ (row & 15))) = (f32x4){acc[mi][nb][4 * g], acc[mi][nb][4 * g + 1], acc[mi][nb][4 * g + 2], acc[mi][nb][4 * g + 3]}; }
    WAIT_L(0); BAR(); asm volatile("" ::: "memory");
    { const int row = tid >> 3, c8 = tid & 7; f32x4 s0 = {}, s1 = {};
#pragma unroll
      for (int w = 0; w < 8; ++w) { s0 += *(const LAS f32x4*)(lds + w * 16384 + row * 256 + 16 * ((2 * c8) ^ (row & 15)));
                                    s1 += *(const LAS f32x4*)(lds + w * 16384 + row * 256 + 16 * ((2 * c8 + 1) ^ (row & 15))); }
      u32x4 w = {cvtpk(s0[0], s0[1]), cvtpk(s0[2], s0[3]), cvtpk(s1[0], s1[1]), cvtpk(s1[2], s1[3])};
      *(u32x4*)(out + (size_t)(rt * 64 + row) * 512 + ct * 64 + c8 * 8) = w; }
}

extern "C" void kernel_launch(void* const* d_in, const int* in_sizes, int n_in, void* d_out, int out_size, void* d_ws, size_t ws_size,
                              hipStream_t stream) {
    (void)in_sizes; (void)n_in; (void)out_size; (void)ws_size;
    const u16* x = (const u16*)d_in[0]; const u16* ckv = (const u16*)d_in[1]; const u16* wq = (const u16*)d_in[2];
    const u16* wuk = (const u16*)d_in[3]; const u16* wuv = (const u16*)d_in[4]; const u16* wo_a = (const u16*)d_in[5];
    const u16* wqkv = (const u16*)d_in[7]; const u16* wphi = (const u16*)d_in[8]; const u16* wo_b = (const u16*)d_in[9]; const u16* gatew = (const u16*)d_in[10];
    char* ws = (char*)d_ws;
    (void)hipFuncSetAttribute((const void*)k1_proj, hipFuncAttributeMaxDynamicSharedMemorySize, LDS_BYTES);
    (void)hipFuncSetAttribute((const void*)k2_attn, hipFuncAttributeMaxDynamicSharedMemorySize, LDS_BYTES);
    (void)hipFuncSetAttribute((const void*)k3_out, hipFuncAttributeMaxDynamicSharedMemorySize, 131072);
    k1_proj<<<256, 512, LDS_BYTES, stream>>>(x, ckv, wuk, wuv, wqkv, wphi, ws + WS_KV, (float*)(ws + WS_KVP));
    k2_attn<<<256, 512, LDS_BYTES, stream>>>(x, wq, wqkv, wphi, gatew, ws + WS_KV, (const float*)(ws + WS_KVP), ws + WS_A3, ws + WS_QB);
    k3_out<<<256, 512, 131072, stream>>>(ws + WS_A3, wo_a, wo_b, (u16*)d_out);
}
```

```cpp
#include <hip/hip_runtime.h>
#include <hip/hip_bf16.h>
#include <stdint.h>

typedef unsigned short u16;
typedef short bf16x8 __attribute__((ext_vector_type(8)));
typedef short s16x4 __attribute__((ext_vector_type(4)));
typedef float f32x2 __attribute__((ext_vector_type(2)));
typedef float f32x4 __attribute__((ext_vector_type(4)));
typedef float f32x16 __attribute__((ext_vector_type(16)));
typedef unsigned u32x4 __attribute__((ext_vector_type(4)));
typedef unsigned u32x2 __attribute__((ext_vector_type(2)));
typedef __bf16 bf16x2_t __attribute__((ext_vector_type(2)));
#define LAS __attribute__((address_space(3)))
#define DI __device__ __forceinline__
typedef LAS const char* lds_cptr;
typedef LAS char* lds_ptr;

constexpr int SEQ = 2048, DM = 512, NH = 32, HD = 128;
constexpr size_t WS_KV = 0;
constexpr size_t WS_A3 = WS_KV + (size_t)NH * 32 * 32768;
constexpr size_t WS_KVP = WS_A3 + (size_t)576 * SEQ * 16;
constexpr size_t WS_QB = WS_KVP + (size_t)NH * 8 * 4096;
constexpr size_t WS_PART = WS_QB + (size_t)256 * 32768;
constexpr size_t WS_CNT = WS_PART + (size_t)64 * 262144;
constexpr size_t WS_END = WS_CNT + 65536;
constexpr int LDS_BYTES = 163840;

#define MFMA32(a, b, c) __builtin_amdgcn_mfma_f32_32x32x16_bf16((a), (b), (c), 0, 0, 0)
#define MFMA16(a, b, c) __builtin_amdgcn_mfma_f32_16x16x32_bf16((a), (b), (c), 0, 0, 0)
#define SBAR() __builtin_amdgcn_sched_barrier(0)
#define WAIT_V(n) asm volatile("s_waitcnt vmcnt(" #n ")" ::: "memory")
#define WAIT_L(n) asm volatile("s_waitcnt lgkmcnt(" #n ")" ::: "memory")
#define BAR() __builtin_amdgcn_s_barrier()
#define WAIT_BAR(N) asm volatile("s_waitcnt vmcnt(" #N ") lgkmcnt(0)\n\ts_barrier" ::: "memory")

#define STAMP_DECL()
#define STAMP()
#define STAMP_RT()
#define STAMP_OUT(base)
#define STAMP_PARAM
#define STAMP_ARG
DI unsigned rfl(unsigned x) { return __builtin_amdgcn_readfirstlane(x); }
DI unsigned cvtpk(float lo, float hi) { f32x2 v = {lo, hi}; bf16x2_t b = __builtin_convertvector(v, bf16x2_t); return __builtin_bit_cast(unsigned, b); }
DI float bfround(float x) { return __uint_as_float(cvtpk(x, x) << 16); }
DI void glds16(const void* sbase, unsigned voff, unsigned lds_dst) {
    unsigned sv;
    asm volatile("s_mov_b32 %0, m0\n\ts_mov_b32 m0, %3\n\ts_nop 2\n\tglobal_load_lds_dwordx4 %1, %2\n\ts_mov_b32 m0, %0"
                 : "=&s"(sv) : "v"(voff), "s"(sbase), "s"(lds_dst) : "memory");
}
typedef short v4i16_t __attribute__((ext_vector_type(4)));
DI s16x4 trrd(lds_cptr p) { return __builtin_bit_cast(s16x4, __builtin_amdgcn_ds_read_tr16_b64_v4i16((LAS v4i16_t*)p)); }
DI bf16x8 cat8(s16x4 a, s16x4 b) { return __builtin_shufflevector(a, b, 0, 1, 2, 3, 4, 5, 6, 7); }
DI bf16x8 ldsr128(lds_cptr p) { return *(const LAS bf16x8*)p; }
DI bf16x8 pack8(float a0, float a1, float a2, float a3, float a4, float a5, float a6, float a7) {
    u32x4 w = {cvtpk(a0, a1), cvtpk(a2, a3), cvtpk(a4, a5), cvtpk(a6, a7)}; return __builtin_bit_cast(bf16x8, w); }
#define PACK8(X, B) pack8((X)[(B)], (X)[(B) + 1], (X)[(B) + 2], (X)[(B) + 3], (X)[(B) + 4], (X)[(B) + 5], (X)[(B) + 6], (X)[(B) + 7])
DI float swap32_max(float v) { auto rr = __builtin_amdgcn_permlane32_swap(__float_as_uint(v), __float_as_uint(v), false, false); return fmaxf(__uint_as_float(rr[0]), __uint_as_float(rr[1])); }
DI float swap32_add(float v) { auto rr = __builtin_amdgcn_permlane32_swap(__float_as_uint(v), __float_as_uint(v), false, false); return __uint_as_float(rr[0]) + __uint_as_float(rr[1]); }
DI float lo32_bcast(float v) { auto rr = __builtin_amdgcn_permlane32_swap(__float_as_uint(v), __float_as_uint(v), false, false); return __uint_as_float(rr[0]); }
template <int CTRL> DI float dpp(float v) { return __uint_as_float(__builtin_amdgcn_update_dpp(0u, __float_as_uint(v), CTRL, 0xF, 0xF, true)); }
DI float row16_max(float v) { v = fmaxf(v, dpp<0xB1>(v)); v = fmaxf(v, dpp<0x4E>(v)); v = fmaxf(v, dpp<0x141>(v)); v = fmaxf(v, dpp<0x140>(v)); return v; }
DI float row16_add(float v) { v += dpp<0xB1>(v); v += dpp<0x4E>(v); v += dpp<0x141>(v); v += dpp<0x140>(v); return v; }

__global__ __launch_bounds__(512, 2) void k1_proj(const u16* __restrict__ x, const u16* __restrict__ ckv, const u16* __restrict__ wuk,
                                                  const u16* __restrict__ wuv, const u16* __restrict__ wqkv, const u16* __restrict__ wphi,
                                                  char* __restrict__ KV, float* __restrict__ KVP, unsigned* __restrict__ CNT) {
    extern __shared__ __attribute__((aligned(16))) char shm[];
    const lds_ptr lds = (lds_ptr)shm; const unsigned lds0 = (unsigned)(uintptr_t)shm;
    const int tid = threadIdx.x, wid = rfl(tid >> 6), lane = tid & 63, wr = wid >> 2, wc = wid & 3, fr = lane & 15, fq = lane >> 4;
    const int bid = blockIdx.x, hh_ = 4 * (bid & 7) + (bid >> 6), it = (bid >> 3) & 7;
    constexpr int HTB = 16384;
    STAMP_DECL(); STAMP_RT(); STAMP();
#define SA(b, h) (((b) * 2 + (h)) * HTB)
#define SB(b, h) ((4 + (b) * 2 + (h)) * HTB)
    if (tid == 0 && bid < 64) CNT[bid * 64] = 0u;
    unsigned voffA, voffB;
    { const int row = 8 * wid + (lane >> 3), ch = (lane & 7) ^ ((row >> 1) & 7); voffA = (unsigned)(row * 1024 + ch * 16); }
    { const int row = 4 * wid + (lane >> 4), j = lane & 15, s = ((row >> 3) & 1) * 4 + (row & 3), c = (j >> 1) ^ s; voffB = (unsigned)(row * 8192 + c * 32 + (j & 1) * 16); }
    const char* gA = (const char*)ckv + (size_t)it * 256 * 1024;
    const char* gB0 = (const char*)wuk + hh_ * 256; const char* gB1 = (const char*)wuv + hh_ * 256;
    const unsigned ldsw = rfl(lds0 + wid * 1024);
#define STAGE_A(b, h, kt) do { const char* g_ = gA + (h) * 131072 + (kt) * 128; glds16(g_, voffA, ldsw + SA(b, h)); glds16(g_ + 65536, voffA, ldsw + SA(b, h) + 8192); } while (0)
#define STAGE_B(b, h, kt) do { const char* g_ = ((h) ? gB1 : gB0) + (size_t)(kt) * 524288; glds16(g_, voffB, ldsw + SB(b, h)); glds16(g_ + 262144, voffB, ldsw + SB(b, h) + 8192); } while (0)
    lds_cptr pa[2], pb[2];
#pragma unroll
    for (int ks = 0; ks < 2; ++ks) pa[ks] = lds + (wr * 64 + fr) * 128 + 16 * ((4 * ks + fq) ^ ((fr >> 1) & 7));
    { const int q = fr >> 2, p = fr & 3, s = (fq & 1) * 4 + q;
#pragma unroll
      for (int n = 0; n < 2; ++n) pb[n] = lds + (8 * fq + q) * 256 + 32 * ((2 * wc + n) ^ s) + 8 * p; }
#define LDA(dst, b, h) do { _Pragma("unroll") for (int m = 0; m < 4; ++m) _Pragma("unroll") for (int k = 0; k < 2; ++k) dst[m][k] = ldsr128(pa[k] + SA(b, h) + m * 2048); } while (0)
#define LDB(dst, b, h) do { _Pragma("unroll") for (int n = 0; n < 2; ++n) _Pragma("unroll") for (int k = 0; k < 2; ++k) \
        dst[n][k] = cat8(trrd(pb[n] + SB(b, h) + k * 8192), trrd(pb[n] + SB(b, h) + k * 8192 + 1024)); } while (0)
#define MMA(ai, bj, At, Bt) do { __builtin_amdgcn_s_setprio(1); _Pragma("unroll") for (int m = 0; m < 4; ++m) _Pragma("unroll") for (int n = 0; n < 2; ++n) _Pragma("unroll") for (int k = 0; k < 2; ++k) \
        acc[ai][bj][m][n] = (bj) ? MFMA16(At[m][k], Bt[n][k], acc[ai][bj][m][n]) : MFMA16(Bt[n][k], At[m][k], acc[ai][bj][m][n]); __builtin_amdgcn_s_setprio(0); } while (0)
    f32x4 acc[2][2][4][2] = {};
    bf16x8 At[4][2], B0[2][2], B1[2][2];
    constexpr int nt = 8;
    STAGE_B(0, 0, 0); STAGE_A(0, 0, 0); STAGE_B(0, 1, 0); STAGE_A(0, 1, 0);
    if (wr == 1) BAR();
    WAIT_V(4); BAR();
    STAGE_B(1, 0, 1); STAGE_A(1, 0, 1); STAGE_B(1, 1, 1);
    WAIT_V(6); BAR();
    STAMP();
    for (int t = 0; t < nt - 2; t += 2) {
        LDB(B0, 0, 0); SBAR(); LDA(At, 0, 0); STAGE_A(1, 1, t + 1);
        WAIT_L(8); BAR(); WAIT_L(0); MMA(0, 0, At, B0); BAR(); SBAR();
        LDB(B1, 0, 1); STAGE_B(0, 0, t + 2);
        BAR(); WAIT_L(0); MMA(0, 1, At, B1); BAR();
        LDA(At, 0, 1); STAGE_A(0, 0, t + 2);
        BAR(); WAIT_L(0); MMA(1, 0, At, B0); BAR(); SBAR();
        STAGE_B(0, 1, t + 2);
        WAIT_V(6); BAR(); MMA(1, 1, At, B1); BAR();
        LDB(B0, 1, 0); SBAR(); LDA(At, 1, 0); STAGE_A(0, 1, t + 2);
        WAIT_L(8); BAR(); WAIT_L(0); MMA(0, 0, At, B0); BAR(); SBAR();
        LDB(B1, 1, 1); STAGE_B(1, 0, t + 3);
        BAR(); WAIT_L(0); MMA(0, 1, At, B1); BAR();
        LDA(At, 1, 1); STAGE_A(1, 0, t + 3);
        BAR(); WAIT_L(0); MMA(1, 0, At, B0); BAR(); SBAR();
        STAGE_B(1, 1, t + 3);
        WAIT_V(6); BAR(); MMA(1, 1, At, B1); BAR();
    }
    { LDB(B0, 0, 0); WAIT_V(0); LDA(At, 0, 0); STAGE_A(1, 1, nt - 1);
      {
        const int row = 64 * wid + (lane >> 2), sl = lane & 3;
        const unsigned vo = (unsigned)(row * 1536 + 512 + 512 * (sl >> 1) + 16 * hh_ + 8 * (sl & 1)) * 2u;
#pragma unroll
        for (int e = 0; e < 4; ++e) glds16((const char*)wqkv + (size_t)e * 16 * 3072, vo, rfl(lds0 + 131072 + wid * 4096 + e * 1024));
      }
      BAR(); WAIT_L(0); MMA(0, 0, At, B0); BAR();
      LDB(B1, 0, 1); BAR(); WAIT_L(0); MMA(0, 1, At, B1); BAR();
      LDA(At, 0, 1); BAR(); WAIT_L(0); MMA(1, 0, At, B0); MMA(1, 1, At, B1); BAR(); }
    { LDB(B0, 1, 0); LDA(At, 1, 0); BAR(); WAIT_L(0); MMA(0, 0, At, B0); BAR();
      LDB(B1, 1, 1); WAIT_V(0); BAR(); WAIT_L(0); MMA(0, 1, At, B1); BAR();
      LDA(At, 1, 1); BAR(); WAIT_L(0); MMA(1, 0, At, B0); MMA(1, 1, At, B1); BAR(); }
    if (wr == 0) BAR();
    STAMP();
    const int r32 = lane & 31, hi = lane >> 5, blk = (lane >> 4) & 1;
    const char* gx = (const char*)x + (size_t)(256 * it + 32 * wid) * 1024;
    const unsigned vox = (unsigned)(hi * 1024 + ((r32 ^ hi) << 4));
#define XDMA(half) do { _Pragma("unroll") for (int p_ = 0; p_ < 16; ++p_) glds16(gx + (half) * 512 + p_ * 2048, vox ^ ((2 * p_ & 15) << 4), ldsw * 1 + wid * 15360 + p_ * 1024); } while (0)
    XDMA(0);
    u16 wph[8];
#pragma unroll
    for (int j = 0; j < 8; ++j) wph[j] = wphi[hh_ * 256 + (8 * (j >> 2) + 4 * hi + (j & 3)) * 16 + fr];
    u32x4 pv[2][2][2];
#pragma unroll
    for (int ai = 0; ai < 2; ++ai) {
        char* tb = KV + (size_t)(hh_ * 32 + 4 * it + 2 * ai + wr) * 32768;
#pragma unroll
        for (int m = 0; m < 4; ++m) { const f32x4 a = acc[ai][0][m][0], b = acc[ai][0][m][1];
            *(u32x4*)(tb + (4 * wc + fq) * 1024 + (m * 16 + fr) * 16) = (u32x4){cvtpk(a[0], a[1]), cvtpk(a[2], a[3]), cvtpk(b[0], b[1]), cvtpk(b[2], b[3])}; }
#pragma unroll
        for (int u = 0; u < 2; ++u)
#pragma unroll
            for (int n = 0; n < 2; ++n) { const f32x4 a = acc[ai][1][2 * u][n], b = acc[ai][1][2 * u + 1][n];
                pv[ai][u][n] = (u32x4){cvtpk(a[0], a[1]), cvtpk(a[2], a[3]), cvtpk(b[0], b[1]), cvtpk(b[2], b[3])}; }
    }
#undef SA
#undef SB
#undef STAGE_A
#undef STAGE_B
#undef LDA
#undef LDB
#undef MMA
    STAMP();
    {
        const int q = fr >> 2, p = fr & 3;
        const lds_cptr wp = lds + 131072 + (8 * hi + q) * 64 + 32 * blk + 8 * p;
        const unsigned xb = (unsigned)(wid * 16384 + r32 * 512 + ((hi ^ (r32 & 15)) << 4));
        f32x16 d1 = {}, d2 = {};
        bf16x8 wf[4], xf[4];
#define WRD(e) cat8(trrd(wp + (e) * 1024), trrd(wp + (e) * 1024 + 256))
#define XRD(e) ldsr128(lds + ((xb ^ (((e) & 7) << 5)) + (((e) >> 3) & 1) * 256))
#define HALF(h) do { _Pragma("unroll") for (int e = 0; e < 3; ++e) { wf[e] = WRD(16 * (h) + e); xf[e] = XRD(e); } \
        _Pragma("unroll") for (int e = 0; e < 16; ++e) { if (e + 3 < 16) { wf[(e + 3) & 3] = WRD(16 * (h) + e + 3); xf[(e + 3) & 3] = XRD(e + 3); } SBAR(); \
            d1 = MFMA32(xf[e & 3], wf[e & 3], d1); d2 = MFMA32(wf[e & 3], xf[e & 3], d2); SBAR(); } } while (0)
        WAIT_V(8);
        HALF(0);
        XDMA(1);
#pragma unroll
        for (int ai = 0; ai < 2; ++ai) {
            char* tb = KV + (size_t)(hh_ * 32 + 4 * it + 2 * ai + wr) * 32768;
#pragma unroll
            for (int u = 0; u < 2; ++u)
#pragma unroll
                for (int n = 0; n < 2; ++n) *(u32x4*)(tb + 16384 + (4 * u + fq) * 2048 + (wc * 32 + n * 16 + fr) * 16) = pv[ai][u][n];
        }
        WAIT_V(8);
        HALF(1);
#undef WRD
#undef XRD
#undef HALF
#undef XDMA
        STAMP();
        bf16x8 wpf;
#pragma unroll
        for (int j = 0; j < 8; ++j) wpf[j] = (short)(blk ? (wph[j] ^ 0x8000) : wph[j]);
        const bf16x8 kp = PACK8(d2, 0);
        f32x16 d3 = MFMA32(kp, wpf, f32x16{});
#pragma unroll
        for (int r = 0; r < 16; ++r) {
            const float v = bfround(d3[r]); const float mx = row16_max(v);
            const float e = __builtin_amdgcn_exp2f((v - mx) * 1.4426950408889634f); const float s = row16_add(e);
            d3[r] = e * __builtin_amdgcn_rcpf(s);
        }
        const bf16x8 ones = {0x3F80, 0x3F80, 0x3F80, 0x3F80, 0x3F80, 0x3F80, 0x3F80, 0x3F80};
        const bf16x8 v0 = PACK8(d1, 0), v1 = PACK8(d1, 8);
        f32x16 d4 = MFMA32(PACK8(d3, 0), blk ? v0 : ones, f32x16{});
        d4 = MFMA32(PACK8(d3, 8), blk ? v1 : ones, d4);
        LAS float* red = (LAS float*)(lds + wid * 16384);
#pragma unroll
        for (int r = 0; r < 16; ++r) red[((r & 3) + 8 * (r >> 2) + 4 * hi) * 32 + r32] = d4[r];
        WAIT_L(0); BAR(); asm volatile("" ::: "memory");
        f32x2 s = {0.f, 0.f};
#pragma unroll
        for (int w = 0; w < 8; ++w) s += *(const LAS f32x2*)(lds + w * 16384 + tid * 8);
        *(f32x2*)(KVP + (size_t)(hh_ * 8 + it) * 1024 + tid * 2) = s;
    }
    STAMP(); WAIT_V(0); STAMP(); STAMP_RT();
    STAMP_OUT(KV + WS_END);
}

constexpr float QSCALE = 0.08838834764831845f * 1.4426950408889634f;
constexpr float THR = 8.f;
constexpr int L_V = 98304, L_GATE = 102400, L_KVS = 103424;
#define PIN(x) asm volatile("" : "+v"(x))
#define MX3(a, b, c) __builtin_fmaxf(__builtin_fmaxf((a), (b)), (c))
#define EX2(v) __builtin_amdgcn_exp2f(v)
DI void mask_tile(f32x16& s0, f32x16& s1, int dq) {
    asm volatile("" : "+v"(dq));
#pragma unroll
    for (int r = 0; r < 16; ++r) { const int kk = 16 * ((r >> 2) & 1) + 8 * (r >> 3) + (r & 3);
        if (kk > dq) s0[r] = -INFINITY; if (kk + 32 > dq) s1[r] = -INFINITY; }
}

__global__ __launch_bounds__(512, 2) void k2_attn(const u16* __restrict__ x, const u16* __restrict__ wq, const u16* __restrict__ wqkv,
                                                  const u16* __restrict__ wphi, const u16* __restrict__ gatew, const char* __restrict__ KV,
                                                  const float* __restrict__ KVP, char* __restrict__ A3, char* __restrict__ QB) {
    extern __shared__ __attribute__((aligned(16))) char shm[];
    const lds_ptr lds = (lds_ptr)shm; const unsigned lds0 = (unsigned)(uintptr_t)shm;
    const int tid = threadIdx.x, wid = rfl(tid >> 6), lane = tid & 63, G = wid >> 2, w4 = wid & 3;
    const int r32 = lane & 31, hi = lane >> 5, blk = (lane >> 4) & 1, q4 = (lane & 15) >> 2, p4 = lane & 3;
    const int bid = blockIdx.x, hh_ = 4 * (bid & 7) + (bid >> 6), jj = (bid >> 3) & 7;
    const int bA = 15 - jj, bB = jj;
    const int myrow0 = (G ? bB : bA) * 128 + 32 * w4;
    const char* kvw = KV + (size_t)hh_ * 32 * 32768 + w4 * 4096;
    const unsigned vlin = (unsigned)lane * 16u;
    const unsigned ldw = lds0 + w4 * 4096;
    char* qbp = QB + (size_t)(bid * 4 + w4) * 8192 + lane * 16;
    bf16x8 qf[8]; float gate;
    STAMP_DECL(); STAMP_RT(); STAMP();
    {
        f32x2 ps[8]; u16 wph[8];
#pragma unroll
        for (int w = 0; w < 8; ++w) ps[w] = *(const f32x2*)(KVP + (size_t)(hh_ * 8 + w) * 1024 + tid * 2);
#pragma unroll
        for (int j = 0; j < 8; ++j) wph[j] = wphi[hh_ * 256 + (8 * (j >> 2) + 4 * hi + (j & 3)) * 16 + (lane & 15)];
        if (wid == 0) glds16(gatew, vlin, rfl(lds0 + L_GATE));
        unsigned voffX, voffW, voffE;
        { const int row = lane >> 3, ch = (lane & 7) ^ ((row >> 1) & 3); voffX = (unsigned)(row * 1024 + ch * 16); }
        { const int row = 4 * wid + (lane >> 4), j = lane & 15, c = (j >> 2) ^ (row & 3); voffW = (unsigned)(row * 8192 + c * 64 + (j & 3) * 16); }
        { const int row = 8 * wid + (lane >> 1); voffE = (unsigned)(row * 3072 + (lane & 1) * 16); }
        const char* gX = (const char*)x + (size_t)myrow0 * 1024; const char* gW = (const char*)wq + hh_ * 256; const char* gE = (const char*)wqkv + hh_ * 32;
        auto issue = [&](int c, unsigned slot) {
            const unsigned sb = lds0 + slot;
#pragma unroll
            for (int e = 0; e < 4; ++e) glds16(gX + e * 8192 + c * 128, voffX ^ ((e & 1) << 6), rfl(sb + wid * 4096 + e * 1024));
            glds16(gW + (size_t)c * 524288, voffW, rfl(sb + 32768 + wid * 1024));
            glds16(gW + (size_t)c * 524288 + 262144, voffW, rfl(sb + 32768 + 8192 + wid * 1024));
            if (lane < 16) glds16(gE + (size_t)c * 196608, voffE, rfl(sb + 49152 + wid * 256));
        };
        issue(0, 51200u);
        { f32x2 s = ps[0];
#pragma unroll
          for (int w = 1; w < 8; ++w) s += ps[w];
          *(LAS f32x2*)(lds + L_KVS + tid * 8) = s; }
        bf16x8 wpf;
#pragma unroll
        for (int j = 0; j < 8; ++j) wpf[j] = (short)(blk ? (wph[j] ^ 0x8000) : wph[j]);
        PIN(wpf);
        issue(1, 112640u);
        f32x16 qa[5] = {};
        unsigned xo[2], wo_[4];
#pragma unroll
        for (int s = 0; s < 2; ++s) xo[s] = (unsigned)((32 * wid + r32) * 128 + 16 * ((2 * s + hi) ^ ((r32 >> 1) & 7)));
#pragma unroll
        for (int c = 0; c < 4; ++c) wo_[c] = (unsigned)(32768 + (8 * hi + q4) * 256 + 64 * (c ^ q4) + 32 * (p4 >> 1) + 16 * blk + 8 * (p4 & 1));
        const unsigned eo = (unsigned)(49152 + (8 * hi + q4) * 32 + 8 * p4);
        bf16x8 fx[2], fw[2][5], fg[2];
#define QRD(buf, sb, c, ks) do { fx[buf] = ldsr128((sb) + (xo[(ks) & 1] ^ (((ks) >> 1) << 6))); \
        _Pragma("unroll") for (int cc = 0; cc < 4; ++cc) fw[buf][cc] = cat8(trrd((sb) + wo_[cc] + (ks) * 4096), trrd((sb) + wo_[cc] + (ks) * 4096 + 1024)); \
        fw[buf][4] = cat8(trrd((sb) + eo + (ks) * 512), trrd((sb) + eo + (ks) * 512 + 128)); \
        fg[buf] = ldsr128(lds + L_GATE + ((c) * 64 + (ks) * 16 + 8 * hi) * 2); } while (0)
#define QMM(buf) do { _Pragma("unroll") for (int cc = 0; cc < 4; ++cc) qa[cc] = MFMA32(fw[buf][cc], fx[buf], qa[cc]); \
        qa[4] = MFMA32(r32 == 16 ? fg[buf] : fw[buf][4], fx[buf], qa[4]); } while (0)
        unsigned s0_ = 51200u, s1_ = 112640u, s2_ = 0u;
        WAIT_BAR(7);
        STAMP();
        QRD(0, lds + s0_, 0, 0);
        for (int c = 0; c < 8; ++c) {
            const lds_cptr sb = lds + s0_; const lds_cptr sn = lds + s1_;
            SBAR(); QRD(1, sb, c, 1); SBAR(); QMM(0); SBAR();
            QRD(0, sb, c, 2); SBAR(); QMM(1); SBAR();
            if (c < 7) { asm volatile("s_waitcnt vmcnt(0)\n\ts_barrier" ::: "memory"); } else { BAR(); asm volatile("" ::: "memory"); }
            if (c < 6) issue(c + 2, s2_);
            else if (c - 6 == G) {
#pragma unroll
                for (int e = 0; e < 8; ++e) glds16(kvw + (size_t)(G + 2 * (e >> 2)) * 32768 + (e & 3) * 1024, vlin, ldw + G * 49152 + (e >> 2) * 16384 + (e & 3) * 1024);
            }
            SBAR(); QRD(1, sb, c, 3); SBAR(); QMM(0); SBAR();
            if (c < 7) QRD(0, sn, c + 1, 0);
            SBAR(); QMM(1); SBAR();
            const unsigned t_ = s0_; s0_ = s1_; s1_ = s2_; s2_ = t_;
        }
#undef QRD
#undef QMM
        STAMP();
#pragma unroll
        for (int cc = 0; cc < 4; ++cc) {
#pragma unroll
            for (int r = 0; r < 16; ++r) qa[cc][r] *= QSCALE;
            qf[2 * cc] = PACK8(qa[cc], 0); qf[2 * cc + 1] = PACK8(qa[cc], 8);
        }
        if (G) {
#pragma unroll
            for (int s = 0; s < 8; ++s) *(bf16x8*)(qbp + s * 1024) = qf[s];
        }
        { const float gz = bfround(lo32_bcast(qa[4][8])); gate = bfround(1.f / (1.f + __expf(-gz))); }
        float kvv[2][8];
        { const int col = r32 < 16 ? 16 + r32 : 0;
#pragma unroll
          for (int s = 0; s < 2; ++s)
#pragma unroll
              for (int j = 0; j < 8; ++j) { const int f = 16 * s + 8 * (j >> 2) + 4 * hi + (j & 3); const float v = *(const LAS float*)(lds + L_KVS + (f * 32 + col) * 4); kvv[s][j] = r32 <= 16 ? v : 0.f; } }
        f32x16 pq = MFMA32(wpf, PACK8(qa[4], 0), f32x16{});
#pragma unroll
        for (int hf = 0; hf < 2; ++hf) {
            float mx = -INFINITY;
#pragma unroll
            for (int r = 0; r < 8; ++r) { pq[8 * hf + r] = bfround(pq[8 * hf + r]); mx = fmaxf(mx, pq[8 * hf + r]); }
            mx = swap32_max(mx); float s = 0.f;
#pragma unroll
            for (int r = 0; r < 8; ++r) { pq[8 * hf + r] = EX2((pq[8 * hf + r] - mx) * 1.4426950408889634f); s += pq[8 * hf + r]; }
            s = __builtin_amdgcn_rcpf(swap32_add(s));
#pragma unroll
            for (int r = 0; r < 8; ++r) pq[8 * hf + r] *= s;
        }
        f32x16 nm = MFMA32(PACK8(kvv[0], 0), PACK8(pq, 0), f32x16{});
        nm = MFMA32(PACK8(kvv[1], 0), PACK8(pq, 8), nm);
        { const float den = bfround(bfround(lo32_bcast(nm[8])) + 1e-6f); const float sc = (1.f - gate) / den;
          float ov[8];
#pragma unroll
          for (int r = 0; r < 8; ++r) ov[r] = bfround(nm[r]) * sc;
          *(bf16x8*)(A3 + ((size_t)((myrow0 >> 7) * 576 + 512 + 2 * hh_ + hi) * 128 + 32 * w4 + r32) * 16) = PACK8(ov, 0); }
        WAIT_L(0); BAR(); asm volatile("" ::: "memory");
        const lds_ptr qp = lds + L_V + 16384 + (w4 >> 1) * 32768 + (w4 & 1) * 8192 + lane * 16;
        if (G == 0) {
#pragma unroll
            for (int s = 0; s < 8; ++s) *(LAS bf16x8*)(qp + s * 1024) = qf[s];
        }
        WAIT_BAR(0);
        if (G) {
#pragma unroll
            for (int s = 0; s < 8; ++s) qf[s] = ldsr128(qp + s * 1024);
        }
    }
    STAMP();
    const int prow = (r32 & 7) | ((r32 & 8) << 1) | ((r32 & 16) >> 1);
    const unsigned kro = (unsigned)(prow * 16 + hi * 1024), vro = (unsigned)(L_V + G * 32768 + r32 * 16 + hi * 2048);
    const int dq = 32 * w4 + r32 - 64 * G - 4 * hi;
    unsigned ks0 = G * 49152u, ks1 = ks0 + 16384u, ks2 = ks0 + 32768u;
    f32x16 o[4], negm, sA0, sA1, sB0, sB1; bf16x8 kf[16], vf[16]; u32x4 pw[4]; float m, l;
#define KRD(i, kp) kf[i] = ldsr128((kp) + ((i) >> 1) * 2048 + ((i) & 1) * 512)
#define VRD(i, vp) vf[i] = ldsr128((vp) + ((i) >> 2) * 4096 + ((i) & 3) * 512)
#define PF(k) __builtin_bit_cast(bf16x8, pw[k])
#define DMA1(i) do { if ((i) < 4) glds16(gk_ + (i) * 1024, vlin, dk_ + (i) * 1024); else glds16(gv_ + ((i) - 4) * 1024, vlin, dv_ + ((i) - 4) * 1024); } while (0)
#define ROT() do { const unsigned t_ = ks0; ks0 = ks1; ks1 = ks2; ks2 = t_; } while (0)
#define DMA_SETUP(r) const int tk_ = (r) + 2 <= R ? (r) + 2 : (pass == 0 ? (r) + 1 - R : 0); \
        const char* gk_ = kvw + (size_t)(2 * tk_ + G) * 32768; const char* gv_ = kvw + (size_t)(2 * (r) + G) * 32768 + 16384; \
        const unsigned dk_ = ldw + ks2, dv_ = ldw + L_V + G * 32768 + ((r) & 1) * 16384;
#define GAPA(i, C0, C1, P0, P1) do { \
        if ((i) + 4 < 16) { KRD((i) + 4, kp_); } else { VRD((i) - 12, vp_); } \
        if (((i) & 1) == 0) DMA1((i) >> 1); \
        if ((i) & 1) C1 = MFMA32(kf[i], qf[(i) >> 1], (i) < 2 ? negm : C1); else C0 = MFMA32(kf[i], qf[(i) >> 1], (i) < 2 ? negm : C0); \
        if ((i) < 8) { P1[2 * (i)] = EX2(P1[2 * (i)]); P1[2 * (i) + 1] = EX2(P1[2 * (i) + 1]); pw[(i) >> 2][(i) & 3] = cvtpk(P0[2 * (i)], P0[2 * (i) + 1]); PIN(P1); PIN(pw[(i) >> 2]); } \
        else { l += P1[2 * (i) - 16]; l2 += P1[2 * (i) - 15]; pw[(i) >> 2][(i) & 3] = cvtpk(P1[2 * (i) - 16], P1[2 * (i) - 15]); PIN(l); PIN(l2); PIN(pw[(i) >> 2]); } \
        SBAR(); } while (0)
#define PVM(i) o[(i) & 3] = MFMA32(vf[i], PF((i) >> 2), o[(i) & 3])
#define RDB(i) do { if ((i) + 4 < 16) { VRD((i) + 4, vp_); } else { KRD((i) - 12, kn_); } } while (0)
#define ROUND(C0, C1, P0, P1, r) do { \
        const lds_cptr kp_ = lds + ks0 + kro; const lds_cptr kn_ = lds + ks1 + kro; const lds_cptr vp_ = lds + vro + (((r) - 1) & 1) * 16384; \
        DMA_SETUP(r) float l2 = 0.f; SBAR(); \
        GAPA(0, C0, C1, P0, P1); GAPA(1, C0, C1, P0, P1); GAPA(2, C0, C1, P0, P1); GAPA(3, C0, C1, P0, P1); \
        GAPA(4, C0, C1, P0, P1); GAPA(5, C0, C1, P0, P1); GAPA(6, C0, C1, P0, P1); GAPA(7, C0, C1, P0, P1); \
        GAPA(8, C0, C1, P0, P1); GAPA(9, C0, C1, P0, P1); GAPA(10, C0, C1, P0, P1); GAPA(11, C0, C1, P0, P1); \
        GAPA(12, C0, C1, P0, P1); GAPA(13, C0, C1, P0, P1); GAPA(14, C0, C1, P0, P1); GAPA(15, C0, C1, P0, P1); \
        l += l2; \
        RDB(0); PVM(0); SBAR(); RDB(1); PVM(1); SBAR(); \
        if ((r) == R) mask_tile(C0, C1, dq); \
        float ma, mb; \
        RDB(2); PVM(2); ma = MX3(C0[0], C0[1], C1[0]); mb = MX3(C0[2], C0[3], C1[1]); ma = MX3(ma, C1[2], C1[3]); mb = MX3(mb, C0[4], C0[5]); PIN(ma); PIN(mb); SBAR(); \
        RDB(3); PVM(3); ma = MX3(ma, C0[6], C0[7]); mb = MX3(mb, C1[4], C1[5]); ma = MX3(ma, C1[6], C1[7]); mb = MX3(mb, C0[8], C0[9]); PIN(ma); PIN(mb); SBAR(); \
        RDB(4); PVM(4); ma = MX3(ma, C0[10], C0[11]); mb = MX3(mb, C1[8], C1[9]); ma = MX3(ma, C1[10], C1[11]); mb = MX3(mb, C0[12], C0[13]); PIN(ma); PIN(mb); SBAR(); \
        RDB(5); PVM(5); ma = MX3(ma, C0[14], C0[15]); mb = MX3(mb, C1[12], C1[13]); ma = MX3(ma, C1[14], C1[15]); \
        const float rm = swap32_max(fmaxf(ma, mb)); bool pend = false; float fsc = 1.f; \
        if (__builtin_expect(__any(rm > THR), 0)) { const float dl = fmaxf(rm, 0.f); m += dl; fsc = EX2(-dl); l *= fsc; pend = true; \
            _Pragma("unroll") for (int e = 0; e < 16; ++e) { C0[e] -= dl; C1[e] -= dl; negm[e] = -m; } } \
        SBAR(); \
        RDB(6); PVM(6); C0[0] = EX2(C0[0]); C0[1] = EX2(C0[1]); PIN(C0); SBAR(); \
        RDB(7); PVM(7); C0[2] = EX2(C0[2]); C0[3] = EX2(C0[3]); l += C0[0]; l2 = C0[1]; PIN(C0); PIN(l); SBAR(); \
        RDB(8); PVM(8); C0[4] = EX2(C0[4]); C0[5] = EX2(C0[5]); l += C0[2]; l2 += C0[3]; PIN(C0); PIN(l); PIN(l2); SBAR(); \
        RDB(9); PVM(9); C0[6] = EX2(C0[6]); C0[7] = EX2(C0[7]); l += C0[4]; l2 += C0[5]; PIN(C0); PIN(l); PIN(l2); SBAR(); \
        RDB(10); PVM(10); C0[8] = EX2(C0[8]); C0[9] = EX2(C0[9]); l += C0[6]; l2 += C0[7]; PIN(C0); PIN(l); PIN(l2); SBAR(); \
        RDB(11); PVM(11); C0[10] = EX2(C0[10]); C0[11] = EX2(C0[11]); l += C0[8]; l2 += C0[9]; PIN(C0); PIN(l); PIN(l2); SBAR(); \
        RDB(12); PVM(12); C0[12] = EX2(C0[12]); C0[13] = EX2(C0[13]); l += C0[10]; l2 += C0[11]; PIN(C0); PIN(l); PIN(l2); SBAR(); \
        RDB(13); PVM(13); C0[14] = EX2(C0[14]); C0[15] = EX2(C0[15]); l += C0[12]; l2 += C0[13]; PIN(C0); PIN(l); PIN(l2); SBAR(); \
        RDB(14); PVM(14); l += C0[14]; l2 += C0[15]; PIN(l); PIN(l2); SBAR(); \
        RDB(15); PVM(15); l += l2; PIN(l); SBAR(); \
        if (__builtin_expect(pend, 0)) { _Pragma("unroll") for (int c = 0; c < 4; ++c) _Pragma("unroll") for (int e = 0; e < 16; ++e) o[c][e] *= fsc; } \
        asm volatile("s_waitcnt vmcnt(0)\n\ts_barrier" ::: "memory"); ROT(); } while (0)

    for (int pass = 0; pass < 2; ++pass) {
        const int R = pass ? bB : bA;
        {
            const lds_cptr kp_ = lds + ks0 + kro; const lds_cptr kn_ = lds + ks1 + kro;
            DMA_SETUP(0)
#pragma unroll
            for (int i = 0; i < 6; ++i) KRD(i, kp_);
#pragma unroll
            for (int i = 0; i < 8; ++i) DMA1(i);
#pragma unroll
            for (int c = 0; c < 4; ++c) o[c] = f32x16{};
            SBAR();
#pragma unroll
            for (int i = 0; i < 16; ++i) {
                if (i + 6 < 16) KRD(i + 6, kp_);
                if (i & 1) sA1 = MFMA32(kf[i], qf[i >> 1], i < 2 ? f32x16{} : sA1); else sA0 = MFMA32(kf[i], qf[i >> 1], i < 2 ? f32x16{} : sA0);
                SBAR();
            }
            if (R == 0) mask_tile(sA0, sA1, dq);
            float ma = MX3(sA0[0], sA0[1], sA1[0]), mb = MX3(sA0[2], sA0[3], sA1[1]); ma = MX3(ma, sA1[2], sA1[3]);
#pragma unroll
            for (int r = 4; r < 16; r += 4) { ma = MX3(ma, sA0[r], sA0[r + 1]); mb = MX3(mb, sA0[r + 2], sA0[r + 3]); ma = MX3(ma, sA1[r], sA1[r + 1]); mb = MX3(mb, sA1[r + 2], sA1[r + 3]); }
            m = fmaxf(swap32_max(fmaxf(ma, mb)), -1e30f); l = 0.f;
#pragma unroll
            for (int i = 0; i < 4; ++i) KRD(i, kn_);
#pragma unroll
            for (int e = 0; e < 16; ++e) { sA0[e] = EX2(sA0[e] - m); l += sA0[e]; sA1[e] -= m; negm[e] = -m; }
            asm volatile("s_waitcnt vmcnt(0)\n\ts_barrier" ::: "memory"); ROT();
        }
        STAMP();
        int r = 1;
        for (; r + 1 <= R; r += 2) { ROUND(sB0, sB1, sA0, sA1, r); ROUND(sA0, sA1, sB0, sB1, r + 1); }
        if (r <= R) { ROUND(sB0, sB1, sA0, sA1, r); sA0 = sB0; sA1 = sB1; }
        STAMP();
        {
            const lds_cptr vp_ = lds + vro + (R & 1) * 16384;
#pragma unroll
            for (int i = 0; i < 8; ++i) VRD(i, vp_);
            if (pass == 0) {
#pragma unroll
                for (int s = 0; s < 8; ++s) qf[s] = *(const bf16x8*)(qbp + s * 1024);
            }
#pragma unroll
            for (int e = 0; e < 16; ++e) { sA1[e] = EX2(sA1[e]); l += sA1[e]; }
#pragma unroll
            for (int i = 0; i < 8; ++i) { pw[i >> 2][i & 3] = cvtpk(sA0[2 * i], sA0[2 * i + 1]); pw[2 + (i >> 2)][i & 3] = cvtpk(sA1[2 * i], sA1[2 * i + 1]); }
            SBAR();
#pragma unroll
            for (int i = 0; i < 16; ++i) { if (i + 8 < 16) VRD(i + 8, vp_); PVM(i); SBAR(); }
        }
        l = swap32_add(l);
        BAR(); asm volatile("" ::: "memory");
        const unsigned st0 = G ? ks2 - 49152u : ks2;
        { const lds_ptr xp = lds + L_V + wid * 8192 + lane * 16;
#pragma unroll
          for (int c = 0; c < 2; ++c)
#pragma unroll
              for (int g = 0; g < 4; ++g) { f32x4 t;
#pragma unroll
                  for (int e = 0; e < 4; ++e) t[e] = G ? o[c][4 * g + e] : o[2 + c][4 * g + e];
                  *(LAS f32x4*)(xp + (c * 4 + g) * 1024) = t; }
          *(LAS f32x4*)(lds + st0 + wid * 1024 + lane * 16) = (f32x4){m, l, gate, 0.f}; }
        WAIT_L(0); BAR(); asm volatile("" ::: "memory");
        { const int ow = wid ^ 4;
          const f32x4 st = *(const LAS f32x4*)(lds + st0 + ow * 1024 + lane * 16);
          const float mn = fmaxf(m, st[0]), f0 = EX2(m - mn), f1 = EX2(st[0] - mn);
          const float gg = (G == pass) ? gate : st[2];
          const float inv = gg / (l * f0 + st[1] * f1); const float c0 = f0 * inv, c1 = f1 * inv;
          const lds_cptr xp = lds + L_V + ow * 8192 + lane * 16;
          char* dst = A3 + ((size_t)(R * 576 + hh_ * 16 + G * 8 + hi) * 128 + 32 * w4 + r32) * 16;
#pragma unroll
          for (int c = 0; c < 2; ++c) {
              float v[16];
#pragma unroll
              for (int g = 0; g < 4; ++g) { const f32x4 t = *(const LAS f32x4*)(xp + (c * 4 + g) * 1024);
#pragma unroll
                  for (int e = 0; e < 4; ++e) v[4 * g + e] = (G ? o[2 + c][4 * g + e] : o[c][4 * g + e]) * c0 + t[e] * c1; }
              *(bf16x8*)(dst + (4 * c) * 2048) = PACK8(v, 0);
              *(bf16x8*)(dst + (4 * c + 2) * 2048) = PACK8(v, 8);
          } }
        WAIT_L(0); BAR(); asm volatile("" ::: "memory");
        STAMP();
    }
    WAIT_V(0); STAMP(); STAMP_RT();
    STAMP_OUT(KV + WS_END + 524288);
#undef KRD
#undef VRD
#undef PF
#undef DMA1
#undef ROT
#undef DMA_SETUP
#undef GAPA
#undef PVM
#undef RDB
#undef ROUND
}

__global__ __launch_bounds__(512, 2) void k3_out(const char* __restrict__ A3, const u16* __restrict__ wo_a, const u16* __restrict__ wo_b, u16* __restrict__ out,
                                                 char* __restrict__ PART) {
    extern __shared__ __attribute__((aligned(16))) char shm[];
    const lds_ptr lds = (lds_ptr)shm; const unsigned lds0 = (unsigned)(uintptr_t)shm;
    const int tid = threadIdx.x, wid = rfl(tid >> 6), lane = tid & 63;
    const int r32 = lane & 31, hi = lane >> 5, blk = (lane >> 4) & 1, q4 = (lane & 15) >> 2, p4 = lane & 3;
    const int bid = blockIdx.x, rt = 4 * (bid & 7) + (bid >> 6), ct = (bid >> 3) & 7;
    const unsigned vlin = (unsigned)lane * 16u;
    STAMP_DECL(); STAMP_RT(); STAMP();
    unsigned voffW; { const int row = lane >> 3, sl = lane & 7, nb = (sl >> 2) ^ ((row >> 1) & 1); voffW = (unsigned)(row * 1024 + nb * 64 + (sl & 3) * 16); }
    const char* gA = A3 + (size_t)((rt >> 1) * 576 + 2 * wid) * 2048 + (rt & 1) * 1024;
    const unsigned wb = lds0 + wid * 20480;
    auto issue = [&](int i, unsigned slot) {
        const int sg = wid + 8 * i;
        const char* gw = (i < 32 ? (const char*)wo_a + (size_t)sg * 16384 : (const char*)wo_b + (size_t)(sg - 256) * 16384) + ct * 128;
        const char* ga = gA + (size_t)i * 32768;
        glds16(ga, vlin, wb + slot); glds16(ga + 2048, vlin, wb + slot + 1024);
        glds16(gw, voffW, wb + slot + 2048); glds16(gw + 8192, voffW, wb + slot + 3072);
    };
    f32x16 acc[2][2] = {};
    const unsigned ao = (unsigned)(wid * 20480 + hi * 1024 + r32 * 16);
    unsigned bo[2];
#pragma unroll
    for (int nb = 0; nb < 2; ++nb) bo[nb] = (unsigned)(wid * 20480 + 2048 + (4 * hi + q4) * 128 + 64 * (nb ^ ((q4 >> 1) & 1)) + 32 * blk + 8 * p4);
#pragma unroll
    for (int i = 0; i < 5; ++i) issue(i, i * 4096u);
    bf16x8 af[2][2], bf[2][2];
#define K3RD(buf, sb) do { af[buf][0] = ldsr128((sb) + ao); bf[buf][0] = cat8(trrd((sb) + bo[0]), trrd((sb) + bo[0] + 1024)); \
        af[buf][1] = ldsr128((sb) + ao + 512); bf[buf][1] = cat8(trrd((sb) + bo[1]), trrd((sb) + bo[1] + 1024)); } while (0)
#define K3STEP(buf, i) do { \
        if ((i) < 32) { asm volatile("s_waitcnt vmcnt(12) lgkmcnt(0)" ::: "memory"); } else if ((i) == 32) { asm volatile("s_waitcnt vmcnt(8) lgkmcnt(0)" ::: "memory"); } \
        else if ((i) == 33) { asm volatile("s_waitcnt vmcnt(4) lgkmcnt(0)" ::: "memory"); } else { asm volatile("s_waitcnt vmcnt(0) lgkmcnt(0)" ::: "memory"); } \
        const unsigned snx = sc == 16384u ? 0u : sc + 4096u; \
        if ((i) + 1 < 36) K3RD(buf ^ 1, lds + snx); \
        SBAR(); acc[0][0] = MFMA32(bf[buf][0], af[buf][0], acc[0][0]); acc[1][0] = MFMA32(bf[buf][0], af[buf][1], acc[1][0]); SBAR(); \
        if ((i) + 5 < 36) issue((i) + 5, sc); \
        SBAR(); acc[0][1] = MFMA32(bf[buf][1], af[buf][0], acc[0][1]); acc[1][1] = MFMA32(bf[buf][1], af[buf][1], acc[1][1]); SBAR(); \
        sc = snx; } while (0)
    unsigned sc = 0u;
    WAIT_V(16);
    K3RD(0, lds);
    for (int i = 0; i < 36; i += 2) { K3STEP(0, i); K3STEP(1, i + 1); }
#undef K3RD
#undef K3STEP
    STAMP();
    BAR(); asm volatile("" ::: "memory");
#pragma unroll
    for (int mi = 0; mi < 2; ++mi)
#pragma unroll
        for (int nb = 0; nb < 2; ++nb)
#pragma unroll
            for (int g = 0; g < 4; ++g) { const int row = 32 * mi + r32, ch = 8 * nb + 2 * g + hi;
                *(LAS f32x4*)(lds + wid * 16384 + row * 256 + 16 * (ch ^ (row & 15))) = (f32x4){acc[mi][nb][4 * g], acc[mi][nb][4 * g + 1], acc[mi][nb][4 * g + 2], acc[mi][nb][4 * g + 3]}; }
    WAIT_L(0); BAR(); asm volatile("" ::: "memory");
    { const int row = tid >> 3, c8 = tid & 7; f32x4 s0 = {}, s1 = {};
#pragma unroll
      for (int w = 0; w < 8; ++w) { s0 += *(const LAS f32x4*)(lds + w * 16384 + row * 256 + 16 * ((2 * c8) ^ (row & 15)));
                                    s1 += *(const LAS f32x4*)(lds + w * 16384 + row * 256 + 16 * ((2 * c8 + 1) ^ (row & 15))); }
      u32x4 w = {cvtpk(s0[0], s0[1]), cvtpk(s0[2], s0[3]), cvtpk(s1[0], s1[1]), cvtpk(s1[2], s1[3])};
      *(u32x4*)(out + (size_t)(rt * 64 + row) * 512 + ct * 64 + c8 * 8) = w; }
    STAMP(); WAIT_V(0); STAMP(); STAMP_RT();
    STAMP_OUT(PART + (WS_END - WS_PART) + 2 * 524288);
}

extern "C" void kernel_launch(void* const* d_in, const int* in_sizes, int n_in, void* d_out, int out_size, void* d_ws, size_t ws_size,
                              hipStream_t stream) {
    (void)in_sizes; (void)n_in; (void)out_size; (void)ws_size;
    const u16* x = (const u16*)d_in[0]; const u16* ckv = (const u16*)d_in[1]; const u16* wq = (const u16*)d_in[2];
    const u16* wuk = (const u16*)d_in[3]; const u16* wuv = (const u16*)d_in[4]; const u16* wo_a = (const u16*)d_in[5];
    const u16* wqkv = (const u16*)d_in[7]; const u16* wphi = (const u16*)d_in[8]; const u16* wo_b = (const u16*)d_in[9]; const u16* gatew = (const u16*)d_in[10];
    char* ws = (char*)d_ws;
    (void)hipFuncSetAttribute((const void*)k1_proj, hipFuncAttributeMaxDynamicSharedMemorySize, LDS_BYTES);
    (void)hipFuncSetAttribute((const void*)k2_attn, hipFuncAttributeMaxDynamicSharedMemorySize, LDS_BYTES);
    (void)hipFuncSetAttribute((const void*)k3_out, hipFuncAttributeMaxDynamicSharedMemorySize, LDS_BYTES);
    k1_proj<<<256, 512, LDS_BYTES, stream>>>(x, ckv, wuk, wuv, wqkv, wphi, ws + WS_KV, (float*)(ws + WS_KVP), (unsigned*)(ws + WS_CNT));
    k2_attn<<<256, 512, LDS_BYTES, stream>>>(x, wq, wqkv, wphi, gatew, ws + WS_KV, (const float*)(ws + WS_KVP), ws + WS_A3, ws + WS_QB);
    k3_out<<<256, 512, LDS_BYTES, stream>>>(ws + WS_A3, wo_a, wo_b, (u16*)d_out, ws + WS_PART);
}
```

```cpp
#include <hip/hip_runtime.h>
#include <hip/hip_bf16.h>
#include <stdint.h>

typedef unsigned short u16;
typedef short bf16x8 __attribute__((ext_vector_type(8)));
typedef short s16x4 __attribute__((ext_vector_type(4)));
typedef float f32x2 __attribute__((ext_vector_type(2)));
typedef float f32x4 __attribute__((ext_vector_type(4)));
typedef float f32x16 __attribute__((ext_vector_type(16)));
typedef unsigned u32x4 __attribute__((ext_vector_type(4)));
typedef unsigned u32x2 __attribute__((ext_vector_type(2)));
typedef __bf16 bf16x2_t __attribute__((ext_vector_type(2)));
#define LAS __attribute__((address_space(3)))
#define DI __device__ __forceinline__
typedef LAS const char* lds_cptr;
typedef LAS char* lds_ptr;

constexpr int SEQ = 2048, DM = 512, NH = 32, HD = 128;
constexpr size_t WS_CNT = 0;
constexpr size_t CNT_BYTES = 80 * 256;
constexpr size_t WS_KV = 65536;
constexpr size_t WS_A3 = WS_KV + (size_t)NH * 32 * 32768;
constexpr size_t WS_KVP = WS_A3 + (size_t)576 * SEQ * 16;
constexpr size_t WS_QB = WS_KVP + (size_t)NH * 8 * 4096;
constexpr size_t WS_PQ = WS_QB + (size_t)256 * 32768;
constexpr size_t WS_END = WS_PQ + (size_t)256 * 16384;
constexpr int LDS_BYTES = 163840;

#define MFMA32(a, b, c) __builtin_amdgcn_mfma_f32_32x32x16_bf16((a), (b), (c), 0, 0, 0)
#define MFMA16(a, b, c) __builtin_amdgcn_mfma_f32_16x16x32_bf16((a), (b), (c), 0, 0, 0)
#define SBAR() __builtin_amdgcn_sched_barrier(0)
#define WAIT_V(n) asm volatile("s_waitcnt vmcnt(" #n ")" ::: "memory")
#define WAIT_L(n) asm volatile("s_waitcnt lgkmcnt(" #n ")" ::: "memory")
#define BAR() __builtin_amdgcn_s_barrier()
#define WAIT_BAR(N) asm volatile("s_waitcnt vmcnt(" #N ") lgkmcnt(0)\n\ts_barrier" ::: "memory")

#define STAMP_DECL()
#define STAMP()
#define STAMP_RT()
#define STAMP_OUT(base)
#define STAMP_PARAM
#define STAMP_ARG
DI unsigned rfl(unsigned x) { return __builtin_amdgcn_readfirstlane(x); }
DI unsigned cvtpk(float lo, float hi) { f32x2 v = {lo, hi}; bf16x2_t b = __builtin_convertvector(v, bf16x2_t); return __builtin_bit_cast(unsigned, b); }
DI float bfround(float x) { return __uint_as_float(cvtpk(x, x) << 16); }
DI void glds16(const void* sbase, unsigned voff, unsigned lds_dst) {
    unsigned sv;
    asm volatile("s_mov_b32 %0, m0\n\ts_mov_b32 m0, %3\n\ts_nop 2\n\tglobal_load_lds_dwordx4 %1, %2\n\ts_mov_b32 m0, %0"
                 : "=&s"(sv) : "v"(voff), "s"(sbase), "s"(lds_dst) : "memory");
}
DI void glds16c(const void* sbase, unsigned voff, unsigned lds_dst) {
    unsigned sv;
    asm volatile("s_mov_b32 %0, m0\n\ts_mov_b32 m0, %3\n\ts_nop 2\n\tglobal_load_lds_dwordx4 %1, %2 sc1\n\ts_mov_b32 m0, %0"
                 : "=&s"(sv) : "v"(voff), "s"(sbase), "s"(lds_dst) : "memory");
}
typedef short v4i16_t __attribute__((ext_vector_type(4)));
DI s16x4 trrd(lds_cptr p) { return __builtin_bit_cast(s16x4, __builtin_amdgcn_ds_read_tr16_b64_v4i16((LAS v4i16_t*)p)); }
DI bf16x8 cat8(s16x4 a, s16x4 b) { return __builtin_shufflevector(a, b, 0, 1, 2, 3, 4, 5, 6, 7); }
DI bf16x8 ldsr128(lds_cptr p) { return *(const LAS bf16x8*)p; }
DI bf16x8 pack8(float a0, float a1, float a2, float a3, float a4, float a5, float a6, float a7) {
    u32x4 w = {cvtpk(a0, a1), cvtpk(a2, a3), cvtpk(a4, a5), cvtpk(a6, a7)}; return __builtin_bit_cast(bf16x8, w); }
#define PACK8(X, B) pack8((X)[(B)], (X)[(B) + 1], (X)[(B) + 2], (X)[(B) + 3], (X)[(B) + 4], (X)[(B) + 5], (X)[(B) + 6], (X)[(B) + 7])
DI float swap32_max(float v) { auto rr = __builtin_amdgcn_permlane32_swap(__float_as_uint(v), __float_as_uint(v), false, false); return fmaxf(__uint_as_float(rr[0]), __uint_as_float(rr[1])); }
DI float swap32_add(float v) { auto rr = __builtin_amdgcn_permlane32_swap(__float_as_uint(v), __float_as_uint(v), false, false); return __uint_as_float(rr[0]) + __uint_as_float(rr[1]); }
DI float lo32_bcast(float v) { auto rr = __builtin_amdgcn_permlane32_swap(__float_as_uint(v), __float_as_uint(v), false, false); return __uint_as_float(rr[0]); }
template <int CTRL> DI float dpp(float v) { return __uint_as_float(__builtin_amdgcn_update_dpp(0u, __float_as_uint(v), CTRL, 0xF, 0xF, true)); }
DI float row16_max(float v) { v = fmaxf(v, dpp<0xB1>(v)); v = fmaxf(v, dpp<0x4E>(v)); v = fmaxf(v, dpp<0x141>(v)); v = fmaxf(v, dpp<0x140>(v)); return v; }
DI float row16_add(float v) { v += dpp<0xB1>(v); v += dpp<0x4E>(v); v += dpp<0x141>(v); v += dpp<0x140>(v); return v; }

constexpr float QSCALE = 0.08838834764831845f * 1.4426950408889634f;
constexpr float THR = 8.f;
constexpr int L_V = 98304;
constexpr int L_WE = 65536, L_GATE = 139264;
#define PIN(x) asm volatile("" : "+v"(x))
#define MX3(a, b, c) __builtin_fmaxf(__builtin_fmaxf((a), (b)), (c))
#define EX2(v) __builtin_amdgcn_exp2f(v)
#define RLX_AGENT __ATOMIC_RELAXED, __HIP_MEMORY_SCOPE_AGENT
#define ST_PL(base, voff, imm, v) asm volatile("s_nop 4\n\tglobal_store_dwordx4 %0, %1, %2 offset:%3\n\ts_nop 1" :: "v"(voff), "v"(v), "s"(base), "i"(imm) : "memory")
#define ST_SC1(base, voff, imm, v) asm volatile("s_nop 4\n\tglobal_store_dwordx4 %0, %1, %2 offset:%3 sc1\n\ts_nop 1" :: "v"(voff), "v"(v), "s"(base), "i"(imm) : "memory")
DI void wait_cnt(const unsigned* p, unsigned want) {
    unsigned spins = 0;
    while (rfl(__hip_atomic_load(p, RLX_AGENT)) < want) { __builtin_amdgcn_s_sleep(2); if (++spins > (1u << 20)) break; }
}
DI void dual_softmax(f32x16& p) {
#pragma unroll
    for (int hf = 0; hf < 2; ++hf) {
        float mx = -INFINITY;
#pragma unroll
        for (int r = 0; r < 8; ++r) { p[8 * hf + r] = bfround(p[8 * hf + r]); mx = fmaxf(mx, p[8 * hf + r]); }
        mx = swap32_max(mx); float s = 0.f;
#pragma unroll
        for (int r = 0; r < 8; ++r) { p[8 * hf + r] = EX2((p[8 * hf + r] - mx) * 1.4426950408889634f); s += p[8 * hf + r]; }
        s = __builtin_amdgcn_rcpf(swap32_add(s));
#pragma unroll
        for (int r = 0; r < 8; ++r) p[8 * hf + r] *= s;
    }
}
DI void mask_tile(f32x16& s0, f32x16& s1, int dq) {
    asm volatile("" : "+v"(dq));
#pragma unroll
    for (int r = 0; r < 16; ++r) { const int kk = 16 * ((r >> 2) & 1) + 8 * (r >> 3) + (r & 3);
        if (kk > dq) s0[r] = -INFINITY; if (kk + 32 > dq) s1[r] = -INFINITY; }
}

__global__ __launch_bounds__(512, 2) void kf_all(const u16* __restrict__ x, const u16* __restrict__ ckv, const u16* __restrict__ wq, const u16* __restrict__ wuk,
                                                 const u16* __restrict__ wuv, const u16* __restrict__ wo_a, const u16* __restrict__ wqkv,
                                                 const u16* __restrict__ wphi, const u16* __restrict__ wo_b, const u16* __restrict__ gatew,
                                                 char* ws, u16* __restrict__ out) {
    extern __shared__ __attribute__((aligned(16))) char shm[];
    const lds_ptr lds = (lds_ptr)shm; const unsigned lds0 = (unsigned)(uintptr_t)shm;
    const int wid = rfl(threadIdx.x >> 6), G = wid >> 2, w4 = wid & 3;
    const int bid = blockIdx.x, hh_ = 4 * (bid & 7) + (bid >> 6), jj = (bid >> 3) & 7;
#define LANE_VARS() int tid = threadIdx.x; asm volatile("" : "+v"(tid)); const int lane = tid & 63, r32 = lane & 31, hi = lane >> 5, blk = (lane >> 4) & 1, \
        q4 = (lane & 15) >> 2, p4 = lane & 3; const unsigned vlin = (unsigned)lane * 16u; (void)r32; (void)hi; (void)blk; (void)q4; (void)p4; (void)vlin
    unsigned* const CNT = (unsigned*)(ws + WS_CNT);
    char* const KVh = ws + WS_KV + (size_t)hh_ * 32 * 32768;
    char* const A3 = ws + WS_A3;
    STAMP_DECL(); STAMP_RT(); STAMP();
    u32x4 pkv[16];
    const int bA = 15 - jj, bB = jj;
    const char* gX = (const char*)x + (size_t)((G ? bB : bA) * 128 + 32 * w4) * 1024; const char* gW = (const char*)wq + hh_ * 256; const char* gE = (const char*)wqkv + hh_ * 32;
    const unsigned lx = lds0 + wid * 4096, lw = lds0 + L_WE + wid * 1024;
    unsigned voffX, voffW, voffE;
    { const int lane = threadIdx.x & 63;
        { const int row = lane >> 3, ch = (lane & 7) ^ ((row >> 1) & 3); voffX = (unsigned)(row * 1024 + ch * 16); }
        { const int row = 4 * wid + (lane >> 4), j = lane & 15, c = (j >> 2) ^ (row & 3); voffW = (unsigned)(row * 8192 + c * 64 + (j & 3) * 16); }
        { const int row = 8 * wid + (lane >> 3), sl = lane & 7, hf = (sl >> 2) ^ ((row >> 1) & 1), part = hf ? 2 : ((sl >> 1) & 1);
          voffE = (unsigned)(row * 3072 + part * 1024 + (sl & 1) * 16); }
        if (wid == 0) glds16(gatew, (unsigned)lane * 16u, lds0 + L_GATE);
        PIN(voffX); PIN(voffW); PIN(voffE); }
#define XISS(c) do { _Pragma("unroll") for (int e = 0; e < 4; ++e) glds16(gX + e * 8192 + (c) * 128, voffX ^ ((e & 1) << 6), lx + ((c) & 1) * 32768 + e * 1024); } while (0)
#define WISS(c) do { glds16(gW + (size_t)(c) * 524288, voffW, lw + ((c) % 3) * 24576); glds16(gW + (size_t)(c) * 524288 + 262144, voffW, lw + ((c) % 3) * 24576 + 8192); \
                     glds16(gE + (size_t)(c) * 196608, voffE, lw + ((c) % 3) * 24576 + 16384); } while (0)
    {
        LANE_VARS();
        const int wr = wid >> 2, wc = wid & 3, fr = lane & 15, fq = lane >> 4;
        constexpr int HTB = 16384;
#define SA(b, h) (((b) * 2 + (h)) * HTB)
#define SB(b, h) ((4 + (b) * 2 + (h)) * HTB)
        unsigned voffA, voffB;
        { const int row = 8 * wid + (lane >> 3), ch = (lane & 7) ^ ((row >> 1) & 7); voffA = (unsigned)(row * 1024 + ch * 16); }
        { const int row = 4 * wid + (lane >> 4), j = lane & 15, s = ((row >> 3) & 1) * 4 + (row & 3), c = (j >> 1) ^ s; voffB = (unsigned)(row * 8192 + c * 32 + (j & 1) * 16); }
        const char* gA = (const char*)ckv + (size_t)jj * 256 * 1024;
        const char* gB0 = (const char*)wuk + hh_ * 256; const char* gB1 = (const char*)wuv + hh_ * 256;
        const unsigned ldsw = rfl(lds0 + wid * 1024);
#define STAGE_A(b, h, kt) do { const char* g_ = gA + (h) * 131072 + (kt) * 128; glds16(g_, voffA, ldsw + SA(b, h)); glds16(g_ + 65536, voffA, ldsw + SA(b, h) + 8192); } while (0)
#define STAGE_B(b, h, kt) do { const char* g_ = ((h) ? gB1 : gB0) + (size_t)(kt) * 524288; glds16(g_, voffB, ldsw + SB(b, h)); glds16(g_ + 262144, voffB, ldsw + SB(b, h) + 8192); } while (0)
        lds_cptr pa[2], pb[2];
#pragma unroll
        for (int ks = 0; ks < 2; ++ks) pa[ks] = lds + (wr * 64 + fr) * 128 + 16 * ((4 * ks + fq) ^ ((fr >> 1) & 7));
        { const int q = fr >> 2, p = fr & 3, s = (fq & 1) * 4 + q;
#pragma unroll
          for (int n = 0; n < 2; ++n) pb[n] = lds + (8 * fq + q) * 256 + 32 * ((2 * wc + n) ^ s) + 8 * p; }
#define LDA(dst, b, h) do { _Pragma("unroll") for (int m = 0; m < 4; ++m) _Pragma("unroll") for (int k = 0; k < 2; ++k) dst[m][k] = ldsr128(pa[k] + SA(b, h) + m * 2048); } while (0)
#define LDB(dst, b, h) do { _Pragma("unroll") for (int n = 0; n < 2; ++n) _Pragma("unroll") for (int k = 0; k < 2; ++k) \
        dst[n][k] = cat8(trrd(pb[n] + SB(b, h) + k * 8192), trrd(pb[n] + SB(b, h) + k * 8192 + 1024)); } while (0)
#define MMA(ai, bj, At, Bt) do { __builtin_amdgcn_s_setprio(1); _Pragma("unroll") for (int m = 0; m < 4; ++m) _Pragma("unroll") for (int n = 0; n < 2; ++n) _Pragma("unroll") for (int k = 0; k < 2; ++k) \
        acc[ai][bj][m][n] = (bj) ? MFMA16(At[m][k], Bt[n][k], acc[ai][bj][m][n]) : MFMA16(Bt[n][k], At[m][k], acc[ai][bj][m][n]); __builtin_amdgcn_s_setprio(0); } while (0)
        f32x4 acc[2][2][4][2] = {};
        bf16x8 At[4][2], B0[2][2], B1[2][2];
        constexpr int nt = 8;
        STAGE_B(0, 0, 0); STAGE_A(0, 0, 0); STAGE_B(0, 1, 0); STAGE_A(0, 1, 0);
        if (wr == 1) BAR();
        WAIT_V(4); BAR();
        STAGE_B(1, 0, 1); STAGE_A(1, 0, 1); STAGE_B(1, 1, 1);
        WAIT_V(6); BAR();
        STAMP();
        for (int t = 0; t < nt - 2; t += 2) {
            LDB(B0, 0, 0); SBAR(); LDA(At, 0, 0); STAGE_A(1, 1, t + 1);
            WAIT_L(8); BAR(); WAIT_L(0); MMA(0, 0, At, B0); BAR(); SBAR();
            LDB(B1, 0, 1); STAGE_B(0, 0, t + 2);
            BAR(); WAIT_L(0); MMA(0, 1, At, B1); BAR();
            LDA(At, 0, 1); STAGE_A(0, 0, t + 2);
            BAR(); WAIT_L(0); MMA(1, 0, At, B0); BAR(); SBAR();
            STAGE_B(0, 1, t + 2);
            WAIT_V(6); BAR(); MMA(1, 1, At, B1); BAR();
            LDB(B0, 1, 0); SBAR(); LDA(At, 1, 0); STAGE_A(0, 1, t + 2);
            WAIT_L(8); BAR(); WAIT_L(0); MMA(0, 0, At, B0); BAR(); SBAR();
            LDB(B1, 1, 1); STAGE_B(1, 0, t + 3);
            BAR(); WAIT_L(0); MMA(0, 1, At, B1); BAR();
            LDA(At, 1, 1); STAGE_A(1, 0, t + 3);
            BAR(); WAIT_L(0); MMA(1, 0, At, B0); BAR(); SBAR();
            STAGE_B(1, 1, t + 3);
            WAIT_V(6); BAR(); MMA(1, 1, At, B1); BAR();
        }
        { LDB(B0, 0, 0); WAIT_V(0); LDA(At, 0, 0); STAGE_A(1, 1, nt - 1);
          BAR(); WAIT_L(0); MMA(0, 0, At, B0); BAR();
          LDB(B1, 0, 1); BAR(); WAIT_L(0); MMA(0, 1, At, B1); BAR();
          LDA(At, 0, 1); BAR(); WAIT_L(0); MMA(1, 0, At, B0); MMA(1, 1, At, B1); BAR(); }
        { LDB(B0, 1, 0); LDA(At, 1, 0); BAR(); WAIT_L(0); MMA(0, 0, At, B0); BAR();
          LDB(B1, 1, 1); WAIT_V(0);
          WISS(0); XISS(0);
          BAR(); WAIT_L(0); MMA(0, 1, At, B1); BAR();
          LDA(At, 1, 1); BAR(); WAIT_L(0); MMA(1, 0, At, B0); MMA(1, 1, At, B1); BAR(); }
        if (wr == 0) BAR();
        WISS(1); XISS(1);
        SBAR();
#undef SA
#undef SB
#undef STAGE_A
#undef STAGE_B
#undef LDA
#undef LDB
#undef MMA
#pragma unroll
        for (int ai = 0; ai < 2; ++ai) {
#pragma unroll
            for (int m = 0; m < 4; ++m) { const f32x4 a = acc[ai][0][m][0], b = acc[ai][0][m][1];
                pkv[ai * 4 + m] = (u32x4){cvtpk(a[0], a[1]), cvtpk(a[2], a[3]), cvtpk(b[0], b[1]), cvtpk(b[2], b[3])}; }
#pragma unroll
            for (int u = 0; u < 2; ++u)
#pragma unroll
                for (int n = 0; n < 2; ++n) { const f32x4 a = acc[ai][1][2 * u][n], b = acc[ai][1][2 * u + 1][n];
                    pkv[8 + ai * 4 + u * 2 + n] = (u32x4){cvtpk(a[0], a[1]), cvtpk(a[2], a[3]), cvtpk(b[0], b[1]), cvtpk(b[2], b[3])}; }
        }
    }
    STAMP();
    const char* kvw = KVh + w4 * 4096;
    const unsigned ldw = lds0 + w4 * 4096;
    char* const qbp = ws + WS_QB + (size_t)(bid * 4 + w4) * 8192;
    char* const pqp = ws + WS_PQ + (size_t)(bid * 8 + wid) * 2048;
    bf16x8 qf[8]; float gate;
    {
        LANE_VARS();
        u16 wph[8];
#pragma unroll
        for (int j = 0; j < 8; ++j) wph[j] = wphi[hh_ * 256 + (8 * (j >> 2) + 4 * hi + (j & 3)) * 16 + (lane & 15)];
        const char* tb0 = KVh + (size_t)(4 * jj + G) * 32768;
        const unsigned vok = (unsigned)((4 * w4 + (lane >> 4)) * 1024 + (lane & 15) * 16), vov = (unsigned)(16384 + (lane >> 4) * 2048 + (w4 * 32 + (lane & 15)) * 16);
#define KVST(i) do { if ((i) < 8) ST_SC1(tb0 + ((i) >> 2) * 65536, vok, ((i) & 3) * 256, pkv[i]); \
                     else ST_SC1(tb0 + (((i) >> 2) & 1) * 65536 + (((i) >> 1) & 1) * 8192, vov, ((i) & 1) * 256, pkv[i]); } while (0)
        f32x16 qa[5] = {}, d1 = {};
        unsigned xo[2], wo_[4];
#pragma unroll
        for (int s = 0; s < 2; ++s) xo[s] = (unsigned)((32 * wid + r32) * 128 + 16 * ((2 * s + hi) ^ ((r32 >> 1) & 7)));
#pragma unroll
        for (int c = 0; c < 4; ++c) wo_[c] = (unsigned)(L_WE + (8 * hi + q4) * 256 + 64 * (c ^ q4) + 32 * (p4 >> 1) + 16 * blk + 8 * (p4 & 1));
        const unsigned eo = (unsigned)(L_WE + 16384 + (8 * hi + q4) * 128 + 64 * ((q4 >> 1) & 1) + 32 * blk + 8 * p4);
        const unsigned vo_ = (unsigned)(L_WE + 16384 + (8 * hi + q4) * 128 + 64 * (((q4 >> 1) & 1) ^ 1) + 8 * p4);
        bf16x8 fx[2], fw[2][6], fg[2];
#define QR_(buf, c, ks, u) do { \
        if ((u) == 0) { fx[buf] = ldsr128(lds + ((c) & 1) * 32768 + (xo[(ks) & 1] ^ (((ks) >> 1) << 6))); fg[buf] = ldsr128(lds + L_GATE + ((c) * 64 + (ks) * 16 + 8 * hi) * 2); } \
        else if ((u) < 5) { fw[buf][(u) - 1] = cat8(trrd(lds + wo_[(u) - 1] + ((c) % 3) * 24576 + (ks) * 4096), trrd(lds + wo_[(u) - 1] + ((c) % 3) * 24576 + (ks) * 4096 + 1024)); } \
        else { fw[buf][4] = cat8(trrd(lds + eo + ((c) % 3) * 24576 + (ks) * 2048), trrd(lds + eo + ((c) % 3) * 24576 + (ks) * 2048 + 512)); \
               fw[buf][5] = cat8(trrd(lds + vo_ + ((c) % 3) * 24576 + (ks) * 2048), trrd(lds + vo_ + ((c) % 3) * 24576 + (ks) * 2048 + 512)); } } while (0)
#define QM_(buf, u) do { if ((u) < 5) qa[u] = MFMA32(fw[buf][u], fx[buf], qa[u]); else d1 = MFMA32(fx[buf], blk ? fg[buf] : fw[buf][5], d1); } while (0)
#define QRD(buf, c, ks) do { _Pragma("unroll") for (int u_ = 0; u_ < 6; ++u_) QR_(buf, c, ks, u_); } while (0)
#define QSTEP(buf, c, ks, rd) do { _Pragma("unroll") for (int u_ = 0; u_ < 6; ++u_) { if (rd) QR_((buf) ^ 1, c, ks, u_); QM_(buf, u_); SBAR(); } } while (0)
#define WV_(n) asm volatile("s_waitcnt vmcnt(%0)" :: "i"(n) : "memory")
#define NST(c) ((c) < 0 ? 0 : (c) < 4 ? 3 : (c) < 6 ? 2 : 0)
#define ST0(c) ((c) < 4 ? 3 * (c) : 12 + 2 * ((c) - 4))
#define QCHUNK(c) do { \
        SBAR(); QSTEP(0, c, 1, true); QSTEP(1, c, 2, true); \
        if ((c) < 7) { WV_(4 + NST((c) - 1)); } BAR(); asm volatile("" ::: "memory"); \
        if ((c) == 7) { if (tid == 0) __hip_atomic_fetch_add(CNT + hh_ * 64, 1u, RLX_AGENT); } \
        if ((c) < 6) WISS((c) + 2); \
        _Pragma("unroll") for (int i_ = 0; i_ < NST(c); ++i_) KVST(ST0(c) + i_); \
        SBAR(); QSTEP(0, c, 3, true); \
        if ((c) < 7) { WV_(((c) < 6 ? 3 : 0) + NST(c)); } \
        SBAR(); QSTEP(1, (c) + 1, 0, (c) < 7); \
        if ((c) < 6) XISS((c) + 2); } while (0)
        asm volatile("s_waitcnt vmcnt(7)\n\ts_barrier" ::: "memory");
        bf16x8 wpf;
#pragma unroll
        for (int j = 0; j < 8; ++j) wpf[j] = (short)(blk ? (wph[j] ^ 0x8000) : wph[j]);
        PIN(wpf);
        STAMP();
        QRD(0, 0, 0);
        QCHUNK(0); QCHUNK(1); QCHUNK(2); QCHUNK(3); QCHUNK(4); QCHUNK(5); QCHUNK(6); QCHUNK(7);
#undef QRD
#undef QR_
#undef QM_
#undef QSTEP
#undef QCHUNK
#undef XISS
#undef WISS
#undef KVST
#undef NST
#undef ST0
        STAMP();
        BAR(); asm volatile("" ::: "memory");
        unsigned f1 = __hip_atomic_load(CNT + hh_ * 64, RLX_AGENT);
#pragma unroll
        for (int cc = 0; cc < 4; ++cc) {
#pragma unroll
            for (int r = 0; r < 16; ++r) qa[cc][r] *= QSCALE;
            qf[2 * cc] = PACK8(qa[cc], 0); qf[2 * cc + 1] = PACK8(qa[cc], 8);
        }
        const lds_ptr qp = lds + L_V + 16384 + (w4 >> 1) * 32768 + (w4 & 1) * 8192 + lane * 16;
        if (G == 0) {
#pragma unroll
            for (int s = 0; s < 8; ++s) *(LAS bf16x8*)(qp + s * 1024) = qf[s];
        }
        if (rfl(f1) < 8u) wait_cnt(CNT + hh_ * 64, 8u);
        asm volatile("" ::: "memory");
        STAMP();
#pragma unroll
        for (int e = 0; e < 8; ++e) glds16c(kvw + (size_t)(G + 2 * (e >> 2)) * 32768 + (e & 3) * 1024, vlin, ldw + G * 49152 + (e >> 2) * 16384 + (e & 3) * 1024);
        LAS float* red = (LAS float*)(lds + L_V + G * 32768 + w4 * 4096);
        if (r32 == 16) {
#pragma unroll
            for (int r = 0; r < 16; ++r) red[(r & 3) + 8 * (r >> 2) + 4 * hi] = d1[r];
        }
        WAIT_L(0);
        { const float gz = bfround(red[r32]); gate = bfround(1.f / (1.f + __expf(-gz))); }
        f32x16 pq = MFMA32(wpf, PACK8(qa[4], 0), f32x16{});
        f32x16 pk = MFMA32(wpf, PACK8(qa[4], 8), f32x16{});
        dual_softmax(pq); dual_softmax(pk);
        f32x16 d3;
        { bf16x8 id0, id1;
#pragma unroll
          for (int j = 0; j < 8; ++j) { const int f = 8 * (j >> 2) + 4 * hi + (j & 3); id0[j] = (short)(r32 == f ? 0x3F80 : 0); id1[j] = (short)(r32 == f + 16 ? 0x3F80 : 0); }
          d3 = MFMA32(PACK8(pk, 0), id0, f32x16{}); d3 = MFMA32(PACK8(pk, 8), id1, d3); }
        { const bf16x8 t0 = PACK8(pq, 0), t1 = PACK8(pq, 8); ST_PL(pqp, vlin, 0, t0); ST_PL(pqp, vlin, 1024, t1); }
        if (G) {
#pragma unroll
            for (int s = 0; s < 8; ++s) ST_PL(qbp + (s >> 2) * 4096, vlin, (s & 3) * 1024, qf[s]);
        }
        const bf16x8 ones = {0x3F80, 0x3F80, 0x3F80, 0x3F80, 0x3F80, 0x3F80, 0x3F80, 0x3F80};
        const bf16x8 v0 = PACK8(d1, 0), v1 = PACK8(d1, 8);
        f32x16 d4 = MFMA32(PACK8(d3, 0), blk ? ones : v0, f32x16{});
        d4 = MFMA32(PACK8(d3, 8), blk ? ones : v1, d4);
#pragma unroll
        for (int r = 0; r < 16; ++r) red[((r & 3) + 8 * (r >> 2) + 4 * hi) * 32 + r32] = d4[r];
        WAIT_L(0); BAR(); asm volatile("" ::: "memory");
        if (tid < 256) {
            f32x4 s = {0.f, 0.f, 0.f, 0.f};
#pragma unroll
            for (int w = 0; w < 8; ++w) s += *(const LAS f32x4*)(lds + L_V + (w >> 2) * 32768 + (w & 3) * 4096 + tid * 16);
            ST_SC1(ws + WS_KVP + (size_t)(hh_ * 8 + jj) * 4096, (unsigned)tid * 16u, 0, s);
        }
        if (G) {
#pragma unroll
            for (int s = 0; s < 8; ++s) qf[s] = ldsr128(qp + s * 1024);
        }
        if (G) { WAIT_BAR(10); } else { WAIT_BAR(3); }
    }
    STAMP();
    {
    LANE_VARS();
    const int prow = (r32 & 7) | ((r32 & 8) << 1) | ((r32 & 16) >> 1);
    const unsigned kro = (unsigned)(prow * 16 + hi * 1024), vro = (unsigned)(L_V + G * 32768 + r32 * 16 + hi * 2048);
    const int dq = 32 * w4 + r32 - 64 * G - 4 * hi;
    unsigned ks0 = G * 49152u, ks1 = ks0 + 16384u, ks2 = ks0 + 32768u;
    f32x16 o[4], negm, sA0, sA1, sB0, sB1; bf16x8 kf[16], vf[16]; u32x4 pw[4]; float m, l;
#define KRD(i, kp) kf[i] = ldsr128((kp) + ((i) >> 1) * 2048 + ((i) & 1) * 512)
#define VRD(i, vp) vf[i] = ldsr128((vp) + ((i) >> 2) * 4096 + ((i) & 3) * 512)
#define PF(k) __builtin_bit_cast(bf16x8, pw[k])
#define DMA1(i) do { if ((i) < 4) glds16c(gk_ + (i) * 1024, vlin, dk_ + (i) * 1024); else glds16c(gv_ + ((i) - 4) * 1024, vlin, dv_ + ((i) - 4) * 1024); } while (0)
#define ROT() do { const unsigned t_ = ks0; ks0 = ks1; ks1 = ks2; ks2 = t_; } while (0)
#define DMA_SETUP(r) const int tk_ = (r) + 2 <= R ? (r) + 2 : (pass == 0 ? (r) + 1 - R : 0); \
        const char* gk_ = kvw + (size_t)(2 * tk_ + G) * 32768; const char* gv_ = kvw + (size_t)(2 * (r) + G) * 32768 + 16384; \
        const unsigned dk_ = ldw + ks2, dv_ = ldw + L_V + G * 32768 + ((r) & 1) * 16384;
#define GAPA(i, C0, C1, P0, P1) do { \
        if ((i) + 4 < 16) { KRD((i) + 4, kp_); } else { VRD((i) - 12, vp_); } \
        if (((i) & 1) == 0) DMA1((i) >> 1); \
        if ((i) & 1) C1 = MFMA32(kf[i], qf[(i) >> 1], (i) < 2 ? negm : C1); else C0 = MFMA32(kf[i], qf[(i) >> 1], (i) < 2 ? negm : C0); \
        if ((i) < 8) { P1[2 * (i)] = EX2(P1[2 * (i)]); P1[2 * (i) + 1] = EX2(P1[2 * (i) + 1]); pw[(i) >> 2][(i) & 3] = cvtpk(P0[2 * (i)], P0[2 * (i) + 1]); PIN(P1); PIN(pw[(i) >> 2]); } \
        else { l += P1[2 * (i) - 16]; l2 += P1[2 * (i) - 15]; pw[(i) >> 2][(i) & 3] = cvtpk(P1[2 * (i) - 16], P1[2 * (i) - 15]); PIN(l); PIN(l2); PIN(pw[(i) >> 2]); } \
        SBAR(); } while (0)
#define PVM(i) o[(i) & 3] = MFMA32(vf[i], PF((i) >> 2), o[(i) & 3])
#define RDB(i) do { if ((i) + 4 < 16) { VRD((i) + 4, vp_); } else { KRD((i) - 12, kn_); } } while (0)
#define ROUND(C0, C1, P0, P1, r) do { \
        const lds_cptr kp_ = lds + ks0 + kro; const lds_cptr kn_ = lds + ks1 + kro; const lds_cptr vp_ = lds + vro + (((r) - 1) & 1) * 16384; \
        DMA_SETUP(r) float l2 = 0.f; SBAR(); \
        GAPA(0, C0, C1, P0, P1); GAPA(1, C0, C1, P0, P1); GAPA(2, C0, C1, P0, P1); GAPA(3, C0, C1, P0, P1); \
        GAPA(4, C0, C1, P0, P1); GAPA(5, C0, C1, P0, P1); GAPA(6, C0, C1, P0, P1); GAPA(7, C0, C1, P0, P1); \
        GAPA(8, C0, C1, P0, P1); GAPA(9, C0, C1, P0, P1); GAPA(10, C0, C1, P0, P1); GAPA(11, C0, C1, P0, P1); \
        GAPA(12, C0, C1, P0, P1); GAPA(13, C0, C1, P0, P1); GAPA(14, C0, C1, P0, P1); GAPA(15, C0, C1, P0, P1); \
        l += l2; \
        RDB(0); PVM(0); SBAR(); RDB(1); PVM(1); SBAR(); \
        if ((r) == R) mask_tile(C0, C1, dq); \
        float ma, mb; \
        RDB(2); PVM(2); ma = MX3(C0[0], C0[1], C1[0]); mb = MX3(C0[2], C0[3], C1[1]); ma = MX3(ma, C1[2], C1[3]); mb = MX3(mb, C0[4], C0[5]); PIN(ma); PIN(mb); SBAR(); \
        RDB(3); PVM(3); ma = MX3(ma, C0[6], C0[7]); mb = MX3(mb, C1[4], C1[5]); ma = MX3(ma, C1[6], C1[7]); mb = MX3(mb, C0[8], C0[9]); PIN(ma); PIN(mb); SBAR(); \
        RDB(4); PVM(4); ma = MX3(ma, C0[10], C0[11]); mb = MX3(mb, C1[8], C1[9]); ma = MX3(ma, C1[10], C1[11]); mb = MX3(mb, C0[12], C0[13]); PIN(ma); PIN(mb); SBAR(); \
        RDB(5); PVM(5); ma = MX3(ma, C0[14], C0[15]); mb = MX3(mb, C1[12], C1[13]); ma = MX3(ma, C1[14], C1[15]); \
        const float rm = swap32_max(fmaxf(ma, mb)); bool pend = false; float fsc = 1.f; \
        if (__builtin_expect(__any(rm > THR), 0)) { const float dl = fmaxf(rm, 0.f); m += dl; fsc = EX2(-dl); l *= fsc; pend = true; \
            _Pragma("unroll") for (int e = 0; e < 16; ++e) { C0[e] -= dl; C1[e] -= dl; negm[e] = -m; } } \
        SBAR(); \
        RDB(6); PVM(6); C0[0] = EX2(C0[0]); C0[1] = EX2(C0[1]); PIN(C0); SBAR(); \
        RDB(7); PVM(7); C0[2] = EX2(C0[2]); C0[3] = EX2(C0[3]); l += C0[0]; l2 = C0[1]; PIN(C0); PIN(l); SBAR(); \
        RDB(8); PVM(8); C0[4] = EX2(C0[4]); C0[5] = EX2(C0[5]); l += C0[2]; l2 += C0[3]; PIN(C0); PIN(l); PIN(l2); SBAR(); \
        RDB(9); PVM(9); C0[6] = EX2(C0[6]); C0[7] = EX2(C0[7]); l += C0[4]; l2 += C0[5]; PIN(C0); PIN(l); PIN(l2); SBAR(); \
        RDB(10); PVM(10); C0[8] = EX2(C0[8]); C0[9] = EX2(C0[9]); l += C0[6]; l2 += C0[7]; PIN(C0); PIN(l); PIN(l2); SBAR(); \
        RDB(11); PVM(11); C0[10] = EX2(C0[10]); C0[11] = EX2(C0[11]); l += C0[8]; l2 += C0[9]; PIN(C0); PIN(l); PIN(l2); SBAR(); \
        RDB(12); PVM(12); C0[12] = EX2(C0[12]); C0[13] = EX2(C0[13]); l += C0[10]; l2 += C0[11]; PIN(C0); PIN(l); PIN(l2); SBAR(); \
        RDB(13); PVM(13); C0[14] = EX2(C0[14]); C0[15] = EX2(C0[15]); l += C0[12]; l2 += C0[13]; PIN(C0); PIN(l); PIN(l2); SBAR(); \
        RDB(14); PVM(14); l += C0[14]; l2 += C0[15]; PIN(l); PIN(l2); SBAR(); \
        RDB(15); PVM(15); l += l2; PIN(l); SBAR(); \
        if (__builtin_expect(pend, 0)) { _Pragma("unroll") for (int c = 0; c < 4; ++c) _Pragma("unroll") for (int e = 0; e < 16; ++e) o[c][e] *= fsc; } \
        asm volatile("s_waitcnt vmcnt(0)\n\ts_barrier" ::: "memory"); ROT(); } while (0)

    for (int pass = 0; pass < 2; ++pass) {
        const int R = pass ? bB : bA;
        {
            const lds_cptr kp_ = lds + ks0 + kro; const lds_cptr kn_ = lds + ks1 + kro;
            DMA_SETUP(0)
#pragma unroll
            for (int i = 0; i < 6; ++i) KRD(i, kp_);
#pragma unroll
            for (int i = 0; i < 8; ++i) DMA1(i);
#pragma unroll
            for (int c = 0; c < 4; ++c) o[c] = f32x16{};
            SBAR();
#pragma unroll
            for (int i = 0; i < 16; ++i) {
                if (i + 6 < 16) KRD(i + 6, kp_);
                if (i & 1) sA1 = MFMA32(kf[i], qf[i >> 1], i < 2 ? f32x16{} : sA1); else sA0 = MFMA32(kf[i], qf[i >> 1], i < 2 ? f32x16{} : sA0);
                SBAR();
            }
            if (R == 0) mask_tile(sA0, sA1, dq);
            float ma = MX3(sA0[0], sA0[1], sA1[0]), mb = MX3(sA0[2], sA0[3], sA1[1]); ma = MX3(ma, sA1[2], sA1[3]);
#pragma unroll
            for (int r = 4; r < 16; r += 4) { ma = MX3(ma, sA0[r], sA0[r + 1]); mb = MX3(mb, sA0[r + 2], sA0[r + 3]); ma = MX3(ma, sA1[r], sA1[r + 1]); mb = MX3(mb, sA1[r + 2], sA1[r + 3]); }
            m = fmaxf(swap32_max(fmaxf(ma, mb)), -1e30f); l = 0.f;
#pragma unroll
            for (int i = 0; i < 4; ++i) KRD(i, kn_);
#pragma unroll
            for (int e = 0; e < 16; ++e) { sA0[e] = EX2(sA0[e] - m); l += sA0[e]; sA1[e] -= m; negm[e] = -m; }
            asm volatile("s_waitcnt vmcnt(0)\n\ts_barrier" ::: "memory"); ROT();
            if (tid == 0) { if (pass == 0) __hip_atomic_fetch_add(CNT + (32 + hh_) * 64, 1u, RLX_AGENT);
                else { __hip_atomic_fetch_or(CNT + (64 + bA) * 64, 1u << hh_, RLX_AGENT);
                       __hip_atomic_fetch_or(CNT + (64 + bA) * 64 + 1, 1u << hh_, RLX_AGENT); __hip_atomic_fetch_or(CNT + (64 + bB) * 64 + 1, 1u << hh_, RLX_AGENT); } }
        }
        STAMP();
        int r = 1;
        for (; r + 1 <= R; r += 2) { ROUND(sB0, sB1, sA0, sA1, r); ROUND(sA0, sA1, sB0, sB1, r + 1); }
        if (r <= R) { ROUND(sB0, sB1, sA0, sA1, r); sA0 = sB0; sA1 = sB1; }
        STAMP();
        f32x2 ps[8]; bf16x8 phq[2];
        {
        LANE_VARS();
        {
            const lds_cptr vp_ = lds + vro + (R & 1) * 16384;
#pragma unroll
            for (int i = 0; i < 8; ++i) VRD(i, vp_);
            const unsigned f2 = __hip_atomic_load(CNT + (32 + hh_) * 64, RLX_AGENT);
#pragma unroll
            for (int i = 0; i < 8; ++i) pw[i >> 2][i & 3] = cvtpk(sA0[2 * i], sA0[2 * i + 1]);
            SBAR();
#pragma unroll
            for (int i = 0; i < 16; ++i) {
                if (i + 8 < 16) VRD(i + 8, vp_);
                if (i == 8) {
                    if (rfl(f2) < 8u) wait_cnt(CNT + (32 + hh_) * 64, 8u);
#pragma unroll
                    for (int w = 0; w < 8; ++w) ps[w] = __builtin_bit_cast(f32x2, __hip_atomic_load((const unsigned long long*)(ws + WS_KVP + (size_t)(hh_ * 8) * 4096 + (unsigned)(w * 4096 + tid * 8)), RLX_AGENT));
#pragma unroll
                    for (int s = 0; s < 8; ++s) qf[s] = *(const bf16x8*)(qbp + (vlin + s * 1024));
                }
                PVM(i);
                if (i < 8) { sA1[2 * i] = EX2(sA1[2 * i]); sA1[2 * i + 1] = EX2(sA1[2 * i + 1]); PIN(sA1); }
                if (i >= 1 && i < 9) { l += sA1[2 * i - 2]; l += sA1[2 * i - 1]; pw[2 + ((i - 1) >> 2)][(i - 1) & 3] = cvtpk(sA1[2 * i - 2], sA1[2 * i - 1]); PIN(l); PIN(pw[2 + ((i - 1) >> 2)]); }
                SBAR(); }
        }
        phq[0] = *(const bf16x8*)(pqp + vlin); phq[1] = *(const bf16x8*)(pqp + (vlin + 1024));
        l = swap32_add(l);
        BAR(); asm volatile("" ::: "memory");
        const unsigned st0 = G ? ks2 - 49152u : ks2;
        { const lds_ptr xp = lds + L_V + wid * 8192 + lane * 16;
#pragma unroll
          for (int c = 0; c < 2; ++c)
#pragma unroll
              for (int g = 0; g < 4; ++g) { f32x4 t;
#pragma unroll
                  for (int e = 0; e < 4; ++e) t[e] = G ? o[c][4 * g + e] : o[2 + c][4 * g + e];
                  *(LAS f32x4*)(xp + (c * 4 + g) * 1024) = t; }
          *(LAS f32x4*)(lds + st0 + wid * 1024 + lane * 16) = (f32x4){m, l, gate, 0.f};
          if (pass == 0) { f32x2 s = ps[0];
#pragma unroll
              for (int w = 1; w < 8; ++w) s += ps[w];
              *(LAS f32x2*)(lds + st0 + 8192 + tid * 8) = s; } }
        WAIT_L(0); BAR(); asm volatile("" ::: "memory");
        { const int ow = wid ^ 4;
          const f32x4 st = *(const LAS f32x4*)(lds + st0 + ow * 1024 + lane * 16);
          const float mn = fmaxf(m, st[0]), f0 = EX2(m - mn), f1 = EX2(st[0] - mn);
          const float gg = (G == pass) ? gate : st[2];
          const float inv = gg / (l * f0 + st[1] * f1); const float c0 = f0 * inv, c1 = f1 * inv;
          const lds_cptr xp = lds + L_V + ow * 8192 + lane * 16;
          const char* dst = A3 + (size_t)(R * 576 + hh_ * 16 + G * 8) * 2048;
          const unsigned vod = (unsigned)(hi * 2048 + (32 * w4 + r32) * 16);
#pragma unroll
          for (int c = 0; c < 2; ++c) {
              float v[16];
#pragma unroll
              for (int g = 0; g < 4; ++g) { const f32x4 t = *(const LAS f32x4*)(xp + (c * 4 + g) * 1024);
#pragma unroll
                  for (int e = 0; e < 4; ++e) v[4 * g + e] = (G ? o[2 + c][4 * g + e] : o[c][4 * g + e]) * c0 + t[e] * c1; }
              { const bf16x8 t0 = PACK8(v, 0), t1 = PACK8(v, 8); ST_SC1(dst + c * 8192, vod, 0, t0); ST_SC1(dst + c * 8192 + 4096, vod, 0, t1); }
          }
          if (pass == 0) {
              float kvv[2][8];
              const int col = r32 < 16 ? r32 : 16;
#pragma unroll
              for (int s = 0; s < 2; ++s)
#pragma unroll
                  for (int j = 0; j < 8; ++j) { const int f = 16 * s + 8 * (j >> 2) + 4 * hi + (j & 3); const float v = *(const LAS float*)(lds + st0 + 8192 + (f * 32 + col) * 4); kvv[s][j] = r32 <= 16 ? v : 0.f; }
              f32x16 nm = MFMA32(PACK8(kvv[0], 0), phq[0], f32x16{});
              nm = MFMA32(PACK8(kvv[1], 0), phq[1], nm);
              const float den = bfround(bfround(lo32_bcast(nm[8])) + 1e-6f); const float sc = (1.f - gate) / den;
              float ov[8];
#pragma unroll
              for (int r = 0; r < 8; ++r) ov[r] = bfround(nm[r]) * sc;
              const bf16x8 t0 = PACK8(ov, 0);
              ST_SC1(A3 + (size_t)((G ? bB : bA) * 576 + 512 + 2 * hh_) * 2048, vod, 0, t0);
          } }
        WAIT_L(0); BAR(); asm volatile("" ::: "memory");
        }
        STAMP();
    }
#undef KRD
#undef VRD
#undef PF
#undef DMA1
#undef ROT
#undef DMA_SETUP
#undef GAPA
#undef PVM
#undef RDB
#undef ROUND
    }
    {
        LANE_VARS();
        const int rt = 4 * (bid & 7) + (bid >> 6), ct = (bid >> 3) & 7;
        unsigned voffW; { const int row = lane >> 3, sl = lane & 7, nb = (sl >> 2) ^ ((row >> 1) & 1); voffW = (unsigned)(row * 1024 + nb * 64 + (sl & 3) * 16); }
        const char* gA = A3 + (size_t)((rt >> 1) * 576 + 2 * wid) * 2048 + (rt & 1) * 1024;
        const char* gWa = (const char*)wo_a + (size_t)wid * 16384 + ct * 128; const char* gWb = (const char*)wo_b + (size_t)wid * 16384 + ct * 128;
        const unsigned wb = lds0 + wid * 20480;
        const unsigned* rdy = CNT + (64 + (rt >> 1)) * 64;
        auto issue = [&](int u, unsigned slot) {
            const char* ga = gA + (size_t)u * 32768; const char* gw = u < 32 ? gWa + (size_t)u * 131072 : gWb + (size_t)(u - 32) * 131072;
            glds16c(ga, vlin, wb + slot); glds16c(ga + 2048, vlin, wb + slot + 1024); glds16(gw, voffW, wb + slot + 2048); glds16(gw + 8192, voffW, wb + slot + 3072); };
        unsigned avail = 0u, seen = 0u;
        auto next_head = [&]() -> int {
            if (avail == 0u) { unsigned spins = 0;
                for (;;) { const unsigned mk = rfl(__hip_atomic_load(rdy, RLX_AGENT)); avail = mk & ~seen; seen |= mk; if (avail != 0u || ++spins > (1u << 20)) break; __builtin_amdgcn_s_sleep(2); }
                if (avail == 0u) { avail = ~seen; seen = ~0u; }
                asm volatile("" ::: "memory"); }
            const int h = __builtin_ctz(avail); avail &= avail - 1u; return h; };
        { unsigned spins = 0; while (rfl(__hip_atomic_load(rdy + 1, RLX_AGENT)) != ~0u) { __builtin_amdgcn_s_sleep(2); if (++spins > (1u << 20)) break; } }
        asm volatile("" ::: "memory");
#pragma unroll
        for (int i = 0; i < 4; ++i) issue(32 + i, i * 4096u);
        asm volatile("s_waitcnt vmcnt(16)\n\ts_barrier" ::: "memory");
        if (tid == 0) __hip_atomic_fetch_or(CNT + (64 + bB) * 64, 1u << hh_, RLX_AGENT);
        STAMP();
        issue(next_head(), 16384u);
        f32x16 acc[2][2] = {};
        const unsigned ao = (unsigned)(wid * 20480 + hi * 1024 + r32 * 16);
        unsigned bo[2];
#pragma unroll
        for (int nb = 0; nb < 2; ++nb) bo[nb] = (unsigned)(wid * 20480 + 2048 + (4 * hi + q4) * 128 + 64 * (nb ^ ((q4 >> 1) & 1)) + 32 * blk + 8 * p4);
        bf16x8 af[2][2], bf[2][2];
#define K3RD(buf, sb) do { af[buf][0] = ldsr128((sb) + ao); bf[buf][0] = cat8(trrd((sb) + bo[0]), trrd((sb) + bo[0] + 1024)); \
        af[buf][1] = ldsr128((sb) + ao + 512); bf[buf][1] = cat8(trrd((sb) + bo[1]), trrd((sb) + bo[1] + 1024)); } while (0)
#define K3BODY(buf, i) do { \
        const unsigned snx = sc == 16384u ? 0u : sc + 4096u; \
        if ((i) + 1 < 36) K3RD(buf ^ 1, lds + snx); \
        SBAR(); acc[0][0] = MFMA32(bf[buf][0], af[buf][0], acc[0][0]); acc[1][0] = MFMA32(bf[buf][0], af[buf][1], acc[1][0]); SBAR(); \
        if ((i) + 5 < 36) issue(next_head(), sc); \
        SBAR(); acc[0][1] = MFMA32(bf[buf][1], af[buf][0], acc[0][1]); acc[1][1] = MFMA32(bf[buf][1], af[buf][1], acc[1][1]); SBAR(); \
        sc = snx; } while (0)
#define K3W(n) asm volatile("s_waitcnt vmcnt(" #n ") lgkmcnt(0)" ::: "memory")
        unsigned sc = 0u;
        WAIT_V(16);
        K3RD(0, lds);
        for (int i = 0; i < 32; i += 2) {
            if (G) __builtin_amdgcn_s_setprio(1); K3W(12); K3BODY(0, i);
            if (G) __builtin_amdgcn_s_setprio(0); K3W(12); K3BODY(1, i + 1); }
        K3W(8); K3BODY(0, 32); K3W(4); K3BODY(1, 33); K3W(0); K3BODY(0, 34); K3W(0); K3BODY(1, 35);
#undef K3RD
#undef K3BODY
#undef K3W
        STAMP();
        BAR(); asm volatile("" ::: "memory");
#pragma unroll
        for (int mi = 0; mi < 2; ++mi)
#pragma unroll
            for (int nb = 0; nb < 2; ++nb)
#pragma unroll
                for (int g = 0; g < 4; ++g) { const int row = 32 * mi + r32, ch = 8 * nb + 2 * g + hi;
                    *(LAS f32x4*)(lds + wid * 16384 + row * 256 + 16 * (ch ^ (row & 15))) = (f32x4){acc[mi][nb][4 * g], acc[mi][nb][4 * g + 1], acc[mi][nb][4 * g + 2], acc[mi][nb][4 * g + 3]}; }
        WAIT_L(0); BAR(); asm volatile("" ::: "memory");
        { const int row = tid >> 3, c8 = tid & 7; f32x4 s0 = {}, s1 = {};
#pragma unroll
          for (int w = 0; w < 8; ++w) { s0 += *(const LAS f32x4*)(lds + w * 16384 + row * 256 + 16 * ((2 * c8) ^ (row & 15)));
                                        s1 += *(const LAS f32x4*)(lds + w * 16384 + row * 256 + 16 * ((2 * c8 + 1) ^ (row & 15))); }
          u32x4 w = {cvtpk(s0[0], s0[1]), cvtpk(s0[2], s0[3]), cvtpk(s1[0], s1[1]), cvtpk(s1[2], s1[3])};
          *(u32x4*)(out + (size_t)(rt * 64 + row) * 512 + ct * 64 + c8 * 8) = w; }
    }
    STAMP(); STAMP_RT();
    STAMP_OUT(ws + WS_END);
}

extern "C" void kernel_launch(void* const* d_in, const int* in_sizes, int n_in, void* d_out, int out_size, void* d_ws, size_t ws_size,
                              hipStream_t stream) {
    (void)in_sizes; (void)n_in; (void)out_size; (void)ws_size;
    char* ws = (char*)d_ws;
    (void)hipFuncSetAttribute((const void*)kf_all, hipFuncAttributeMaxDynamicSharedMemorySize, LDS_BYTES);
    (void)hipMemsetAsync(ws + WS_CNT, 0, CNT_BYTES, stream);
    kf_all<<<256, 512, LDS_BYTES, stream>>>((const u16*)d_in[0], (const u16*)d_in[1], (const u16*)d_in[2], (const u16*)d_in[3], (const u16*)d_in[4], (const u16*)d_in[5],
                                            (const u16*)d_in[7], (const u16*)d_in[8], (const u16*)d_in[9], (const u16*)d_in[10], ws, (u16*)d_out);
}
```

```cpp
#include <hip/hip_runtime.h>
#include <hip/hip_bf16.h>
#include <stdint.h>
#define PREFETCH 1

typedef unsigned short u16;
typedef short bf16x8 __attribute__((ext_vector_type(8)));
typedef short s16x4 __attribute__((ext_vector_type(4)));
typedef float f32x2 __attribute__((ext_vector_type(2)));
typedef float f32x4 __attribute__((ext_vector_type(4)));
typedef float f32x16 __attribute__((ext_vector_type(16)));
typedef unsigned u32x4 __attribute__((ext_vector_type(4)));
typedef unsigned u32x2 __attribute__((ext_vector_type(2)));
typedef __bf16 bf16x2_t __attribute__((ext_vector_type(2)));
#define LAS __attribute__((address_space(3)))
#define DI __device__ __forceinline__
typedef LAS const char* lds_cptr;
typedef LAS char* lds_ptr;

constexpr int SEQ = 2048, DM = 512, NH = 32, HD = 128;
constexpr size_t WS_CNT = 0;
constexpr size_t CNT_BYTES = 80 * 256;
constexpr size_t WS_KV = 65536;
constexpr size_t WS_A3 = WS_KV + (size_t)NH * 32 * 32768;
constexpr size_t WS_KVP = WS_A3 + (size_t)576 * SEQ * 16;
constexpr size_t WS_QB = WS_KVP + (size_t)NH * 8 * 4096;
constexpr size_t WS_PQ = WS_QB + (size_t)256 * 32768;
constexpr size_t WS_END = WS_PQ + (size_t)256 * 16384;
constexpr int LDS_BYTES = 163840;

#define MFMA32(a, b, c) __builtin_amdgcn_mfma_f32_32x32x16_bf16((a), (b), (c), 0, 0, 0)
#define MFMA16(a, b, c) __builtin_amdgcn_mfma_f32_16x16x32_bf16((a), (b), (c), 0, 0, 0)
#define SBAR() __builtin_amdgcn_sched_barrier(0)
#define WAIT_V(n) asm volatile("s_waitcnt vmcnt(" #n ")" ::: "memory")
#define WAIT_L(n) asm volatile("s_waitcnt lgkmcnt(" #n ")" ::: "memory")
#define BAR() __builtin_amdgcn_s_barrier()
#define WAIT_BAR(N) asm volatile("s_waitcnt vmcnt(" #N ") lgkmcnt(0)\n\ts_barrier" ::: "memory")

#define STAMP_DECL()
#define STAMP()
#define STAMP_RT()
#define STAMP_OUT(base)
#define STAMP_PARAM
#define STAMP_ARG
DI unsigned rfl(unsigned x) { return __builtin_amdgcn_readfirstlane(x); }
DI unsigned cvtpk(float lo, float hi) { f32x2 v = {lo, hi}; bf16x2_t b = __builtin_convertvector(v, bf16x2_t); return __builtin_bit_cast(unsigned, b); }
DI float bfround(float x) { return __uint_as_float(cvtpk(x, x) << 16); }
DI void glds16(const void* sbase, unsigned voff, unsigned lds_dst) {
    unsigned sv;
    asm volatile("s_mov_b32 %0, m0\n\ts_mov_b32 m0, %3\n\ts_nop 2\n\tglobal_load_lds_dwordx4 %1, %2\n\ts_mov_b32 m0, %0"
                 : "=&s"(sv) : "v"(voff), "s"(sbase), "s"(lds_dst) : "memory");
}
DI void glds16c(const void* sbase, unsigned voff, unsigned lds_dst) {
    unsigned sv;
    asm volatile("s_mov_b32 %0, m0\n\ts_mov_b32 m0, %3\n\ts_nop 2\n\tglobal_load_lds_dwordx4 %1, %2 sc1\n\ts_mov_b32 m0, %0"
                 : "=&s"(sv) : "v"(voff), "s"(sbase), "s"(lds_dst) : "memory");
}
DI void touch8k(const void* sbase, unsigned voff, unsigned lds_dst) {
    unsigned sv;
    asm volatile("s_mov_b32 %0, m0\n\ts_mov_b32 m0, %3\n\ts_nop 2\n\tglobal_load_lds_dword %1, %2\n\ts_mov_b32 m0, %0"
                 : "=&s"(sv) : "v"(voff), "s"(sbase), "s"(lds_dst) : "memory");
}
typedef short v4i16_t __attribute__((ext_vector_type(4)));
DI s16x4 trrd(lds_cptr p) { return __builtin_bit_cast(s16x4, __builtin_amdgcn_ds_read_tr16_b64_v4i16((LAS v4i16_t*)p)); }
DI bf16x8 cat8(s16x4 a, s16x4 b) { return __builtin_shufflevector(a, b, 0, 1, 2, 3, 4, 5, 6, 7); }
DI bf16x8 ldsr128(lds_cptr p) { return *(const LAS bf16x8*)p; }
DI bf16x8 pack8(float a0, float a1, float a2, float a3, float a4, float a5, float a6, float a7) {
    u32x4 w = {cvtpk(a0, a1), cvtpk(a2, a3), cvtpk(a4, a5), cvtpk(a6, a7)}; return __builtin_bit_cast(bf16x8, w); }
#define PACK8(X, B) pack8((X)[(B)], (X)[(B) + 1], (X)[(B) + 2], (X)[(B) + 3], (X)[(B) + 4], (X)[(B) + 5], (X)[(B) + 6], (X)[(B) + 7])
DI float swap32_max(float v) { auto rr = __builtin_amdgcn_permlane32_swap(__float_as_uint(v), __float_as_uint(v), false, false); return fmaxf(__uint_as_float(rr[0]), __uint_as_float(rr[1])); }
DI float swap32_add(float v) { auto rr = __builtin_amdgcn_permlane32_swap(__float_as_uint(v), __float_as_uint(v), false, false); return __uint_as_float(rr[0]) + __uint_as_float(rr[1]); }
DI float lo32_bcast(float v) { auto rr = __builtin_amdgcn_permlane32_swap(__float_as_uint(v), __float_as_uint(v), false, false); return __uint_as_float(rr[0]); }
template <int CTRL> DI float dpp(float v) { return __uint_as_float(__builtin_amdgcn_update_dpp(0u, __float_as_uint(v), CTRL, 0xF, 0xF, true)); }
DI float row16_max(float v) { v = fmaxf(v, dpp<0xB1>(v)); v = fmaxf(v, dpp<0x4E>(v)); v = fmaxf(v, dpp<0x141>(v)); v = fmaxf(v, dpp<0x140>(v)); return v; }
DI float row16_add(float v) { v += dpp<0xB1>(v); v += dpp<0x4E>(v); v += dpp<0x141>(v); v += dpp<0x140>(v); return v; }

constexpr float QSCALE = 0.08838834764831845f * 1.4426950408889634f;
constexpr float THR = 8.f;
constexpr int L_V = 98304;
constexpr int L_WE = 65536, L_GATE = 139264;
#define PIN(x) asm volatile("" : "+v"(x))
#define MX3(a, b, c) __builtin_fmaxf(__builtin_fmaxf((a), (b)), (c))
#define EX2(v) __builtin_amdgcn_exp2f(v)
#define RLX_AGENT __ATOMIC_RELAXED, __HIP_MEMORY_SCOPE_AGENT
#define ST_PL(base, voff, imm, v) asm volatile("s_nop 4\n\tglobal_store_dwordx4 %0, %1, %2 offset:%3\n\ts_nop 1" :: "v"(voff), "v"(v), "s"(base), "i"(imm) : "memory")
#define ST_SC1(base, voff, imm, v) asm volatile("s_nop 4\n\tglobal_store_dwordx4 %0, %1, %2 offset:%3 sc1\n\ts_nop 1" :: "v"(voff), "v"(v), "s"(base), "i"(imm) : "memory")
DI void wait_cnt(const unsigned* p, unsigned want) {
    unsigned spins = 0;
    while (rfl(__hip_atomic_load(p, RLX_AGENT)) < want) { __builtin_amdgcn_s_sleep(2); if (++spins > (1u << 20)) break; }
}
DI void dual_softmax(f32x16& p) {
#pragma unroll
    for (int hf = 0; hf < 2; ++hf) {
        float mx = -INFINITY;
#pragma unroll
        for (int r = 0; r < 8; ++r) { p[8 * hf + r] = bfround(p[8 * hf + r]); mx = fmaxf(mx, p[8 * hf + r]); }
        mx = swap32_max(mx); float s = 0.f;
#pragma unroll
        for (int r = 0; r < 8; ++r) { p[8 * hf + r] = EX2((p[8 * hf + r] - mx) * 1.4426950408889634f); s += p[8 * hf + r]; }
        s = __builtin_amdgcn_rcpf(swap32_add(s));
#pragma unroll
        for (int r = 0; r < 8; ++r) p[8 * hf + r] *= s;
    }
}
DI void mask_tile(f32x16& s0, f32x16& s1, int dq) {
    asm volatile("" : "+v"(dq));
#pragma unroll
    for (int r = 0; r < 16; ++r) { const int kk = 16 * ((r >> 2) & 1) + 8 * (r >> 3) + (r & 3);
        if (kk > dq) s0[r] = -INFINITY; if (kk + 32 > dq) s1[r] = -INFINITY; }
}

__global__ __launch_bounds__(512, 2) void kf_all(const u16* __restrict__ x, const u16* __restrict__ ckv, const u16* __restrict__ wq, const u16* __restrict__ wuk,
                                                 const u16* __restrict__ wuv, const u16* __restrict__ wo_a, const u16* __restrict__ wqkv,
                                                 const u16* __restrict__ wphi, const u16* __restrict__ wo_b, const u16* __restrict__ gatew,
                                                 char* ws, u16* __restrict__ out) {
    extern __shared__ __attribute__((aligned(16))) char shm[];
    const lds_ptr lds = (lds_ptr)shm; const unsigned lds0 = (unsigned)(uintptr_t)shm;
    const int wid = rfl(threadIdx.x >> 6), G = wid >> 2, w4 = wid & 3;
    const int bid = blockIdx.x, hh_ = 4 * (bid & 7) + (bid >> 6), jj = (bid >> 3) & 7;
#define LANE_VARS() int tid = threadIdx.x; asm volatile("" : "+v"(tid)); const int lane = tid & 63, r32 = lane & 31, hi = lane >> 5, blk = (lane >> 4) & 1, \
        q4 = (lane & 15) >> 2, p4 = lane & 3; const unsigned vlin = (unsigned)lane * 16u; (void)r32; (void)hi; (void)blk; (void)q4; (void)p4; (void)vlin
    unsigned* const CNT = (unsigned*)(ws + WS_CNT);
    char* const KVh = ws + WS_KV + (size_t)hh_ * 32 * 32768;
    char* const A3 = ws + WS_A3;
    STAMP_DECL(); STAMP_RT(); STAMP();
    u32x4 pkv[16];
    const int bA = 15 - jj, bB = jj;
    const char* gX = (const char*)x + (size_t)((G ? bB : bA) * 128 + 32 * w4) * 1024; const char* gW = (const char*)wq + hh_ * 256; const char* gE = (const char*)wqkv + hh_ * 32;
    const unsigned lx = lds0 + wid * 4096, lw = lds0 + L_WE + wid * 1024;
    unsigned voffX, voffW, voffE;
    { const int lane = threadIdx.x & 63;
        { const int row = lane >> 3, ch = (lane & 7) ^ ((row >> 1) & 3); voffX = (unsigned)(row * 1024 + ch * 16); }
        { const int row = 4 * wid + (lane >> 4), j = lane & 15, c = (j >> 2) ^ (row & 3); voffW = (unsigned)(row * 8192 + c * 64 + (j & 3) * 16); }
        { const int row = 8 * wid + (lane >> 3), sl = lane & 7, hf = (sl >> 2) ^ ((row >> 1) & 1), part = hf ? 2 : ((sl >> 1) & 1);
          voffE = (unsigned)(row * 3072 + part * 1024 + (sl & 1) * 16); }
        if (wid == 0) glds16(gatew, (unsigned)lane * 16u, lds0 + L_GATE);
        PIN(voffX); PIN(voffW); PIN(voffE);
    }
#define XISS(c) do { _Pragma("unroll") for (int e = 0; e < 4; ++e) glds16(gX + e * 8192 + (c) * 128, voffX ^ ((e & 1) << 6), lx + ((c) & 1) * 32768 + e * 1024); } while (0)
#define WISS(c) do { glds16(gW + (size_t)(c) * 524288, voffW, lw + ((c) % 3) * 24576); glds16(gW + (size_t)(c) * 524288 + 262144, voffW, lw + ((c) % 3) * 24576 + 8192); \
                     glds16(gE + (size_t)(c) * 196608, voffE, lw + ((c) % 3) * 24576 + 16384); } while (0)
    {
        LANE_VARS();
        const int wr = wid >> 2, wc = wid & 3, fr = lane & 15, fq = lane >> 4;
        constexpr int HTB = 16384;
#define SA(b, h) (((b) * 2 + (h)) * HTB)
#define SB(b, h) ((4 + (b) * 2 + (h)) * HTB)
        unsigned voffA, voffB;
        { const int row = 8 * wid + (lane >> 3), ch = (lane & 7) ^ ((row >> 1) & 7); voffA = (unsigned)(row * 1024 + ch * 16); }
        { const int row = 4 * wid + (lane >> 4), j = lane & 15, s = ((row >> 3) & 1) * 4 + (row & 3), c = (j >> 1) ^ s; voffB = (unsigned)(row * 8192 + c * 32 + (j & 1) * 16); }
        const char* gA = (const char*)ckv + (size_t)jj * 256 * 1024;
        const char* gB0 = (const char*)wuk + hh_ * 256; const char* gB1 = (const char*)wuv + hh_ * 256;
        const unsigned ldsw = rfl(lds0 + wid * 1024);
#define STAGE_A(b, h, kt) do { const char* g_ = gA + (h) * 131072 + (kt) * 128; glds16(g_, voffA, ldsw + SA(b, h)); glds16(g_ + 65536, voffA, ldsw + SA(b, h) + 8192); } while (0)
#define STAGE_B(b, h, kt) do { const char* g_ = ((h) ? gB1 : gB0) + (size_t)(kt) * 524288; glds16(g_, voffB, ldsw + SB(b, h)); glds16(g_ + 262144, voffB, ldsw + SB(b, h) + 8192); } while (0)
        lds_cptr pa[2], pb[2];
#pragma unroll
        for (int ks = 0; ks < 2; ++ks) pa[ks] = lds + (wr * 64 + fr) * 128 + 16 * ((4 * ks + fq) ^ ((fr >> 1) & 7));
        { const int q = fr >> 2, p = fr & 3, s = (fq & 1) * 4 + q;
#pragma unroll
          for (int n = 0; n < 2; ++n) pb[n] = lds + (8 * fq + q) * 256 + 32 * ((2 * wc + n) ^ s) + 8 * p; }
#define LDA(dst, b, h) do { _Pragma("unroll") for (int m = 0; m < 4; ++m) _Pragma("unroll") for (int k = 0; k < 2; ++k) dst[m][k] = ldsr128(pa[k] + SA(b, h) + m * 2048); } while (0)
#define LDB(dst, b, h) do { _Pragma("unroll") for (int n = 0; n < 2; ++n) _Pragma("unroll") for (int k = 0; k < 2; ++k) \
        dst[n][k] = cat8(trrd(pb[n] + SB(b, h) + k * 8192), trrd(pb[n] + SB(b, h) + k * 8192 + 1024)); } while (0)
#define MMA(ai, bj, At, Bt) do { __builtin_amdgcn_s_setprio(1); _Pragma("unroll") for (int m = 0; m < 4; ++m) _Pragma("unroll") for (int n = 0; n < 2; ++n) _Pragma("unroll") for (int k = 0; k < 2; ++k) \
        acc[ai][bj][m][n] = (bj) ? MFMA16(At[m][k], Bt[n][k], acc[ai][bj][m][n]) : MFMA16(Bt[n][k], At[m][k], acc[ai][bj][m][n]); __builtin_amdgcn_s_setprio(0); } while (0)
        const unsigned scr = lds0 + 143360 + wid * 256; const size_t gw = (size_t)(bid * 8 + wid);
        auto warm = [&](int slot) {
            const unsigned vo = (unsigned)lane * 128u;
            if (slot == 0) { if (lane < 8) touch8k((const char*)x + gw * 1024, vo, scr); }
            else if (slot == 1) { if (lane < 16) touch8k((const char*)wq + gw * 2048, vo, scr); }
            else if (slot == 2) { if (lane < 6) touch8k((const char*)wqkv + gw * 768, vo, scr); }
            else if (slot == 3) { if (lane < 16) touch8k((const char*)wo_a + gw * 2048, vo, scr); }
            else if (slot == 4) { if (lane < 2) touch8k((const char*)wo_b + gw * 256, vo, scr); }
        };
        f32x4 acc[2][2][4][2] = {};
        bf16x8 At[4][2], B0[2][2], B1[2][2];
        constexpr int nt = 8;
        STAGE_B(0, 0, 0); STAGE_A(0, 0, 0); STAGE_B(0, 1, 0); STAGE_A(0, 1, 0);
        if (wr == 1) BAR();
        WAIT_V(4); BAR();
        STAGE_B(1, 0, 1); STAGE_A(1, 0, 1); STAGE_B(1, 1, 1);
        WAIT_V(6); BAR();
        STAMP();
        for (int t = 0; t < nt - 2; t += 2) {
            LDB(B0, 0, 0); SBAR(); LDA(At, 0, 0); STAGE_A(1, 1, t + 1);
            WAIT_L(8); BAR(); WAIT_L(0); MMA(0, 0, At, B0); BAR(); SBAR();
            LDB(B1, 0, 1); STAGE_B(0, 0, t + 2);
            BAR(); WAIT_L(0); MMA(0, 1, At, B1); BAR();
            LDA(At, 0, 1); STAGE_A(0, 0, t + 2);
            BAR(); WAIT_L(0); MMA(1, 0, At, B0); BAR(); SBAR();
            STAGE_B(0, 1, t + 2);
            WAIT_V(6); BAR(); MMA(1, 1, At, B1); BAR();
            warm(t);
            LDB(B0, 1, 0); SBAR(); LDA(At, 1, 0); STAGE_A(0, 1, t + 2);
            WAIT_L(8); BAR(); WAIT_L(0); MMA(0, 0, At, B0); BAR(); SBAR();
            LDB(B1, 1, 1); STAGE_B(1, 0, t + 3);
            BAR(); WAIT_L(0); MMA(0, 1, At, B1); BAR();
            LDA(At, 1, 1); STAGE_A(1, 0, t + 3);
            BAR(); WAIT_L(0); MMA(1, 0, At, B0); BAR(); SBAR();
            STAGE_B(1, 1, t + 3);
            WAIT_V(6); BAR(); MMA(1, 1, At, B1); BAR();
            warm(t + 1);
        }
        { LDB(B0, 0, 0); WAIT_V(0); LDA(At, 0, 0); STAGE_A(1, 1, nt - 1);
          BAR(); WAIT_L(0); MMA(0, 0, At, B0); BAR();
          LDB(B1, 0, 1); BAR(); WAIT_L(0); MMA(0, 1, At, B1); BAR();
          LDA(At, 0, 1); BAR(); WAIT_L(0); MMA(1, 0, At, B0); MMA(1, 1, At, B1); BAR(); }
        { LDB(B0, 1, 0); LDA(At, 1, 0); BAR(); WAIT_L(0); MMA(0, 0, At, B0); BAR();
          LDB(B1, 1, 1); WAIT_V(0);
          WISS(0); XISS(0);
          BAR(); WAIT_L(0); MMA(0, 1, At, B1); BAR();
          LDA(At, 1, 1); BAR(); WAIT_L(0); MMA(1, 0, At, B0); MMA(1, 1, At, B1); BAR(); }
        if (wr == 0) BAR();
        WISS(1); XISS(1);
        SBAR();
#undef SA
#undef SB
#undef STAGE_A
#undef STAGE_B
#undef LDA
#undef LDB
#undef MMA
#pragma unroll
        for (int ai = 0; ai < 2; ++ai) {
#pragma unroll
            for (int m = 0; m < 4; ++m) { const f32x4 a = acc[ai][0][m][0], b = acc[ai][0][m][1];
                pkv[ai * 4 + m] = (u32x4){cvtpk(a[0], a[1]), cvtpk(a[2], a[3]), cvtpk(b[0], b[1]), cvtpk(b[2], b[3])}; }
#pragma unroll
            for (int u = 0; u < 2; ++u)
#pragma unroll
                for (int n = 0; n < 2; ++n) { const f32x4 a = acc[ai][1][2 * u][n], b = acc[ai][1][2 * u + 1][n];
                    pkv[8 + ai * 4 + u * 2 + n] = (u32x4){cvtpk(a[0], a[1]), cvtpk(a[2], a[3]), cvtpk(b[0], b[1]), cvtpk(b[2], b[3])}; }
        }
    }
    STAMP();
    const char* kvw = KVh + w4 * 4096;
    const unsigned ldw = lds0 + w4 * 4096;
    char* const qbp = ws + WS_QB + (size_t)(bid * 4 + w4) * 8192;
    char* const pqp = ws + WS_PQ + (size_t)(bid * 8 + wid) * 2048;
    bf16x8 qf[8]; float gate;
    {
        LANE_VARS();
        u16 wph[8];
#pragma unroll
        for (int j = 0; j < 8; ++j) wph[j] = wphi[hh_ * 256 + (8 * (j >> 2) + 4 * hi + (j & 3)) * 16 + (lane & 15)];
        const char* tb0 = KVh + (size_t)(4 * jj + G) * 32768;
        const unsigned vok = (unsigned)((4 * w4 + (lane >> 4)) * 1024 + (lane & 15) * 16), vov = (unsigned)(16384 + (lane >> 4) * 2048 + (w4 * 32 + (lane & 15)) * 16);
#define KVST(i) do { if ((i) < 8) ST_SC1(tb0 + ((i) >> 2) * 65536, vok, ((i) & 3) * 256, pkv[i]); \
                     else ST_SC1(tb0 + (((i) >> 2) & 1) * 65536 + (((i) >> 1) & 1) * 8192, vov, ((i) & 1) * 256, pkv[i]); } while (0)
        f32x16 qa[5] = {}, d1 = {};
        unsigned xo[2], wo_[4];
#pragma unroll
        for (int s = 0; s < 2; ++s) xo[s] = (unsigned)((32 * wid + r32) * 128 + 16 * ((2 * s + hi) ^ ((r32 >> 1) & 7)));
#pragma unroll
        for (int c = 0; c < 4; ++c) wo_[c] = (unsigned)(L_WE + (8 * hi + q4) * 256 + 64 * (c ^ q4) + 32 * (p4 >> 1) + 16 * blk + 8 * (p4 & 1));
        const unsigned eo = (unsigned)(L_WE + 16384 + (8 * hi + q4) * 128 + 64 * ((q4 >> 1) & 1) + 32 * blk + 8 * p4);
        const unsigned vo_ = (unsigned)(L_WE + 16384 + (8 * hi + q4) * 128 + 64 * (((q4 >> 1) & 1) ^ 1) + 8 * p4);
        bf16x8 fx[2], fw[2][6], fg[2];
#define QR_(buf, c, ks, u) do { \
        if ((u) == 0) { fx[buf] = ldsr128(lds + ((c) & 1) * 32768 + (xo[(ks) & 1] ^ (((ks) >> 1) << 6))); fg[buf] = ldsr128(lds + L_GATE + ((c) * 64 + (ks) * 16 + 8 * hi) * 2); } \
        else if ((u) < 5) { fw[buf][(u) - 1] = cat8(trrd(lds + wo_[(u) - 1] + ((c) % 3) * 24576 + (ks) * 4096), trrd(lds + wo_[(u) - 1] + ((c) % 3) * 24576 + (ks) * 4096 + 1024)); } \
        else { fw[buf][4] = cat8(trrd(lds + eo + ((c) % 3) * 24576 + (ks) * 2048), trrd(lds + eo + ((c) % 3) * 24576 + (ks) * 2048 + 512)); \
               fw[buf][5] = cat8(trrd(lds + vo_ + ((c) % 3) * 24576 + (ks) * 2048), trrd(lds + vo_ + ((c) % 3) * 24576 + (ks) * 2048 + 512)); } } while (0)
#define QM_(buf, u) do { if ((u) < 5) qa[u] = MFMA32(fw[buf][u], fx[buf], qa[u]); else d1 = MFMA32(fx[buf], blk ? fg[buf] : fw[buf][5], d1); } while (0)
#define QRD(buf, c, ks) do { _Pragma("unroll") for (int u_ = 0; u_ < 6; ++u_) QR_(buf, c, ks, u_); } while (0)
#define QSTEP(buf, c, ks, rd) do { _Pragma("unroll") for (int u_ = 0; u_ < 6; ++u_) { if (rd) QR_((buf) ^ 1, c, ks, u_); QM_(buf, u_); SBAR(); } } while (0)
#define WV_(n) asm volatile("s_waitcnt vmcnt(%0)" :: "i"(n) : "memory")
#define NST(c) ((c) < 0 ? 0 : (c) < 4 ? 3 : (c) < 6 ? 2 : 0)
#define ST0(c) ((c) < 4 ? 3 * (c) : 12 + 2 * ((c) - 4))
#define QCHUNK(c) do { \
        SBAR(); QSTEP(0, c, 1, true); QSTEP(1, c, 2, true); \
        if ((c) < 7) { WV_(4 + NST((c) - 1)); } BAR(); asm volatile("" ::: "memory"); \
        if ((c) == 7) { if (tid == 0) __hip_atomic_fetch_add(CNT + hh_ * 64, 1u, RLX_AGENT); } \
        if ((c) < 6) WISS((c) + 2); \
        _Pragma("unroll") for (int i_ = 0; i_ < NST(c); ++i_) KVST(ST0(c) + i_); \
        SBAR(); QSTEP(0, c, 3, true); \
        if ((c) < 7) { WV_(((c) < 6 ? 3 : 0) + NST(c)); } \
        SBAR(); QSTEP(1, (c) + 1, 0, (c) < 7); \
        if ((c) < 6) XISS((c) + 2); } while (0)
        asm volatile("s_waitcnt vmcnt(7)\n\ts_barrier" ::: "memory");
        bf16x8 wpf;
#pragma unroll
        for (int j = 0; j < 8; ++j) wpf[j] = (short)(blk ? (wph[j] ^ 0x8000) : wph[j]);
        PIN(wpf);
        STAMP();
        QRD(0, 0, 0);
        QCHUNK(0); QCHUNK(1); QCHUNK(2); QCHUNK(3); QCHUNK(4); QCHUNK(5); QCHUNK(6); QCHUNK(7);
#undef QRD
#undef QR_
#undef QM_
#undef QSTEP
#undef QCHUNK
#undef XISS
#undef WISS
#undef KVST
#undef NST
#undef ST0
        STAMP();
        BAR(); asm volatile("" ::: "memory");
        unsigned f1 = __hip_atomic_load(CNT + hh_ * 64, RLX_AGENT);
#pragma unroll
        for (int cc = 0; cc < 4; ++cc) {
#pragma unroll
            for (int r = 0; r < 16; ++r) qa[cc][r] *= QSCALE;
            qf[2 * cc] = PACK8(qa[cc], 0); qf[2 * cc + 1] = PACK8(qa[cc], 8);
        }
        const lds_ptr qp = lds + L_V + 16384 + (w4 >> 1) * 32768 + (w4 & 1) * 8192 + lane * 16;
        if (G == 0) {
#pragma unroll
            for (int s = 0; s < 8; ++s) *(LAS bf16x8*)(qp + s * 1024) = qf[s];
        }
        if (rfl(f1) < 8u) wait_cnt(CNT + hh_ * 64, 8u);
        asm volatile("" ::: "memory");
        STAMP();
#pragma unroll
        for (int e = 0; e < 8; ++e) glds16c(kvw + (size_t)(G + 2 * (e >> 2)) * 32768 + (e & 3) * 1024, vlin, ldw + G * 49152 + (e >> 2) * 16384 + (e & 3) * 1024);
        LAS float* red = (LAS float*)(lds + L_V + G * 32768 + w4 * 4096);
        if (r32 == 16) {
#pragma unroll
            for (int r = 0; r < 16; ++r) red[(r & 3) + 8 * (r >> 2) + 4 * hi] = d1[r];
        }
        WAIT_L(0);
        { const float gz = bfround(red[r32]); gate = bfround(1.f / (1.f + __expf(-gz))); }
        f32x16 pq = MFMA32(wpf, PACK8(qa[4], 0), f32x16{});
        f32x16 pk = MFMA32(wpf, PACK8(qa[4], 8), f32x16{});
        dual_softmax(pq); dual_softmax(pk);
        f32x16 d3;
        { bf16x8 id0, id1;
#pragma unroll
          for (int j = 0; j < 8; ++j) { const int f = 8 * (j >> 2) + 4 * hi + (j & 3); id0[j] = (short)(r32 == f ? 0x3F80 : 0); id1[j] = (short)(r32 == f + 16 ? 0x3F80 : 0); }
          d3 = MFMA32(PACK8(pk, 0), id0, f32x16{}); d3 = MFMA32(PACK8(pk, 8), id1, d3); }
        { const bf16x8 t0 = PACK8(pq, 0), t1 = PACK8(pq, 8); ST_PL(pqp, vlin, 0, t0); ST_PL(pqp, vlin, 1024, t1); }
        if (G) {
#pragma unroll
            for (int s = 0; s < 8; ++s) ST_PL(qbp + (s >> 2) * 4096, vlin, (s & 3) * 1024, qf[s]);
        }
        const bf16x8 ones = {0x3F80, 0x3F80, 0x3F80, 0x3F80, 0x3F80, 0x3F80, 0x3F80, 0x3F80};
        const bf16x8 v0 = PACK8(d1, 0), v1 = PACK8(d1, 8);
        f32x16 d4 = MFMA32(PACK8(d3, 0), blk ? ones : v0, f32x16{});
        d4 = MFMA32(PACK8(d3, 8), blk ? ones : v1, d4);
#pragma unroll
        for (int r = 0; r < 16; ++r) red[((r & 3) + 8 * (r >> 2) + 4 * hi) * 32 + r32] = d4[r];
        WAIT_L(0); BAR(); asm volatile("" ::: "memory");
        if (tid < 256) {
            f32x4 s = {0.f, 0.f, 0.f, 0.f};
#pragma unroll
            for (int w = 0; w < 8; ++w) s += *(const LAS f32x4*)(lds + L_V + (w >> 2) * 32768 + (w & 3) * 4096 + tid * 16);
            ST_SC1(ws + WS_KVP + (size_t)(hh_ * 8 + jj) * 4096, (unsigned)tid * 16u, 0, s);
        }
        if (G) {
#pragma unroll
            for (int s = 0; s < 8; ++s) qf[s] = ldsr128(qp + s * 1024);
        }
        if (G) { WAIT_BAR(10); } else { WAIT_BAR(3); }
    }
    STAMP();
    {
    LANE_VARS();
    const int prow = (r32 & 7) | ((r32 & 8) << 1) | ((r32 & 16) >> 1);
    const unsigned kro = (unsigned)(prow * 16 + hi * 1024), vro = (unsigned)(L_V + G * 32768 + r32 * 16 + hi * 2048);
    const int dq = 32 * w4 + r32 - 64 * G - 4 * hi;
    unsigned ks0 = G * 49152u, ks1 = ks0 + 16384u, ks2 = ks0 + 32768u;
    f32x16 o[4], negm, sA0, sA1, sB0, sB1; bf16x8 kf[16], vf[16]; u32x4 pw[4]; float m, l;
#define KRD(i, kp) kf[i] = ldsr128((kp) + ((i) >> 1) * 2048 + ((i) & 1) * 512)
#define VRD(i, vp) vf[i] = ldsr128((vp) + ((i) >> 2) * 4096 + ((i) & 3) * 512)
#define PF(k) __builtin_bit_cast(bf16x8, pw[k])
#define DMA1(i) do { if ((i) < 4) glds16c(gk_ + (i) * 1024, vlin, dk_ + (i) * 1024); else glds16c(gv_ + ((i) - 4) * 1024, vlin, dv_ + ((i) - 4) * 1024); } while (0)
#define ROT() do { const unsigned t_ = ks0; ks0 = ks1; ks1 = ks2; ks2 = t_; } while (0)
#define DMA_SETUP(r) const int tk_ = (r) + 2 <= R ? (r) + 2 : (pass == 0 ? (r) + 1 - R : 0); \
        const char* gk_ = kvw + (size_t)(2 * tk_ + G) * 32768; const char* gv_ = kvw + (size_t)(2 * (r) + G) * 32768 + 16384; \
        const unsigned dk_ = ldw + ks2, dv_ = ldw + L_V + G * 32768 + ((r) & 1) * 16384;
#define GAPA(i, C0, C1, P0, P1) do { \
        if ((i) + 4 < 16) { KRD((i) + 4, kp_); } else { VRD((i) - 12, vp_); } \
        if (((i) & 1) == 0) DMA1((i) >> 1); \
        if ((i) & 1) C1 = MFMA32(kf[i], qf[(i) >> 1], (i) < 2 ? negm : C1); else C0 = MFMA32(kf[i], qf[(i) >> 1], (i) < 2 ? negm : C0); \
        if ((i) < 8) { P1[2 * (i)] = EX2(P1[2 * (i)]); P1[2 * (i) + 1] = EX2(P1[2 * (i) + 1]); pw[(i) >> 2][(i) & 3] = cvtpk(P0[2 * (i)], P0[2 * (i) + 1]); PIN(P1); PIN(pw[(i) >> 2]); } \
        else { l += P1[2 * (i) - 16]; l2 += P1[2 * (i) - 15]; pw[(i) >> 2][(i) & 3] = cvtpk(P1[2 * (i) - 16], P1[2 * (i) - 15]); PIN(l); PIN(l2); PIN(pw[(i) >> 2]); } \
        SBAR(); } while (0)
#define PVM(i) o[(i) & 3] = MFMA32(vf[i], PF((i) >> 2), o[(i) & 3])
#define RDB(i) do { if ((i) + 4 < 16) { VRD((i) + 4, vp_); } else { KRD((i) - 12, kn_); } } while (0)
#define ROUND(C0, C1, P0, P1, r) do { \
        const lds_cptr kp_ = lds + ks0 + kro; const lds_cptr kn_ = lds + ks1 + kro; const lds_cptr vp_ = lds + vro + (((r) - 1) & 1) * 16384; \
        DMA_SETUP(r) float l2 = 0.f; SBAR(); \
        GAPA(0, C0, C1, P0, P1); GAPA(1, C0, C1, P0, P1); GAPA(2, C0, C1, P0, P1); GAPA(3, C0, C1, P0, P1); \
        GAPA(4, C0, C1, P0, P1); GAPA(5, C0, C1, P0, P1); GAPA(6, C0, C1, P0, P1); GAPA(7, C0, C1, P0, P1); \
        GAPA(8, C0, C1, P0, P1); GAPA(9, C0, C1, P0, P1); GAPA(10, C0, C1, P0, P1); GAPA(11, C0, C1, P0, P1); \
        GAPA(12, C0, C1, P0, P1); GAPA(13, C0, C1, P0, P1); GAPA(14, C0, C1, P0, P1); GAPA(15, C0, C1, P0, P1); \
        l += l2; \
        RDB(0); PVM(0); SBAR(); RDB(1); PVM(1); SBAR(); \
        if ((r) == R) mask_tile(C0, C1, dq); \
        float ma, mb; \
        RDB(2); PVM(2); ma = MX3(C0[0], C0[1], C1[0]); mb = MX3(C0[2], C0[3], C1[1]); ma = MX3(ma, C1[2], C1[3]); mb = MX3(mb, C0[4], C0[5]); PIN(ma); PIN(mb); SBAR(); \
        RDB(3); PVM(3); ma = MX3(ma, C0[6], C0[7]); mb = MX3(mb, C1[4], C1[5]); ma = MX3(ma, C1[6], C1[7]); mb = MX3(mb, C0[8], C0[9]); PIN(ma); PIN(mb); SBAR(); \
        RDB(4); PVM(4); ma = MX3(ma, C0[10], C0[11]); mb = MX3(mb, C1[8], C1[9]); ma = MX3(ma, C1[10], C1[11]); mb = MX3(mb, C0[12], C0[13]); PIN(ma); PIN(mb); SBAR(); \
        RDB(5); PVM(5); ma = MX3(ma, C0[14], C0[15]); mb = MX3(mb, C1[12], C1[13]); ma = MX3(ma, C1[14], C1[15]); \
        const float rm = swap32_max(fmaxf(ma, mb)); bool pend = false; float fsc = 1.f; \
        if (__builtin_expect(__any(rm > THR), 0)) { const float dl = fmaxf(rm, 0.f); m += dl; fsc = EX2(-dl); l *= fsc; pend = true; \
            _Pragma("unroll") for (int e = 0; e < 16; ++e) { C0[e] -= dl; C1[e] -= dl; negm[e] = -m; } } \
        SBAR(); \
        RDB(6); PVM(6); C0[0] = EX2(C0[0]); C0[1] = EX2(C0[1]); PIN(C0); SBAR(); \
        RDB(7); PVM(7); C0[2] = EX2(C0[2]); C0[3] = EX2(C0[3]); l += C0[0]; l2 = C0[1]; PIN(C0); PIN(l); SBAR(); \
        RDB(8); PVM(8); C0[4] = EX2(C0[4]); C0[5] = EX2(C0[5]); l += C0[2]; l2 += C0[3]; PIN(C0); PIN(l); PIN(l2); SBAR(); \
        RDB(9); PVM(9); C0[6] = EX2(C0[6]); C0[7] = EX2(C0[7]); l += C0[4]; l2 += C0[5]; PIN(C0); PIN(l); PIN(l2); SBAR(); \
        RDB(10); PVM(10); C0[8] = EX2(C0[8]); C0[9] = EX2(C0[9]); l += C0[6]; l2 += C0[7]; PIN(C0); PIN(l); PIN(l2); SBAR(); \
        RDB(11); PVM(11); C0[10] = EX2(C0[10]); C0[11] = EX2(C0[11]); l += C0[8]; l2 += C0[9]; PIN(C0); PIN(l); PIN(l2); SBAR(); \
        RDB(12); PVM(12); C0[12] = EX2(C0[12]); C0[13] = EX2(C0[13]); l += C0[10]; l2 += C0[11]; PIN(C0); PIN(l); PIN(l2); SBAR(); \
        RDB(13); PVM(13); C0[14] = EX2(C0[14]); C0[15] = EX2(C0[15]); l += C0[12]; l2 += C0[13]; PIN(C0); PIN(l); PIN(l2); SBAR(); \
        RDB(14); PVM(14); l += C0[14]; l2 += C0[15]; PIN(l); PIN(l2); SBAR(); \
        RDB(15); PVM(15); l += l2; PIN(l); SBAR(); \
        if (__builtin_expect(pend, 0)) { _Pragma("unroll") for (int c = 0; c < 4; ++c) _Pragma("unroll") for (int e = 0; e < 16; ++e) o[c][e] *= fsc; } \
        asm volatile("s_waitcnt vmcnt(0)\n\ts_barrier" ::: "memory"); ROT(); } while (0)

    for (int pass = 0; pass < 2; ++pass) {
        const int R = pass ? bB : bA;
        {
            const lds_cptr kp_ = lds + ks0 + kro; const lds_cptr kn_ = lds + ks1 + kro;
            DMA_SETUP(0)
#pragma unroll
            for (int i = 0; i < 6; ++i) KRD(i, kp_);
#pragma unroll
            for (int i = 0; i < 8; ++i) DMA1(i);
#pragma unroll
            for (int c = 0; c < 4; ++c) o[c] = f32x16{};
            SBAR();
#pragma unroll
            for (int i = 0; i < 16; ++i) {
                if (i + 6 < 16) KRD(i + 6, kp_);
                if (i & 1) sA1 = MFMA32(kf[i], qf[i >> 1], i < 2 ? f32x16{} : sA1); else sA0 = MFMA32(kf[i], qf[i >> 1], i < 2 ? f32x16{} : sA0);
                SBAR();
            }
            if (R == 0) mask_tile(sA0, sA1, dq);
            float ma = MX3(sA0[0], sA0[1], sA1[0]), mb = MX3(sA0[2], sA0[3], sA1[1]); ma = MX3(ma, sA1[2], sA1[3]);
#pragma unroll
            for (int r = 4; r < 16; r += 4) { ma = MX3(ma, sA0[r], sA0[r + 1]); mb = MX3(mb, sA0[r + 2], sA0[r + 3]); ma = MX3(ma, sA1[r], sA1[r + 1]); mb = MX3(mb, sA1[r + 2], sA1[r + 3]); }
            m = fmaxf(swap32_max(fmaxf(ma, mb)), -1e30f); l = 0.f;
#pragma unroll
            for (int i = 0; i < 4; ++i) KRD(i, kn_);
#pragma unroll
            for (int e = 0; e < 16; ++e) { sA0[e] = EX2(sA0[e] - m); l += sA0[e]; sA1[e] -= m; negm[e] = -m; }
            asm volatile("s_waitcnt vmcnt(0)\n\ts_barrier" ::: "memory"); ROT();
            if (tid == 0) { if (pass == 0) __hip_atomic_fetch_add(CNT + (32 + hh_) * 64, 1u, RLX_AGENT);
                else { __hip_atomic_fetch_or(CNT + (64 + bA) * 64, 1u << hh_, RLX_AGENT);
                       __hip_atomic_fetch_or(CNT + (64 + bA) * 64 + 1, 1u << hh_, RLX_AGENT); __hip_atomic_fetch_or(CNT + (64 + bB) * 64 + 1, 1u << hh_, RLX_AGENT); } }
        }
        STAMP();
        int r = 1;
        for (; r + 1 <= R; r += 2) { ROUND(sB0, sB1, sA0, sA1, r); ROUND(sA0, sA1, sB0, sB1, r + 1); }
        if (r <= R) { ROUND(sB0, sB1, sA0, sA1, r); sA0 = sB0; sA1 = sB1; }
        STAMP();
        f32x2 ps[8]; bf16x8 phq[2];
        {
        LANE_VARS();
        {
            const lds_cptr vp_ = lds + vro + (R & 1) * 16384;
#pragma unroll
            for (int i = 0; i < 8; ++i) VRD(i, vp_);
            const unsigned f2 = __hip_atomic_load(CNT + (32 + hh_) * 64, RLX_AGENT);
#pragma unroll
            for (int i = 0; i < 8; ++i) pw[i >> 2][i & 3] = cvtpk(sA0[2 * i], sA0[2 * i + 1]);
            SBAR();
#pragma unroll
            for (int i = 0; i < 16; ++i) {
                if (i + 8 < 16) VRD(i + 8, vp_);
                if (i == 8) {
                    if (rfl(f2) < 8u) wait_cnt(CNT + (32 + hh_) * 64, 8u);
#pragma unroll
                    for (int w = 0; w < 8; ++w) ps[w] = __builtin_bit_cast(f32x2, __hip_atomic_load((const unsigned long long*)(ws + WS_KVP + (size_t)(hh_ * 8) * 4096 + (unsigned)(w * 4096 + tid * 8)), RLX_AGENT));
#pragma unroll
                    for (int s = 0; s < 8; ++s) qf[s] = *(const bf16x8*)(qbp + (vlin + s * 1024));
                }
                PVM(i);
                if (i < 8) { sA1[2 * i] = EX2(sA1[2 * i]); sA1[2 * i + 1] = EX2(sA1[2 * i + 1]); PIN(sA1); }
                if (i >= 1 && i < 9) { l += sA1[2 * i - 2]; l += sA1[2 * i - 1]; pw[2 + ((i - 1) >> 2)][(i - 1) & 3] = cvtpk(sA1[2 * i - 2], sA1[2 * i - 1]); PIN(l); PIN(pw[2 + ((i - 1) >> 2)]); }
                SBAR(); }
        }
        phq[0] = *(const bf16x8*)(pqp + vlin); phq[1] = *(const bf16x8*)(pqp + (vlin + 1024));
        l = swap32_add(l);
        BAR(); asm volatile("" ::: "memory");
        const unsigned st0 = G ? ks2 - 49152u : ks2;
        { const lds_ptr xp = lds + L_V + wid * 8192 + lane * 16;
#pragma unroll
          for (int c = 0; c < 2; ++c)
#pragma unroll
              for (int g = 0; g < 4; ++g) { f32x4 t;
#pragma unroll
                  for (int e = 0; e < 4; ++e) t[e] = G ? o[c][4 * g + e] : o[2 + c][4 * g + e];
                  *(LAS f32x4*)(xp + (c * 4 + g) * 1024) = t; }
          *(LAS f32x4*)(lds + st0 + wid * 1024 + lane * 16) = (f32x4){m, l, gate, 0.f};
          if (pass == 0) { f32x2 s = ps[0];
#pragma unroll
              for (int w = 1; w < 8; ++w) s += ps[w];
              *(LAS f32x2*)(lds + st0 + 8192 + tid * 8) = s; } }
        WAIT_L(0); BAR(); asm volatile("" ::: "memory");
        { const int ow = wid ^ 4;
          const f32x4 st = *(const LAS f32x4*)(lds + st0 + ow * 1024 + lane * 16);
          const float mn = fmaxf(m, st[0]), f0 = EX2(m - mn), f1 = EX2(st[0] - mn);
          const float gg = (G == pass) ? gate : st[2];
          const float inv = gg / (l * f0 + st[1] * f1); const float c0 = f0 * inv, c1 = f1 * inv;
          const lds_cptr xp = lds + L_V + ow * 8192 + lane * 16;
          const char* dst = A3 + (size_t)(R * 576 + hh_ * 16 + G * 8) * 2048;
          const unsigned vod = (unsigned)(hi * 2048 + (32 * w4 + r32) * 16);
#pragma unroll
          for (int c = 0; c < 2; ++c) {
              float v[16];
#pragma unroll
              for (int g = 0; g < 4; ++g) { const f32x4 t = *(const LAS f32x4*)(xp + (c * 4 + g) * 1024);
#pragma unroll
                  for (int e = 0; e < 4; ++e) v[4 * g + e] = (G ? o[2 + c][4 * g + e] : o[c][4 * g + e]) * c0 + t[e] * c1; }
              { const bf16x8 t0 = PACK8(v, 0), t1 = PACK8(v, 8); ST_SC1(dst + c * 8192, vod, 0, t0); ST_SC1(dst + c * 8192 + 4096, vod, 0, t1); }
          }
          if (pass == 0) {
              float kvv[2][8];
              const int col = r32 < 16 ? r32 : 16;
#pragma unroll
              for (int s = 0; s < 2; ++s)
#pragma unroll
                  for (int j = 0; j < 8; ++j) { const int f = 16 * s + 8 * (j >> 2) + 4 * hi + (j & 3); const float v = *(const LAS float*)(lds + st0 + 8192 + (f * 32 + col) * 4); kvv[s][j] = r32 <= 16 ? v : 0.f; }
              f32x16 nm = MFMA32(PACK8(kvv[0], 0), phq[0], f32x16{});
              nm = MFMA32(PACK8(kvv[1], 0), phq[1], nm);
              const float den = bfround(bfround(lo32_bcast(nm[8])) + 1e-6f); const float sc = (1.f - gate) / den;
              float ov[8];
#pragma unroll
              for (int r = 0; r < 8; ++r) ov[r] = bfround(nm[r]) * sc;
              const bf16x8 t0 = PACK8(ov, 0);
              ST_SC1(A3 + (size_t)((G ? bB : bA) * 576 + 512 + 2 * hh_) * 2048, vod, 0, t0);
          } }
        WAIT_L(0); BAR(); asm volatile("" ::: "memory");
        }
        STAMP();
    }
#undef KRD
#undef VRD
#undef PF
#undef DMA1
#undef ROT
#undef DMA_SETUP
#undef GAPA
#undef PVM
#undef RDB
#undef ROUND
    }
    {
        LANE_VARS();
        const int rt = 4 * (bid & 7) + (bid >> 6), ct = (bid >> 3) & 7;
        unsigned voffW; { const int row = lane >> 3, sl = lane & 7, nb = (sl >> 2) ^ ((row >> 1) & 1); voffW = (unsigned)(row * 1024 + nb * 64 + (sl & 3) * 16); }
        const char* gA = A3 + (size_t)((rt >> 1) * 576 + 2 * wid) * 2048 + (rt & 1) * 1024;
        const char* gWa = (const char*)wo_a + (size_t)wid * 16384 + ct * 128; const char* gWb = (const char*)wo_b + (size_t)wid * 16384 + ct * 128;
        const unsigned wb = lds0 + wid * 20480;
        const unsigned* rdy = CNT + (64 + (rt >> 1)) * 64;
        auto issue = [&](int u, unsigned slot) {
            const char* ga = gA + (size_t)u * 32768; const char* gw = u < 32 ? gWa + (size_t)u * 131072 : gWb + (size_t)(u - 32) * 131072;
            glds16c(ga, vlin, wb + slot); glds16c(ga + 2048, vlin, wb + slot + 1024); glds16(gw, voffW, wb + slot + 2048); glds16(gw + 8192, voffW, wb + slot + 3072); };
        unsigned seen = 0u; int hnext = 0;
        auto next_head = [&]() -> int {
            if (!((seen >> hnext) & 1u)) { unsigned spins = 0;
                for (;;) { seen = rfl(__hip_atomic_load(rdy, RLX_AGENT)); if (((seen >> hnext) & 1u) || ++spins > (1u << 20)) break; __builtin_amdgcn_s_sleep(2); }
                asm volatile("" ::: "memory"); }
            return hnext++; };
        { unsigned spins = 0; while (rfl(__hip_atomic_load(rdy + 1, RLX_AGENT)) != ~0u) { __builtin_amdgcn_s_sleep(2); if (++spins > (1u << 20)) break; } }
        asm volatile("" ::: "memory");
#pragma unroll
        for (int i = 0; i < 4; ++i) issue(32 + i, i * 4096u);
        asm volatile("s_waitcnt vmcnt(16)\n\ts_barrier" ::: "memory");
        if (tid == 0) __hip_atomic_fetch_or(CNT + (64 + bB) * 64, 1u << hh_, RLX_AGENT);
        STAMP();
        issue(next_head(), 16384u);
        f32x16 acc[2][2] = {};
        const unsigned ao = (unsigned)(wid * 20480 + hi * 1024 + r32 * 16);
        unsigned bo[2];
#pragma unroll
        for (int nb = 0; nb < 2; ++nb) bo[nb] = (unsigned)(wid * 20480 + 2048 + (4 * hi + q4) * 128 + 64 * (nb ^ ((q4 >> 1) & 1)) + 32 * blk + 8 * p4);
        bf16x8 af[2][2], bf[2][2];
#define K3RD(buf, sb) do { af[buf][0] = ldsr128((sb) + ao); bf[buf][0] = cat8(trrd((sb) + bo[0]), trrd((sb) + bo[0] + 1024)); \
        af[buf][1] = ldsr128((sb) + ao + 512); bf[buf][1] = cat8(trrd((sb) + bo[1]), trrd((sb) + bo[1] + 1024)); } while (0)
#define K3BODY(buf, i) do { \
        const unsigned snx = sc == 16384u ? 0u : sc + 4096u; \
        if ((i) + 1 < 36) K3RD(buf ^ 1, lds + snx); \
        SBAR(); acc[0][0] = MFMA32(bf[buf][0], af[buf][0], acc[0][0]); acc[1][0] = MFMA32(bf[buf][0], af[buf][1], acc[1][0]); SBAR(); \
        if ((i) + 5 < 36) issue(next_head(), sc); \
        SBAR(); acc[0][1] = MFMA32(bf[buf][1], af[buf][0], acc[0][1]); acc[1][1] = MFMA32(bf[buf][1], af[buf][1], acc[1][1]); SBAR(); \
        sc = snx; } while (0)
#define K3W(n) asm volatile("s_waitcnt vmcnt(" #n ") lgkmcnt(0)" ::: "memory")
        unsigned sc = 0u;
        WAIT_V(16);
        K3RD(0, lds);
        for (int i = 0; i < 32; i += 2) {
            if (G) __builtin_amdgcn_s_setprio(1); K3W(12); K3BODY(0, i);
            if (G) __builtin_amdgcn_s_setprio(0); K3W(12); K3BODY(1, i + 1); }
        K3W(8); K3BODY(0, 32); K3W(4); K3BODY(1, 33); K3W(0); K3BODY(0, 34); K3W(0); K3BODY(1, 35);
#undef K3RD
#undef K3BODY
#undef K3W
        STAMP();
        BAR(); asm volatile("" ::: "memory");
#pragma unroll
        for (int mi = 0; mi < 2; ++mi)
#pragma unroll
            for (int nb = 0; nb < 2; ++nb)
#pragma unroll
                for (int g = 0; g < 4; ++g) { const int row = 32 * mi + r32, ch = 8 * nb + 2 * g + hi;
                    *(LAS f32x4*)(lds + wid * 16384 + row * 256 + 16 * (ch ^ (row & 15))) = (f32x4){acc[mi][nb][4 * g], acc[mi][nb][4 * g + 1], acc[mi][nb][4 * g + 2], acc[mi][nb][4 * g + 3]}; }
        WAIT_L(0); BAR(); asm volatile("" ::: "memory");
        { const int row = tid >> 3, c8 = tid & 7; f32x4 s0 = {}, s1 = {};
#pragma unroll
          for (int w = 0; w < 8; ++w) { s0 += *(const LAS f32x4*)(lds + w * 16384 + row * 256 + 16 * ((2 * c8) ^ (row & 15)));
                                        s1 += *(const LAS f32x4*)(lds + w * 16384 + row * 256 + 16 * ((2 * c8 + 1) ^ (row & 15))); }
          u32x4 w = {cvtpk(s0[0], s0[1]), cvtpk(s0[2], s0[3]), cvtpk(s1[0], s1[1]), cvtpk(s1[2], s1[3])};
          *(u32x4*)(out + (size_t)(rt * 64 + row) * 512 + ct * 64 + c8 * 8) = w; }
    }
    STAMP(); STAMP_RT();
    STAMP_OUT(ws + WS_END);
}

extern "C" void kernel_launch(void* const* d_in, const int* in_sizes, int n_in, void* d_out, int out_size, void* d_ws, size_t ws_size,
                              hipStream_t stream) {
    (void)in_sizes; (void)n_in; (void)out_size; (void)ws_size;
    char* ws = (char*)d_ws;
    (void)hipFuncSetAttribute((const void*)kf_all, hipFuncAttributeMaxDynamicSharedMemorySize, LDS_BYTES);
    (void)hipMemsetAsync(ws + WS_CNT, 0, CNT_BYTES, stream);
    kf_all<<<256, 512, LDS_BYTES, stream>>>((const u16*)d_in[0], (const u16*)d_in[1], (const u16*)d_in[2], (const u16*)d_in[3], (const u16*)d_in[4], (const u16*)d_in[5],
                                            (const u16*)d_in[7], (const u16*)d_in[8], (const u16*)d_in[9], (const u16*)d_in[10], ws, (u16*)d_out);
}
```

```cpp
#include <hip/hip_runtime.h>
#include <hip/hip_bf16.h>
#include <stdint.h>
#define PREFETCH 1

typedef unsigned short u16;
typedef short bf16x8 __attribute__((ext_vector_type(8)));
typedef short s16x4 __attribute__((ext_vector_type(4)));
typedef float f32x2 __attribute__((ext_vector_type(2)));
typedef float f32x4 __attribute__((ext_vector_type(4)));
typedef float f32x16 __attribute__((ext_vector_type(16)));
typedef unsigned u32x4 __attribute__((ext_vector_type(4)));
typedef unsigned u32x2 __attribute__((ext_vector_type(2)));
typedef __bf16 bf16x2_t __attribute__((ext_vector_type(2)));
#define LAS __attribute__((address_space(3)))
#define DI __device__ __forceinline__
typedef LAS const char* lds_cptr;
typedef LAS char* lds_ptr;

constexpr int SEQ = 2048, DM = 512, NH = 32, HD = 128;
constexpr size_t WS_CNT = 0;
constexpr size_t CNT_BYTES = 80 * 256;
constexpr size_t WS_KV = 65536;
constexpr size_t WS_A3 = WS_KV + (size_t)NH * 32 * 32768;
constexpr size_t WS_KVP = WS_A3 + (size_t)576 * SEQ * 16;
constexpr size_t WS_QB = WS_KVP + (size_t)NH * 8 * 4096;
constexpr size_t WS_PQ = WS_QB + (size_t)256 * 32768;
constexpr size_t WS_END = WS_PQ + (size_t)256 * 16384;
constexpr int LDS_BYTES = 163840;

#define MFMA32(a, b, c) __builtin_amdgcn_mfma_f32_32x32x16_bf16((a), (b), (c), 0, 0, 0)
#define MFMA16(a, b, c) __builtin_amdgcn_mfma_f32_16x16x32_bf16((a), (b), (c), 0, 0, 0)
#define SBAR() __builtin_amdgcn_sched_barrier(0)
#define WAIT_V(n) asm volatile("s_waitcnt vmcnt(" #n ")" ::: "memory")
#define WAIT_L(n) asm volatile("s_waitcnt lgkmcnt(" #n ")" ::: "memory")
#define BAR() __builtin_amdgcn_s_barrier()
#define WAIT_BAR(N) asm volatile("s_waitcnt vmcnt(" #N ") lgkmcnt(0)\n\ts_barrier" ::: "memory")

#define STAMP_DECL()
#define STAMP()
#define STAMP_RT()
#define STAMP_OUT(base)
#define STAMP_PARAM
#define STAMP_ARG
DI unsigned rfl(unsigned x) { return __builtin_amdgcn_readfirstlane(x); }
DI unsigned cvtpk(float lo, float hi) { f32x2 v = {lo, hi}; bf16x2_t b = __builtin_convertvector(v, bf16x2_t); return __builtin_bit_cast(unsigned, b); }
DI float bfround(float x) { return __uint_as_float(cvtpk(x, x) << 16); }
DI void glds16(const void* sbase, unsigned voff, unsigned lds_dst) {
    unsigned sv;
    asm volatile("s_mov_b32 %0, m0\n\ts_mov_b32 m0, %3\n\ts_nop 2\n\tglobal_load_lds_dwordx4 %1, %2\n\ts_mov_b32 m0, %0"
                 : "=&s"(sv) : "v"(voff), "s"(sbase), "s"(lds_dst) : "memory");
}
DI void glds16c(const void* sbase, unsigned voff, unsigned lds_dst) {
    unsigned sv;
    asm volatile("s_mov_b32 %0, m0\n\ts_mov_b32 m0, %3\n\ts_nop 2\n\tglobal_load_lds_dwordx4 %1, %2 sc1\n\ts_mov_b32 m0, %0"
                 : "=&s"(sv) : "v"(voff), "s"(sbase), "s"(lds_dst) : "memory");
}
DI void touch8k(const void* sbase, unsigned voff, unsigned lds_dst) {
    unsigned sv;
    asm volatile("s_mov_b32 %0, m0\n\ts_mov_b32 m0, %3\n\ts_nop 2\n\tglobal_load_lds_dword %1, %2\n\ts_mov_b32 m0, %0"
                 : "=&s"(sv) : "v"(voff), "s"(sbase), "s"(lds_dst) : "memory");
}
DI void glds16c_unless(const void* sbase, unsigned voff, unsigned lds_dst, int a, int b) {
    unsigned sv;
    asm volatile("s_cmp_lt_i32 %4, %5\n\ts_cselect_b64 exec, 0, -1\n\ts_mov_b32 %0, m0\n\ts_mov_b32 m0, %3\n\ts_nop 2\n\tglobal_load_lds_dwordx4 %1, %2 sc1\n\ts_mov_b32 m0, %0\n\ts_mov_b64 exec, -1"
                 : "=&s"(sv) : "v"(voff), "s"(sbase), "s"(lds_dst), "s"(a), "s"(b) : "memory", "scc");
}
DI void ldq_unless(bf16x8* q, const void* sbase, unsigned voff, unsigned skip) {
    asm volatile("s_cmp_lg_u32 %10, 0\n\ts_cbranch_scc0 .Lrare%=\n.Lback%=:\n\t.subsection 1\n.Lrare%=:\n\ts_nop 4\n\t"
                 "global_load_dwordx4 %0, %8, %9 offset:-4096\n\tglobal_load_dwordx4 %1, %8, %9 offset:-3072\n\tglobal_load_dwordx4 %2, %8, %9 offset:-2048\n\tglobal_load_dwordx4 %3, %8, %9 offset:-1024\n\t"
                 "global_load_dwordx4 %4, %8, %9\n\tglobal_load_dwordx4 %5, %8, %9 offset:1024\n\tglobal_load_dwordx4 %6, %8, %9 offset:2048\n\tglobal_load_dwordx4 %7, %8, %9 offset:3072\n\ts_branch .Lback%=\n\t.subsection 0"
                 : "+v"(q[0]), "+v"(q[1]), "+v"(q[2]), "+v"(q[3]), "+v"(q[4]), "+v"(q[5]), "+v"(q[6]), "+v"(q[7]) : "v"(voff), "s"((const char*)sbase + 4096), "s"(skip) : "memory", "scc");
}
typedef short v4i16_t __attribute__((ext_vector_type(4)));
DI s16x4 trrd(lds_cptr p) { return __builtin_bit_cast(s16x4, __builtin_amdgcn_ds_read_tr16_b64_v4i16((LAS v4i16_t*)p)); }
DI bf16x8 cat8(s16x4 a, s16x4 b) { return __builtin_shufflevector(a, b, 0, 1, 2, 3, 4, 5, 6, 7); }
DI bf16x8 ldsr128(lds_cptr p) { return *(const LAS bf16x8*)p; }
DI bf16x8 pack8(float a0, float a1, float a2, float a3, float a4, float a5, float a6, float a7) {
    u32x4 w = {cvtpk(a0, a1), cvtpk(a2, a3), cvtpk(a4, a5), cvtpk(a6, a7)}; return __builtin_bit_cast(bf16x8, w); }
#define PACK8(X, B) pack8((X)[(B)], (X)[(B) + 1], (X)[(B) + 2], (X)[(B) + 3], (X)[(B) + 4], (X)[(B) + 5], (X)[(B) + 6], (X)[(B) + 7])
DI float swap32_max(float v) { auto rr = __builtin_amdgcn_permlane32_swap(__float_as_uint(v), __float_as_uint(v), false, false); return fmaxf(__uint_as_float(rr[0]), __uint_as_float(rr[1])); }
DI float swap32_add(float v) { auto rr = __builtin_amdgcn_permlane32_swap(__float_as_uint(v), __float_as_uint(v), false, false); return __uint_as_float(rr[0]) + __uint_as_float(rr[1]); }
DI float lo32_bcast(float v) { auto rr = __builtin_amdgcn_permlane32_swap(__float_as_uint(v), __float_as_uint(v), false, false); return __uint_as_float(rr[0]); }
template <int CTRL> DI float dpp(float v) { return __uint_as_float(__builtin_amdgcn_update_dpp(0u, __float_as_uint(v), CTRL, 0xF, 0xF, true)); }
DI float row16_max(float v) { v = fmaxf(v, dpp<0xB1>(v)); v = fmaxf(v, dpp<0x4E>(v)); v = fmaxf(v, dpp<0x141>(v)); v = fmaxf(v, dpp<0x140>(v)); return v; }
DI float row16_add(float v) { v += dpp<0xB1>(v); v += dpp<0x4E>(v); v += dpp<0x141>(v); v += dpp<0x140>(v); return v; }

constexpr float QSCALE = 0.08838834764831845f * 1.4426950408889634f;
constexpr float THR = 8.f;
constexpr int L_V = 98304;
constexpr int L_WE = 65536, L_GATE = 139264;
#define PIN(x) asm volatile("" : "+v"(x))
#define MX3(a, b, c) __builtin_fmaxf(__builtin_fmaxf((a), (b)), (c))
#define EX2(v) __builtin_amdgcn_exp2f(v)
#define RLX_AGENT __ATOMIC_RELAXED, __HIP_MEMORY_SCOPE_AGENT
#define ST_PL(base, voff, imm, v) asm volatile("s_nop 4\n\tglobal_store_dwordx4 %0, %1, %2 offset:%3\n\ts_nop 1" :: "v"(voff), "v"(v), "s"(base), "i"(imm) : "memory")
#define ST_SC1(base, voff, imm, v) asm volatile("s_nop 4\n\tglobal_store_dwordx4 %0, %1, %2 offset:%3 sc1\n\ts_nop 1" :: "v"(voff), "v"(v), "s"(base), "i"(imm) : "memory")
DI int fresh_lane() { int l; asm volatile("v_mbcnt_lo_u32_b32 %0, -1, 0\n\tv_mbcnt_hi_u32_b32 %0, -1, %0" : "=v"(l)); return l; }
DI void wait_cnt(const unsigned* p, unsigned want) {
    unsigned spins = 0;
    while (rfl(__hip_atomic_load(p, RLX_AGENT)) < want) { __builtin_amdgcn_s_sleep(2); if (++spins > (1u << 20)) break; }
}
DI void dual_softmax(f32x16& p) {
#pragma unroll
    for (int hf = 0; hf < 2; ++hf) {
        float mx = -INFINITY;
#pragma unroll
        for (int r = 0; r < 8; ++r) { p[8 * hf + r] = bfround(p[8 * hf + r]); mx = fmaxf(mx, p[8 * hf + r]); }
        mx = swap32_max(mx); float s = 0.f;
#pragma unroll
        for (int r = 0; r < 8; ++r) { p[8 * hf + r] = EX2((p[8 * hf + r] - mx) * 1.4426950408889634f); s += p[8 * hf + r]; }
        s = __builtin_amdgcn_rcpf(swap32_add(s));
#pragma unroll
        for (int r = 0; r < 8; ++r) p[8 * hf + r] *= s;
    }
}
DI void mask_half(f32x16& sc, int dqw) {
    if (dqw < 0) {
#pragma unroll
        for (int r = 0; r < 16; ++r) sc[r] = -INFINITY;
    } else if (dqw == 0) {
        const int ln = fresh_lane(); const int lr = (ln & 31) - 4 * (ln >> 5); const float ninf = -INFINITY;
#pragma unroll
        for (int r = 0; r < 16; ++r)
            asm volatile("s_nop 0\n\tv_cmp_gt_i32 vcc, %c3, %2\n\tv_cndmask_b32 %0, %0, %1, vcc" : "+v"(sc[r]) : "v"(ninf), "v"(lr), "i"(16 * ((r >> 2) & 1) + 8 * (r >> 3) + (r & 3)) : "vcc");
    }
}
DI void mask_tile(f32x16& s0, f32x16& s1, int dqw) {
    asm volatile("s_nop 7\n\ts_nop 7" ::: "memory");
    mask_half(s0, dqw); mask_half(s1, dqw - 32); }

__global__ __launch_bounds__(512, 2) void kf_all(const u16* __restrict__ x, const u16* __restrict__ ckv, const u16* __restrict__ wq, const u16* __restrict__ wuk,
                                                 const u16* __restrict__ wuv, const u16* __restrict__ wo_a, const u16* __restrict__ wqkv,
                                                 const u16* __restrict__ wphi, const u16* __restrict__ wo_b, const u16* __restrict__ gatew,
                                                 char* ws, u16* __restrict__ out) {
    extern __shared__ __attribute__((aligned(16))) char shm[];
    const lds_ptr lds = (lds_ptr)shm; const unsigned lds0 = (unsigned)(uintptr_t)shm;
    const int wid = rfl(threadIdx.x >> 6), G = wid >> 2, w4 = wid & 3;
    const int bid = blockIdx.x, hh_ = 4 * (bid & 7) + (bid >> 6), jj = (bid >> 3) & 7;
#define LANE_VARS() int tid = threadIdx.x; asm volatile("" : "+v"(tid)); const int lane = tid & 63, r32 = lane & 31, hi = lane >> 5, blk = (lane >> 4) & 1, \
        q4 = (lane & 15) >> 2, p4 = lane & 3; const unsigned vlin = (unsigned)lane * 16u; (void)r32; (void)hi; (void)blk; (void)q4; (void)p4; (void)vlin
    unsigned* const CNT = (unsigned*)(ws + WS_CNT);
    char* const KVh = ws + WS_KV + (size_t)hh_ * 32 * 32768;
    char* const A3 = ws + WS_A3;
    STAMP_DECL(); STAMP_RT(); STAMP();
    u32x4 pkv[16];
    const int bA = 15 - jj, bB = jj;
    const char* gX = (const char*)x + (size_t)((G ? bB : bA) * 128 + 32 * w4) * 1024; const char* gW = (const char*)wq + hh_ * 256; const char* gE = (const char*)wqkv + hh_ * 32;
    const unsigned lx = lds0 + wid * 4096, lw = lds0 + L_WE + wid * 1024;
    unsigned voffX, voffW, voffE;
    { const int lane = threadIdx.x & 63;
        { const int row = lane >> 3, ch = (lane & 7) ^ ((row >> 1) & 3); voffX = (unsigned)(row * 1024 + ch * 16); }
        { const int row = 4 * wid + (lane >> 4), j = lane & 15, c = (j >> 2) ^ (row & 3); voffW = (unsigned)(row * 8192 + c * 64 + (j & 3) * 16); }
        { const int row = 8 * wid + (lane >> 3), sl = lane & 7, hf = (sl >> 2) ^ ((row >> 1) & 1), part = hf ? 2 : ((sl >> 1) & 1);
          voffE = (unsigned)(row * 3072 + part * 1024 + (sl & 1) * 16); }
        if (wid == 0) glds16(gatew, (unsigned)lane * 16u, lds0 + L_GATE);
        PIN(voffX); PIN(voffW); PIN(voffE);
    }
#define XISS(c) do { _Pragma("unroll") for (int e = 0; e < 4; ++e) glds16(gX + e * 8192 + (c) * 128, voffX ^ ((e & 1) << 6), lx + ((c) & 1) * 32768 + e * 1024); } while (0)
#define WISS(c) do { glds16(gW + (size_t)(c) * 524288, voffW, lw + ((c) % 3) * 24576); glds16(gW + (size_t)(c) * 524288 + 262144, voffW, lw + ((c) % 3) * 24576 + 8192); \
                     glds16(gE + (size_t)(c) * 196608, voffE, lw + ((c) % 3) * 24576 + 16384); } while (0)
    {
        LANE_VARS();
        const int wr = wid >> 2, wc = wid & 3, fr = lane & 15, fq = lane >> 4;
        constexpr int HTB = 16384;
#define SA(b, h) (((b) * 2 + (h)) * HTB)
#define SB(b, h) ((4 + (b) * 2 + (h)) * HTB)
        unsigned voffA, voffB;
        { const int row = 8 * wid + (lane >> 3), ch = (lane & 7) ^ ((row >> 1) & 7); voffA = (unsigned)(row * 1024 + ch * 16); }
        { const int row = 4 * wid + (lane >> 4), j = lane & 15, s = ((row >> 3) & 1) * 4 + (row & 3), c = (j >> 1) ^ s; voffB = (unsigned)(row * 8192 + c * 32 + (j & 1) * 16); }
        const char* gA = (const char*)ckv + (size_t)jj * 256 * 1024;
        const char* gB0 = (const char*)wuk + hh_ * 256; const char* gB1 = (const char*)wuv + hh_ * 256;
        const unsigned ldsw = rfl(lds0 + wid * 1024);
#define STAGE_A(b, h, kt) do { const char* g_ = gA + (h) * 131072 + (kt) * 128; glds16(g_, voffA, ldsw + SA(b, h)); glds16(g_ + 65536, voffA, ldsw + SA(b, h) + 8192); } while (0)
#define STAGE_B(b, h, kt) do { const char* g_ = ((h) ? gB1 : gB0) + (size_t)(kt) * 524288; glds16(g_, voffB, ldsw + SB(b, h)); glds16(g_ + 262144, voffB, ldsw + SB(b, h) + 8192); } while (0)
        lds_cptr pa[2], pb[2];
#pragma unroll
        for (int ks = 0; ks < 2; ++ks) pa[ks] = lds + (wr * 64 + fr) * 128 + 16 * ((4 * ks + fq) ^ ((fr >> 1) & 7));
        { const int q = fr >> 2, p = fr & 3, s = (fq & 1) * 4 + q;
#pragma unroll
          for (int n = 0; n < 2; ++n) pb[n] = lds + (8 * fq + q) * 256 + 32 * ((2 * wc + n) ^ s) + 8 * p; }
#define LDA(dst, b, h) do { _Pragma("unroll") for (int m = 0; m < 4; ++m) _Pragma("unroll") for (int k = 0; k < 2; ++k) dst[m][k] = ldsr128(pa[k] + SA(b, h) + m * 2048); } while (0)
#define LDB(dst, b, h) do { _Pragma("unroll") for (int n = 0; n < 2; ++n) _Pragma("unroll") for (int k = 0; k < 2; ++k) \
        dst[n][k] = cat8(trrd(pb[n] + SB(b, h) + k * 8192), trrd(pb[n] + SB(b, h) + k * 8192 + 1024)); } while (0)
#define MMA(ai, bj, At, Bt) do { __builtin_amdgcn_s_setprio(1); _Pragma("unroll") for (int m = 0; m < 4; ++m) _Pragma("unroll") for (int n = 0; n < 2; ++n) _Pragma("unroll") for (int k = 0; k < 2; ++k) \
        acc[ai][bj][m][n] = (bj) ? MFMA16(At[m][k], Bt[n][k], acc[ai][bj][m][n]) : MFMA16(Bt[n][k], At[m][k], acc[ai][bj][m][n]); __builtin_amdgcn_s_setprio(0); } while (0)
        const unsigned scr = lds0 + 143360 + wid * 256; const size_t gw = (size_t)(bid * 8 + wid);
        auto warm = [&](int slot) {
            const unsigned vo = (unsigned)lane * 128u;
            if (slot == 0) { if (lane < 8) touch8k((const char*)x + gw * 1024, vo, scr); }
            else if (slot == 1) { if (lane < 16) touch8k((const char*)wq + gw * 2048, vo, scr); }
            else if (slot == 2) { if (lane < 6) touch8k((const char*)wqkv + gw * 768, vo, scr); }
            else if (slot == 3) { if (lane < 16) touch8k((const char*)wo_a + gw * 2048, vo, scr); }
            else if (slot == 4) { if (lane < 2) touch8k((const char*)wo_b + gw * 256, vo, scr); }
        };
        f32x4 acc[2][2][4][2] = {};
        bf16x8 At[4][2], B0[2][2], B1[2][2];
        constexpr int nt = 8;
        STAGE_B(0, 0, 0); STAGE_A(0, 0, 0); STAGE_B(0, 1, 0); STAGE_A(0, 1, 0);
        if (wr == 1) BAR();
        WAIT_V(4); BAR();
        STAGE_B(1, 0, 1); STAGE_A(1, 0, 1); STAGE_B(1, 1, 1);
        WAIT_V(6); BAR();
        STAMP();
        for (int t = 0; t < nt - 2; t += 2) {
            LDB(B0, 0, 0); SBAR(); LDA(At, 0, 0); STAGE_A(1, 1, t + 1);
            WAIT_L(8); BAR(); WAIT_L(0); MMA(0, 0, At, B0); BAR(); SBAR();
            LDB(B1, 0, 1); STAGE_B(0, 0, t + 2);
            BAR(); WAIT_L(0); MMA(0, 1, At, B1); BAR();
            LDA(At, 0, 1); STAGE_A(0, 0, t + 2);
            BAR(); WAIT_L(0); MMA(1, 0, At, B0); BAR(); SBAR();
            STAGE_B(0, 1, t + 2);
            WAIT_V(6); BAR(); MMA(1, 1, At, B1); BAR();
            warm(t);
            LDB(B0, 1, 0); SBAR(); LDA(At, 1, 0); STAGE_A(0, 1, t + 2);
            WAIT_L(8); BAR(); WAIT_L(0); MMA(0, 0, At, B0); BAR(); SBAR();
            LDB(B1, 1, 1); STAGE_B(1, 0, t + 3);
            BAR(); WAIT_L(0); MMA(0, 1, At, B1); BAR();
            LDA(At, 1, 1); STAGE_A(1, 0, t + 3);
            BAR(); WAIT_L(0); MMA(1, 0, At, B0); BAR(); SBAR();
            STAGE_B(1, 1, t + 3);
            WAIT_V(6); BAR(); MMA(1, 1, At, B1); BAR();
            warm(t + 1);
        }
        { LDB(B0, 0, 0); WAIT_V(0); LDA(At, 0, 0); STAGE_A(1, 1, nt - 1);
          BAR(); WAIT_L(0); MMA(0, 0, At, B0); BAR();
          LDB(B1, 0, 1); BAR(); WAIT_L(0); MMA(0, 1, At, B1); BAR();
          LDA(At, 0, 1); BAR(); WAIT_L(0); MMA(1, 0, At, B0); MMA(1, 1, At, B1); BAR(); }
        { LDB(B0, 1, 0); LDA(At, 1, 0); BAR(); WAIT_L(0); MMA(0, 0, At, B0); BAR();
          LDB(B1, 1, 1); WAIT_V(0);
          WISS(0); XISS(0);
          BAR(); WAIT_L(0); MMA(0, 1, At, B1); BAR();
          LDA(At, 1, 1); BAR(); WAIT_L(0); MMA(1, 0, At, B0); MMA(1, 1, At, B1); BAR(); }
        if (wr == 0) BAR();
        WISS(1); XISS(1);
        SBAR();
#undef SA
#undef SB
#undef STAGE_A
#undef STAGE_B
#undef LDA
#undef LDB
#undef MMA
#pragma unroll
        for (int ai = 0; ai < 2; ++ai) {
#pragma unroll
            for (int m = 0; m < 4; ++m) { const f32x4 a = acc[ai][0][m][0], b = acc[ai][0][m][1];
                pkv[ai * 4 + m] = (u32x4){cvtpk(a[0], a[1]), cvtpk(a[2], a[3]), cvtpk(b[0], b[1]), cvtpk(b[2], b[3])}; }
#pragma unroll
            for (int u = 0; u < 2; ++u)
#pragma unroll
                for (int n = 0; n < 2; ++n) { const f32x4 a = acc[ai][1][2 * u][n], b = acc[ai][1][2 * u + 1][n];
                    pkv[8 + ai * 4 + u * 2 + n] = (u32x4){cvtpk(a[0], a[1]), cvtpk(a[2], a[3]), cvtpk(b[0], b[1]), cvtpk(b[2], b[3])}; }
        }
    }
    STAMP();
    const char* kvw = KVh + w4 * 4096;
    const unsigned ldw = lds0 + w4 * 4096;
    char* const qbp = ws + WS_QB + (size_t)(bid * 4 + w4) * 8192;
    char* const pqp = ws + WS_PQ + (size_t)(bid * 8 + wid) * 2048;
    bf16x8 qf[8]; float gate;
    {
        LANE_VARS();
        u16 wph[8];
#pragma unroll
        for (int j = 0; j < 8; ++j) wph[j] = wphi[hh_ * 256 + (8 * (j >> 2) + 4 * hi + (j & 3)) * 16 + (lane & 15)];
        const char* tb0 = KVh + (size_t)(4 * jj + G) * 32768;
        const unsigned vok = (unsigned)((4 * w4 + (lane >> 4)) * 1024 + (lane & 15) * 16), vov = (unsigned)(16384 + (lane >> 4) * 2048 + (w4 * 32 + (lane & 15)) * 16);
#define KVST(i) do { if ((i) < 8) ST_SC1(tb0 + ((i) >> 2) * 65536, vok, ((i) & 3) * 256, pkv[i]); \
                     else ST_SC1(tb0 + (((i) >> 2) & 1) * 65536 + (((i) >> 1) & 1) * 8192, vov, ((i) & 1) * 256, pkv[i]); } while (0)
        f32x16 qa[5] = {}, d1 = {};
        unsigned xo[2], wo_[4];
#pragma unroll
        for (int s = 0; s < 2; ++s) xo[s] = (unsigned)((32 * wid + r32) * 128 + 16 * ((2 * s + hi) ^ ((r32 >> 1) & 7)));
#pragma unroll
        for (int c = 0; c < 4; ++c) wo_[c] = (unsigned)(L_WE + (8 * hi + q4) * 256 + 64 * (c ^ q4) + 32 * (p4 >> 1) + 16 * blk + 8 * (p4 & 1));
        const unsigned eo = (unsigned)(L_WE + 16384 + (8 * hi + q4) * 128 + 64 * ((q4 >> 1) & 1) + 32 * blk + 8 * p4);
        const unsigned vo_ = (unsigned)(L_WE + 16384 + (8 * hi + q4) * 128 + 64 * (((q4 >> 1) & 1) ^ 1) + 8 * p4);
        bf16x8 fx[2], fw[2][6], fg[2];
#define QR_(buf, c, ks, u) do { \
        if ((u) == 0) { fx[buf] = ldsr128(lds + ((c) & 1) * 32768 + (xo[(ks) & 1] ^ (((ks) >> 1) << 6))); fg[buf] = ldsr128(lds + L_GATE + ((c) * 64 + (ks) * 16 + 8 * hi) * 2); } \
        else if ((u) < 5) { fw[buf][(u) - 1] = cat8(trrd(lds + wo_[(u) - 1] + ((c) % 3) * 24576 + (ks) * 4096), trrd(lds + wo_[(u) - 1] + ((c) % 3) * 24576 + (ks) * 4096 + 1024)); } \
        else { fw[buf][4] = cat8(trrd(lds + eo + ((c) % 3) * 24576 + (ks) * 2048), trrd(lds + eo + ((c) % 3) * 24576 + (ks) * 2048 + 512)); \
               fw[buf][5] = cat8(trrd(lds + vo_ + ((c) % 3) * 24576 + (ks) * 2048), trrd(lds + vo_ + ((c) % 3) * 24576 + (ks) * 2048 + 512)); } } while (0)
#define QM_(buf, u) do { if ((u) < 5) qa[u] = MFMA32(fw[buf][u], fx[buf], qa[u]); else d1 = MFMA32(fx[buf], blk ? fg[buf] : fw[buf][5], d1); } while (0)
#define QRD(buf, c, ks) do { _Pragma("unroll") for (int u_ = 0; u_ < 6; ++u_) QR_(buf, c, ks, u_); } while (0)
#define QSTEP(buf, c, ks, rd) do { _Pragma("unroll") for (int u_ = 0; u_ < 6; ++u_) { if (rd) QR_((buf) ^ 1, c, ks, u_); QM_(buf, u_); SBAR(); } } while (0)
#define WV_(n) asm volatile("s_waitcnt vmcnt(%0)" :: "i"(n) : "memory")
#define NST(c) ((c) < 0 ? 0 : (c) < 4 ? 3 : (c) < 6 ? 2 : 0)
#define ST0(c) ((c) < 4 ? 3 * (c) : 12 + 2 * ((c) - 4))
#define QCHUNK(c) do { \
        SBAR(); QSTEP(0, c, 1, true); QSTEP(1, c, 2, true); \
        if ((c) < 7) { WV_(4 + NST((c) - 1)); } BAR(); asm volatile("" ::: "memory"); \
        if ((c) == 7) { if (tid == 0) __hip_atomic_fetch_add(CNT + hh_ * 64, 1u, RLX_AGENT); } \
        if ((c) < 6) WISS((c) + 2); \
        _Pragma("unroll") for (int i_ = 0; i_ < NST(c); ++i_) KVST(ST0(c) + i_); \
        SBAR(); QSTEP(0, c, 3, true); \
        if ((c) < 7) { WV_(((c) < 6 ? 3 : 0) + NST(c)); } \
        SBAR(); QSTEP(1, (c) + 1, 0, (c) < 7); \
        if ((c) < 6) XISS((c) + 2); } while (0)
        asm volatile("s_waitcnt vmcnt(7)\n\ts_barrier" ::: "memory");
        bf16x8 wpf;
#pragma unroll
        for (int j = 0; j < 8; ++j) wpf[j] = (short)(blk ? (wph[j] ^ 0x8000) : wph[j]);
        PIN(wpf);
        STAMP();
        QRD(0, 0, 0);
        QCHUNK(0); QCHUNK(1); QCHUNK(2); QCHUNK(3); QCHUNK(4); QCHUNK(5); QCHUNK(6); QCHUNK(7);
#undef QRD
#undef QR_
#undef QM_
#undef QSTEP
#undef QCHUNK
#undef XISS
#undef WISS
#undef KVST
#undef NST
#undef ST0
        STAMP();
        BAR(); asm volatile("" ::: "memory");
        unsigned f1 = __hip_atomic_load(CNT + hh_ * 64, RLX_AGENT);
#pragma unroll
        for (int cc = 0; cc < 4; ++cc) {
#pragma unroll
            for (int r = 0; r < 16; ++r) qa[cc][r] *= QSCALE;
            qf[2 * cc] = PACK8(qa[cc], 0); qf[2 * cc + 1] = PACK8(qa[cc], 8);
        }
        if (rfl(f1) < 8u) wait_cnt(CNT + hh_ * 64, 8u);
        asm volatile("" ::: "memory");
        STAMP();
#pragma unroll
        for (int e = 0; e < 4; ++e) glds16c(kvw + (size_t)(e >> 1) * 32768 + (2 * G + (e & 1)) * 1024, vlin, ldw + (e >> 1) * 16384 + (2 * G + (e & 1)) * 1024);
        LAS float* red = (LAS float*)(lds + L_V + G * 32768 + w4 * 4096);
        if (r32 == 16) {
#pragma unroll
            for (int r = 0; r < 16; ++r) red[(r & 3) + 8 * (r >> 2) + 4 * hi] = d1[r];
        }
        WAIT_L(0);
        { const float gz = bfround(red[r32]); gate = bfround(1.f / (1.f + __expf(-gz))); }
        f32x16 pq = MFMA32(wpf, PACK8(qa[4], 0), f32x16{});
        f32x16 pk = MFMA32(wpf, PACK8(qa[4], 8), f32x16{});
        dual_softmax(pq); dual_softmax(pk);
        f32x16 d3;
        { bf16x8 id0, id1;
#pragma unroll
          for (int j = 0; j < 8; ++j) { const int f = 8 * (j >> 2) + 4 * hi + (j & 3); id0[j] = (short)(r32 == f ? 0x3F80 : 0); id1[j] = (short)(r32 == f + 16 ? 0x3F80 : 0); }
          d3 = MFMA32(PACK8(pk, 0), id0, f32x16{}); d3 = MFMA32(PACK8(pk, 8), id1, d3); }
        { const bf16x8 t0 = PACK8(pq, 0), t1 = PACK8(pq, 8); ST_PL(pqp, vlin, 0, t0); ST_PL(pqp, vlin, 1024, t1); }
        if (G == 0) {
#pragma unroll
            for (int s = 0; s < 8; ++s) ST_PL(qbp + (s >> 2) * 4096, vlin, (s & 3) * 1024, qf[s]);
        }
        const bf16x8 ones = {0x3F80, 0x3F80, 0x3F80, 0x3F80, 0x3F80, 0x3F80, 0x3F80, 0x3F80};
        const bf16x8 v0 = PACK8(d1, 0), v1 = PACK8(d1, 8);
        f32x16 d4 = MFMA32(PACK8(d3, 0), blk ? ones : v0, f32x16{});
        d4 = MFMA32(PACK8(d3, 8), blk ? ones : v1, d4);
#pragma unroll
        for (int r = 0; r < 16; ++r) red[((r & 3) + 8 * (r >> 2) + 4 * hi) * 32 + r32] = d4[r];
        WAIT_L(0); BAR(); asm volatile("" ::: "memory");
        if (tid < 256) {
            f32x4 s = {0.f, 0.f, 0.f, 0.f};
#pragma unroll
            for (int w = 0; w < 8; ++w) s += *(const LAS f32x4*)(lds + L_V + (w >> 2) * 32768 + (w & 3) * 4096 + tid * 16);
            ST_SC1(ws + WS_KVP + (size_t)(hh_ * 8 + jj) * 4096, (unsigned)tid * 16u, 0, s);
        }
        if (G) { WAIT_BAR(2); } else { WAIT_BAR(11); }
    }
    STAMP();
    {
    LANE_VARS();
    const int prow = (r32 & 7) | ((r32 & 8) << 1) | ((r32 & 16) >> 1);
    const unsigned kro = (unsigned)(prow * 16 + hi * 1024), vro = (unsigned)(L_V + r32 * 16 + hi * 2048);
    const int dq0 = 32 * w4;
    const int T = 2 * jj + 2;
    unsigned ks0 = 0u, ks1 = 16384u, ks2 = 32768u;
    int rho = 0; const int TG = G ? T : 99;
    f32x16 o[4], negm, sA0, sA1, sB0, sB1; bf16x8 kf[16], vf[16]; u32x4 pw[4]; float m, l;
#define KRD(i, kp) kf[i] = ldsr128((kp) + ((i) >> 1) * 2048 + ((i) & 1) * 512)
#define VRD(i, vp) vf[i] = ldsr128((vp) + ((i) >> 2) * 4096 + ((i) & 3) * 512)
#define PF(k) __builtin_bit_cast(bf16x8, pw[k])
#define DMA1(i) do { if ((i) < 2) glds16c(gk_ + (i) * 1024, vlin, dk_ + (i) * 1024); else if ((i) < 4) glds16c_unless(gk_ + (i) * 1024, vlin, dk_ + (i) * 1024, rk_, T); \
                     else if ((i) < 6) glds16c(gv_ + ((i) - 4) * 1024, vlin, dv_ + ((i) - 4) * 1024); else glds16c_unless(gv_ + ((i) - 4) * 1024, vlin, dv_ + ((i) - 4) * 1024, rho, T); } while (0)
    const char* gk_; const char* gv_; unsigned dk_, dv_, ko_, vo_; int rk_;
    unsigned go1, go2;
#define GOFF(r) ((r) < T ? (unsigned)(r) * 32768u + G * 2048u : (unsigned)(2 * (r) - T + G) * 32768u)
#define PREP_A(r, k0) do { ko_ = ((r) >= TG ? 49152u : 0u) + (k0); vo_ = ((r) - 1 >= TG ? 32768u : 0u) + (((r) - 1) & 1) * 16384u; } while (0)
#define PREP_B(r, gcur) do { gv_ = kvw + ((gcur) + 16384u); dv_ = ldw + L_V + ((r) & 1) * 16384u + ((r) < T ? G * 2048u : G * 32768u); } while (0)
#define PREP_C(r) do { rk_ = (r) + 2 <= 16 ? (r) + 2 : 16; go1 = go2; go2 = GOFF(rk_); } while (0)
#define PREP_D(k2) do { gk_ = kvw + go2; dk_ = ldw + (k2) + (rk_ < T ? G * 2048u : G * 49152u); } while (0)
#define ROT() do { const unsigned t_ = ks0; ks0 = ks1; ks1 = ks2; ks2 = t_; ++rho; } while (0)
#define GAPA(i, C0, C1, P0, P1) do { \
        if ((i) + 4 < 16) { KRD((i) + 4, kp_); } else { VRD((i) - 12, vp_); } \
        if (((i) & 1) == 0) DMA1((i) >> 1); \
        if ((i) & 1) C1 = MFMA32(kf[i], qf[(i) >> 1], (i) < 2 ? negm : C1); else C0 = MFMA32(kf[i], qf[(i) >> 1], (i) < 2 ? negm : C0); \
        if ((i) < 8) { P1[2 * (i)] = EX2(P1[2 * (i)]); P1[2 * (i) + 1] = EX2(P1[2 * (i) + 1]); pw[(i) >> 2][(i) & 3] = cvtpk(P0[2 * (i)], P0[2 * (i) + 1]); PIN(P1); PIN(pw[(i) >> 2]); } \
        else { l += P1[2 * (i) - 16]; l2 += P1[2 * (i) - 15]; pw[(i) >> 2][(i) & 3] = cvtpk(P1[2 * (i) - 16], P1[2 * (i) - 15]); PIN(l); PIN(l2); PIN(pw[(i) >> 2]); } \
        SBAR(); } while (0)
#define PVM(i) o[(i) & 3] = MFMA32(vf[i], PF((i) >> 2), o[(i) & 3])
#define RDB(i) do { if ((i) + 4 < 16) { VRD((i) + 4, vp_); } else { KRD((i) - 12, kn_); } } while (0)
#define ROUND(C0, C1, P0, P1) do { \
        const lds_cptr kp_ = lds + ko_ + kro; const lds_cptr vp_ = lds + vo_ + vro; \
        float l2 = 0.f; lds_cptr kn_ = kp_; SBAR(); \
        GAPA(0, C0, C1, P0, P1); GAPA(1, C0, C1, P0, P1); GAPA(2, C0, C1, P0, P1); GAPA(3, C0, C1, P0, P1); \
        GAPA(4, C0, C1, P0, P1); GAPA(5, C0, C1, P0, P1); GAPA(6, C0, C1, P0, P1); GAPA(7, C0, C1, P0, P1); \
        GAPA(8, C0, C1, P0, P1); GAPA(9, C0, C1, P0, P1); GAPA(10, C0, C1, P0, P1); GAPA(11, C0, C1, P0, P1); \
        GAPA(12, C0, C1, P0, P1); GAPA(13, C0, C1, P0, P1); GAPA(14, C0, C1, P0, P1); GAPA(15, C0, C1, P0, P1); \
        l += l2; \
        ldq_unless(qf, qbp, vlin, (unsigned)(rho + 1 - TG));        \
        RDB(0); PVM(0); SBAR(); RDB(1); PVM(1); SBAR(); \
        if (__builtin_expect(rho == 16 || (rho >= TG - 2 && rho < TG), 0)) mask_tile(C0, C1, dq0 - (rho == 16 ? 64 * G : 64 * (rho - TG + 2))); \
        float ma, mb; \
        RDB(2); PVM(2); ma = MX3(C0[0], C0[1], C1[0]); mb = MX3(C0[2], C0[3], C1[1]); ma = MX3(ma, C1[2], C1[3]); mb = MX3(mb, C0[4], C0[5]); PIN(ma); PIN(mb); SBAR(); \
        RDB(3); PVM(3); ma = MX3(ma, C0[6], C0[7]); mb = MX3(mb, C1[4], C1[5]); ma = MX3(ma, C1[6], C1[7]); mb = MX3(mb, C0[8], C0[9]); PIN(ma); PIN(mb); SBAR(); \
        RDB(4); PVM(4); ma = MX3(ma, C0[10], C0[11]); mb = MX3(mb, C1[8], C1[9]); ma = MX3(ma, C1[10], C1[11]); mb = MX3(mb, C0[12], C0[13]); PIN(ma); PIN(mb); SBAR(); \
        RDB(5); PVM(5); ma = MX3(ma, C0[14], C0[15]); mb = MX3(mb, C1[12], C1[13]); ma = MX3(ma, C1[14], C1[15]); \
        const float rm = swap32_max(fmaxf(ma, mb)); bool pend = false; float fsc = 1.f; \
        if (__builtin_expect(__any(rm > THR) || rho == TG, 0)) { float dl; pend = true; \
            if (rho == TG) { dl = fmaxf(rm, -1e30f); m = dl; fsc = gate / swap32_add(l); l = 0.f; }        \
            else { dl = fmaxf(rm, 0.f); m += dl; fsc = EX2(-dl); l *= fsc; } \
            _Pragma("unroll") for (int e = 0; e < 16; ++e) { C0[e] -= dl; C1[e] -= dl; negm[e] = -m; } } \
        SBAR(); \
        RDB(6); PVM(6); C0[0] = EX2(C0[0]); C0[1] = EX2(C0[1]); PIN(C0); SBAR(); \
        RDB(7); PVM(7); C0[2] = EX2(C0[2]); C0[3] = EX2(C0[3]); l += C0[0]; l2 = C0[1]; PIN(C0); PIN(l); SBAR(); \
        RDB(8); PVM(8); PREP_A(rho + 1, ks1); kn_ = lds + ko_ + kro; C0[4] = EX2(C0[4]); C0[5] = EX2(C0[5]); l += C0[2]; l2 += C0[3]; PIN(C0); PIN(l); PIN(l2); SBAR(); \
        RDB(9); PVM(9); PREP_B(rho + 1, go1); C0[6] = EX2(C0[6]); C0[7] = EX2(C0[7]); l += C0[4]; l2 += C0[5]; PIN(C0); PIN(l); PIN(l2); SBAR(); \
        RDB(10); PVM(10); PREP_C(rho + 1); C0[8] = EX2(C0[8]); C0[9] = EX2(C0[9]); l += C0[6]; l2 += C0[7]; PIN(C0); PIN(l); PIN(l2); SBAR(); \
        RDB(11); PVM(11); PREP_D(ks0); C0[10] = EX2(C0[10]); C0[11] = EX2(C0[11]); l += C0[8]; l2 += C0[9]; PIN(C0); PIN(l); PIN(l2); SBAR(); \
        RDB(12); PVM(12); C0[12] = EX2(C0[12]); C0[13] = EX2(C0[13]); l += C0[10]; l2 += C0[11]; PIN(C0); PIN(l); PIN(l2); SBAR(); \
        RDB(13); PVM(13); C0[14] = EX2(C0[14]); C0[15] = EX2(C0[15]); l += C0[12]; l2 += C0[13]; PIN(C0); PIN(l); PIN(l2); SBAR(); \
        RDB(14); PVM(14); l += C0[14]; l2 += C0[15]; PIN(l); PIN(l2); SBAR(); \
        RDB(15); PVM(15); l += l2; PIN(l); SBAR(); \
        if (__builtin_expect(pend || rho + 1 == TG, 0)) { \
            if (pend) { _Pragma("unroll") for (int c = 0; c < 4; ++c) _Pragma("unroll") for (int e = 0; e < 16; ++e) o[c][e] *= fsc; } \
            if (rho + 1 == TG) negm = f32x16{}; \
            if (rho == TG) { const int ln = fresh_lane(); const unsigned vod = (unsigned)((ln >> 5) * 2048 + (32 * w4 + (ln & 31)) * 16); const char* dst = A3 + (size_t)(bB * 576 + hh_ * 16) * 2048; \
                _Pragma("unroll") for (int c = 0; c < 4; ++c) { const bf16x8 t0 = PACK8(o[c], 0), t1 = PACK8(o[c], 8); ST_SC1(dst + c * 8192, vod, 0, t0); ST_SC1(dst + c * 8192 + 4096, vod, 0, t1); o[c] = f32x16{}; } } } \
        asm volatile("s_cmp_eq_u32 %0, %1\n\ts_cbranch_scc1 .Lw%=\n\ts_waitcnt vmcnt(0)\n.Lw%=:\n\ts_waitcnt vmcnt(8)\n\ts_barrier" :: "s"(rho), "s"(TG) : "memory", "scc");     \
        if (__builtin_expect(rho == TG + 1 && w4 == 0, 0)) { if (fresh_lane() == 0) __hip_atomic_fetch_or(CNT + (64 + bB) * 64, 1u << hh_, RLX_AGENT); }        \
        ROT(); } while (0)

#define ROUND0(C0, C1) do { \
        const lds_cptr kp_ = lds + ks0 + kro; const lds_cptr kn_ = lds + ks1 + kro; \
        go2 = GOFF(1); PREP_B(0, GOFF(0)); PREP_C(0); PREP_D(ks2); \
        _Pragma("unroll") for (int i = 0; i < 6; ++i) KRD(i, kp_); \
        _Pragma("unroll") for (int i = 0; i < 8; ++i) DMA1(i); \
        _Pragma("unroll") for (int c = 0; c < 4; ++c) o[c] = f32x16{}; \
        PREP_A(1, ks1); PREP_B(1, go1); PREP_C(1); PREP_D(ks0); SBAR(); \
        _Pragma("unroll") for (int i = 0; i < 16; ++i) { \
            if (i + 6 < 16) KRD(i + 6, kp_); \
            if (i & 1) C1 = MFMA32(kf[i], qf[i >> 1], i < 2 ? f32x16{} : C1); else C0 = MFMA32(kf[i], qf[i >> 1], i < 2 ? f32x16{} : C0); \
            SBAR(); } \
        if (TG == 2) mask_tile(C0, C1, dq0); \
        float ma = MX3(C0[0], C0[1], C1[0]), mb = MX3(C0[2], C0[3], C1[1]); ma = MX3(ma, C1[2], C1[3]); \
        _Pragma("unroll") for (int r = 4; r < 16; r += 4) { ma = MX3(ma, C0[r], C0[r + 1]); mb = MX3(mb, C0[r + 2], C0[r + 3]); ma = MX3(ma, C1[r], C1[r + 1]); mb = MX3(mb, C1[r + 2], C1[r + 3]); } \
        m = fmaxf(swap32_max(fmaxf(ma, mb)), -1e30f); l = 0.f; \
        _Pragma("unroll") for (int i = 0; i < 4; ++i) KRD(i, kn_); \
        _Pragma("unroll") for (int e = 0; e < 16; ++e) { C0[e] = EX2(C0[e] - m); l += C0[e]; C1[e] -= m; negm[e] = -m; } \
        asm volatile("s_waitcnt vmcnt(0)\n\ts_barrier" ::: "memory"); ROT(); } while (0)
#define DRAIN(P0, P1, MID) do { \
        const lds_cptr vp_ = lds + G * 32768 + vro; \
        _Pragma("unroll") for (int i = 0; i < 8; ++i) VRD(i, vp_); \
        _Pragma("unroll") for (int i = 0; i < 8; ++i) pw[i >> 2][i & 3] = cvtpk(P0[2 * i], P0[2 * i + 1]); \
        SBAR(); \
        _Pragma("unroll") for (int i = 0; i < 16; ++i) { \
            if (i + 8 < 16) VRD(i + 8, vp_); \
            if (i == 8) { MID } \
            PVM(i); \
            if (i < 8) { P1[2 * i] = EX2(P1[2 * i]); P1[2 * i + 1] = EX2(P1[2 * i + 1]); PIN(P1); } \
            if (i >= 1 && i < 9) { l += P1[2 * i - 2]; l += P1[2 * i - 1]; pw[2 + ((i - 1) >> 2)][(i - 1) & 3] = cvtpk(P1[2 * i - 2], P1[2 * i - 1]); PIN(l); PIN(pw[2 + ((i - 1) >> 2)]); } \
            SBAR(); } \
        l = swap32_add(l); } while (0)
    ROUND0(sA0, sA1);
    if (wid == 0 && fresh_lane() == 0) __hip_atomic_fetch_add(CNT + (32 + hh_) * 64, 1u, RLX_AGENT);
    STAMP();
#pragma clang loop unroll(disable)
    for (int pr = 0; pr < 8; ++pr) { ROUND(sB0, sB1, sA0, sA1); ROUND(sA0, sA1, sB0, sB1); }
    STAMP();
    {
        f32x2 ps[8]; bf16x8 phq[2];
        LANE_VARS();
        const unsigned f2 = __hip_atomic_load(CNT + (32 + hh_) * 64, RLX_AGENT);
        DRAIN(sA0, sA1,
            if (rfl(f2) < 8u) wait_cnt(CNT + (32 + hh_) * 64, 8u);
            _Pragma("unroll") for (int w = 0; w < 8; ++w) ps[w] = __builtin_bit_cast(f32x2, __hip_atomic_load((const unsigned long long*)(ws + WS_KVP + (size_t)(hh_ * 8) * 4096 + (unsigned)(w * 4096 + tid * 8)), RLX_AGENT));
            phq[0] = *(const bf16x8*)(pqp + vlin); phq[1] = *(const bf16x8*)(pqp + (vlin + 1024)); );
        const unsigned vod = (unsigned)(hi * 2048 + (32 * w4 + r32) * 16);
        BAR(); asm volatile("" ::: "memory");
        const unsigned st0 = ks2;
        { const lds_ptr xp = lds + L_V + wid * 8192 + lane * 16;
#pragma unroll
          for (int c = 0; c < 2; ++c)
#pragma unroll
              for (int g = 0; g < 4; ++g) { f32x4 t;
#pragma unroll
                  for (int e = 0; e < 4; ++e) t[e] = G ? o[c][4 * g + e] : o[2 + c][4 * g + e];
                  *(LAS f32x4*)(xp + (c * 4 + g) * 1024) = t; }
          *(LAS f32x4*)(lds + st0 + wid * 1024 + lane * 16) = (f32x4){m, l, gate, 0.f};
          f32x2 s = ps[0];
#pragma unroll
          for (int w = 1; w < 8; ++w) s += ps[w];
          *(LAS f32x2*)(lds + st0 + 8192 + tid * 8) = s; }
        WAIT_L(0); BAR(); asm volatile("" ::: "memory");
        { const int ow = wid ^ 4;
          const f32x4 st = *(const LAS f32x4*)(lds + st0 + ow * 1024 + lane * 16);
          const float mn = fmaxf(m, st[0]), f0 = EX2(m - mn), f1 = EX2(st[0] - mn);
          const float gg = G ? st[2] : gate;
          const float inv = gg / (l * f0 + st[1] * f1); const float c0 = f0 * inv, c1 = f1 * inv;
          const lds_cptr xp = lds + L_V + ow * 8192 + lane * 16;
          const char* dst = A3 + (size_t)(bA * 576 + hh_ * 16 + G * 8) * 2048;
#pragma unroll
          for (int c = 0; c < 2; ++c) {
              float v[16];
#pragma unroll
              for (int g = 0; g < 4; ++g) { const f32x4 t = *(const LAS f32x4*)(xp + (c * 4 + g) * 1024);
#pragma unroll
                  for (int e = 0; e < 4; ++e) v[4 * g + e] = (G ? o[2 + c][4 * g + e] : o[c][4 * g + e]) * c0 + t[e] * c1; }
              { const bf16x8 t0 = PACK8(v, 0), t1 = PACK8(v, 8); ST_SC1(dst + c * 8192, vod, 0, t0); ST_SC1(dst + c * 8192 + 4096, vod, 0, t1); }
          }
          float kvv[2][8];
          const int col = r32 < 16 ? r32 : 16;
#pragma unroll
          for (int s = 0; s < 2; ++s)
#pragma unroll
              for (int j = 0; j < 8; ++j) { const int f = 16 * s + 8 * (j >> 2) + 4 * hi + (j & 3); const float v = *(const LAS float*)(lds + st0 + 8192 + (f * 32 + col) * 4); kvv[s][j] = r32 <= 16 ? v : 0.f; }
          f32x16 nm = MFMA32(PACK8(kvv[0], 0), phq[0], f32x16{});
          nm = MFMA32(PACK8(kvv[1], 0), phq[1], nm);
          const float den = bfround(bfround(lo32_bcast(nm[8])) + 1e-6f); const float sc = (1.f - gate) / den;
          float ov[8];
#pragma unroll
          for (int r = 0; r < 8; ++r) ov[r] = bfround(nm[r]) * sc;
          const bf16x8 t0 = PACK8(ov, 0);
          ST_SC1(A3 + (size_t)((G ? bB : bA) * 576 + 512 + 2 * hh_) * 2048, vod, 0, t0);
        }
        WAIT_L(0); BAR(); asm volatile("" ::: "memory");
        STAMP();
    }
#undef KRD
#undef VRD
#undef PF
#undef DMA1
#undef ROT
#undef GOFF
#undef PREP_A
#undef PREP_B
#undef PREP_C
#undef PREP_D
#undef GAPA
#undef PVM
#undef RDB
#undef ROUND
#undef ROUND0
#undef DRAIN
    }
    {
        LANE_VARS();
        const int rt = 4 * (bid & 7) + (bid >> 6), ct = (bid >> 3) & 7;
        unsigned voffW; { const int row = lane >> 3, sl = lane & 7, nb = (sl >> 2) ^ ((row >> 1) & 1); voffW = (unsigned)(row * 1024 + nb * 64 + (sl & 3) * 16); }
        const char* gA = A3 + (size_t)((rt >> 1) * 576 + 2 * wid) * 2048 + (rt & 1) * 1024;
        const char* gWa = (const char*)wo_a + (size_t)wid * 16384 + ct * 128; const char* gWb = (const char*)wo_b + (size_t)wid * 16384 + ct * 128;
        const unsigned wb = lds0 + wid * 20480;
        const unsigned* rdy = CNT + (64 + (rt >> 1)) * 64;
        auto issue = [&](int u, unsigned slot) {
            const char* ga = gA + (size_t)u * 32768; const char* gw = u < 32 ? gWa + (size_t)u * 131072 : gWb + (size_t)(u - 32) * 131072;
            glds16c(ga, vlin, wb + slot); glds16c(ga + 2048, vlin, wb + slot + 1024); glds16(gw, voffW, wb + slot + 2048); glds16(gw + 8192, voffW, wb + slot + 3072); };
        unsigned seen = 0u; int hnext = 0;
        auto next_head = [&]() -> int {
            if (hnext >= 32) { if (hnext == 32) { unsigned spins = 0; while (rfl(__hip_atomic_load(rdy + 1, RLX_AGENT)) != ~0u) { __builtin_amdgcn_s_sleep(2); if (++spins > (1u << 20)) break; }
                                             asm volatile("" ::: "memory"); } }
            else if (!((seen >> hnext) & 1u)) { unsigned spins = 0;
                for (;;) { seen = rfl(__hip_atomic_load(rdy, RLX_AGENT)); if (((seen >> hnext) & 1u) || ++spins > (1u << 20)) break; __builtin_amdgcn_s_sleep(2); }
                asm volatile("" ::: "memory"); }
            return hnext++; };
        asm volatile("s_waitcnt vmcnt(0)\n\ts_barrier" ::: "memory");
        if (tid == 0) { __hip_atomic_fetch_or(CNT + (64 + bA) * 64, 1u << hh_, RLX_AGENT); __hip_atomic_fetch_or(CNT + (64 + bB) * 64, 1u << hh_, RLX_AGENT);
                        __hip_atomic_fetch_or(CNT + (64 + bA) * 64 + 1, 1u << hh_, RLX_AGENT); __hip_atomic_fetch_or(CNT + (64 + bB) * 64 + 1, 1u << hh_, RLX_AGENT); }
        STAMP();
#pragma unroll
        for (int i = 0; i < 5; ++i) issue(next_head(), i * 4096u);
        f32x16 acc[2][2] = {};
        const unsigned ao = (unsigned)(wid * 20480 + hi * 1024 + r32 * 16);
        unsigned bo[2];
#pragma unroll
        for (int nb = 0; nb < 2; ++nb) bo[nb] = (unsigned)(wid * 20480 + 2048 + (4 * hi + q4) * 128 + 64 * (nb ^ ((q4 >> 1) & 1)) + 32 * blk + 8 * p4);
        bf16x8 af[2][2], bf[2][2];
#define K3RD(buf, sb) do { af[buf][0] = ldsr128((sb) + ao); bf[buf][0] = cat8(trrd((sb) + bo[0]), trrd((sb) + bo[0] + 1024)); \
        af[buf][1] = ldsr128((sb) + ao + 512); bf[buf][1] = cat8(trrd((sb) + bo[1]), trrd((sb) + bo[1] + 1024)); } while (0)
#define K3BODY(buf, i) do { \
        const unsigned snx = sc == 16384u ? 0u : sc + 4096u; \
        if ((i) + 1 < 36) K3RD(buf ^ 1, lds + snx); \
        SBAR(); acc[0][0] = MFMA32(bf[buf][0], af[buf][0], acc[0][0]); acc[1][0] = MFMA32(bf[buf][0], af[buf][1], acc[1][0]); SBAR(); \
        if ((i) + 5 < 36) issue(next_head(), sc); \
        SBAR(); acc[0][1] = MFMA32(bf[buf][1], af[buf][0], acc[0][1]); acc[1][1] = MFMA32(bf[buf][1], af[buf][1], acc[1][1]); SBAR(); \
        sc = snx; } while (0)
#define K3W(n) asm volatile("s_waitcnt vmcnt(" #n ") lgkmcnt(0)" ::: "memory")
        unsigned sc = 0u;
        WAIT_V(16);
        K3RD(0, lds);
        for (int i = 0; i < 32; i += 2) {
            if (G) __builtin_amdgcn_s_setprio(1); K3W(12); K3BODY(0, i);
            if (G) __builtin_amdgcn_s_setprio(0); K3W(12); K3BODY(1, i + 1); }
        K3W(8); K3BODY(0, 32); K3W(4); K3BODY(1, 33); K3W(0); K3BODY(0, 34); K3W(0); K3BODY(1, 35);
#undef K3RD
#undef K3BODY
#undef K3W
        STAMP();
        BAR(); asm volatile("" ::: "memory");
#pragma unroll
        for (int mi = 0; mi < 2; ++mi)
#pragma unroll
            for (int nb = 0; nb < 2; ++nb)
#pragma unroll
                for (int g = 0; g < 4; ++g) { const int row = 32 * mi + r32, ch = 8 * nb + 2 * g + hi;
                    *(LAS f32x4*)(lds + wid * 16384 + row * 256 + 16 * (ch ^ (row & 15))) = (f32x4){acc[mi][nb][4 * g], acc[mi][nb][4 * g + 1], acc[mi][nb][4 * g + 2], acc[mi][nb][4 * g + 3]}; }
        WAIT_L(0); BAR(); asm volatile("" ::: "memory");
        { const int row = tid >> 3, c8 = tid & 7; f32x4 s0 = {}, s1 = {};
#pragma unroll
          for (int w = 0; w < 8; ++w) { s0 += *(const LAS f32x4*)(lds + w * 16384 + row * 256 + 16 * ((2 * c8) ^ (row & 15)));
                                        s1 += *(const LAS f32x4*)(lds + w * 16384 + row * 256 + 16 * ((2 * c8 + 1) ^ (row & 15))); }
          u32x4 w = {cvtpk(s0[0], s0[1]), cvtpk(s0[2], s0[3]), cvtpk(s1[0], s1[1]), cvtpk(s1[2], s1[3])};
          *(u32x4*)(out + (size_t)(rt * 64 + row) * 512 + ct * 64 + c8 * 8) = w; }
    }
    STAMP(); STAMP_RT();
    STAMP_OUT(ws + WS_END);
}

extern "C" void kernel_launch(void* const* d_in, const int* in_sizes, int n_in, void* d_out, int out_size, void* d_ws, size_t ws_size,
                              hipStream_t stream) {
    (void)in_sizes; (void)n_in; (void)out_size; (void)ws_size;
    char* ws = (char*)d_ws;
    (void)hipFuncSetAttribute((const void*)kf_all, hipFuncAttributeMaxDynamicSharedMemorySize, LDS_BYTES);
    (void)hipMemsetAsync(ws + WS_CNT, 0, CNT_BYTES, stream);
    kf_all<<<256, 512, LDS_BYTES, stream>>>((const u16*)d_in[0], (const u16*)d_in[1], (const u16*)d_in[2], (const u16*)d_in[3], (const u16*)d_in[4], (const u16*)d_in[5],
                                            (const u16*)d_in[7], (const u16*)d_in[8], (const u16*)d_in[9], (const u16*)d_in[10], ws, (u16*)d_out);
}
```

```cpp
#include <hip/hip_runtime.h>
#include <hip/hip_bf16.h>
#include <stdint.h>
#define PREFETCH 1

typedef unsigned short u16;
typedef short bf16x8 __attribute__((ext_vector_type(8)));
typedef short s16x4 __attribute__((ext_vector_type(4)));
typedef float f32x2 __attribute__((ext_vector_type(2)));
typedef float f32x4 __attribute__((ext_vector_type(4)));
typedef float f32x16 __attribute__((ext_vector_type(16)));
typedef unsigned u32x4 __attribute__((ext_vector_type(4)));
typedef unsigned u32x2 __attribute__((ext_vector_type(2)));
typedef __bf16 bf16x2_t __attribute__((ext_vector_type(2)));
#define LAS __attribute__((address_space(3)))
#define DI __device__ __forceinline__
typedef LAS const char* lds_cptr;
typedef LAS char* lds_ptr;

constexpr int SEQ = 2048, DM = 512, NH = 32, HD = 128;
constexpr size_t WS_CNT = 0;
constexpr size_t WS_HQ = 32768, WS_HR = 33792;
constexpr size_t WS_KV = 65536;
constexpr size_t WS_A3 = WS_KV + (size_t)NH * 32 * 32768;
constexpr size_t WS_KVP = WS_A3 + (size_t)576 * SEQ * 16;
constexpr size_t WS_QB = WS_KVP + (size_t)NH * 8 * 4096;
constexpr size_t WS_PQ = WS_QB + (size_t)256 * 32768;
constexpr size_t WS_END = WS_PQ + (size_t)256 * 16384;
constexpr int LDS_BYTES = 163840;

#define MFMA32(a, b, c) __builtin_amdgcn_mfma_f32_32x32x16_bf16((a), (b), (c), 0, 0, 0)
#define MFMA16(a, b, c) __builtin_amdgcn_mfma_f32_16x16x32_bf16((a), (b), (c), 0, 0, 0)
#define SBAR() __builtin_amdgcn_sched_barrier(0)
#define WAIT_V(n) asm volatile("s_waitcnt vmcnt(" #n ")" ::: "memory")
#define WAIT_L(n) asm volatile("s_waitcnt lgkmcnt(" #n ")" ::: "memory")
#define BAR() __builtin_amdgcn_s_barrier()
#define WAIT_BAR(N) asm volatile("s_waitcnt vmcnt(" #N ") lgkmcnt(0)\n\ts_barrier" ::: "memory")

#define STAMP_DECL()
#define STAMP()
#define STAMP_RT()
#define STAMP_OUT(base)
#define STAMP_PARAM
#define STAMP_ARG
DI unsigned rfl(unsigned x) { return __builtin_amdgcn_readfirstlane(x); }
DI unsigned cvtpk(float lo, float hi) { f32x2 v = {lo, hi}; bf16x2_t b = __builtin_convertvector(v, bf16x2_t); return __builtin_bit_cast(unsigned, b); }
DI float bfround(float x) { return __uint_as_float(cvtpk(x, x) << 16); }
DI void glds16(const void* sbase, unsigned voff, unsigned lds_dst) {
    unsigned sv;
    asm volatile("s_mov_b32 %0, m0\n\ts_mov_b32 m0, %3\n\ts_nop 2\n\tglobal_load_lds_dwordx4 %1, %2\n\ts_mov_b32 m0, %0"
                 : "=&s"(sv) : "v"(voff), "s"(sbase), "s"(lds_dst) : "memory");
}
DI void glds16c(const void* sbase, unsigned voff, unsigned lds_dst) {
    unsigned sv;
    asm volatile("s_mov_b32 %0, m0\n\ts_mov_b32 m0, %3\n\ts_nop 2\n\tglobal_load_lds_dwordx4 %1, %2 sc1\n\ts_mov_b32 m0, %0"
                 : "=&s"(sv) : "v"(voff), "s"(sbase), "s"(lds_dst) : "memory");
}
DI void touch8k(const void* sbase, unsigned voff, unsigned lds_dst) {
    unsigned sv;
    asm volatile("s_mov_b32 %0, m0\n\ts_mov_b32 m0, %3\n\ts_nop 2\n\tglobal_load_lds_dword %1, %2\n\ts_mov_b32 m0, %0"
                 : "=&s"(sv) : "v"(voff), "s"(sbase), "s"(lds_dst) : "memory");
}
DI void glds16c_unless(const void* sbase, unsigned voff, unsigned lds_dst, int a, int b) {
    unsigned sv;
    asm volatile("s_cmp_lt_i32 %4, %5\n\ts_cselect_b64 exec, 0, -1\n\ts_mov_b32 %0, m0\n\ts_mov_b32 m0, %3\n\ts_nop 2\n\tglobal_load_lds_dwordx4 %1, %2 sc1\n\ts_mov_b32 m0, %0\n\ts_mov_b64 exec, -1"
                 : "=&s"(sv) : "v"(voff), "s"(sbase), "s"(lds_dst), "s"(a), "s"(b) : "memory", "scc");
}
DI void ldq_unless(bf16x8* q, const void* sbase, unsigned voff, unsigned skip) {
    asm volatile("s_cmp_lg_u32 %10, 0\n\ts_cbranch_scc0 .Lrare%=\n.Lback%=:\n\t.subsection 1\n.Lrare%=:\n\ts_nop 4\n\t"
                 "global_load_dwordx4 %0, %8, %9 offset:-4096\n\tglobal_load_dwordx4 %1, %8, %9 offset:-3072\n\tglobal_load_dwordx4 %2, %8, %9 offset:-2048\n\tglobal_load_dwordx4 %3, %8, %9 offset:-1024\n\t"
                 "global_load_dwordx4 %4, %8, %9\n\tglobal_load_dwordx4 %5, %8, %9 offset:1024\n\tglobal_load_dwordx4 %6, %8, %9 offset:2048\n\tglobal_load_dwordx4 %7, %8, %9 offset:3072\n\ts_branch .Lback%=\n\t.subsection 0"
                 : "+v"(q[0]), "+v"(q[1]), "+v"(q[2]), "+v"(q[3]), "+v"(q[4]), "+v"(q[5]), "+v"(q[6]), "+v"(q[7]) : "v"(voff), "s"((const char*)sbase + 4096), "s"(skip) : "memory", "scc");
}
DI void glds4c(const void* sbase, unsigned lds_dst) {
    unsigned sv, z;
    asm volatile("v_mov_b32 %1, 0\n\ts_mov_b32 %0, m0\n\ts_mov_b32 m0, %3\n\ts_nop 2\n\tglobal_load_lds_dword %1, %2 sc1\n\ts_mov_b32 m0, %0"
                 : "=&s"(sv), "=&v"(z) : "s"(sbase), "s"(lds_dst) : "memory");
}
typedef short v4i16_t __attribute__((ext_vector_type(4)));
DI s16x4 trrd(lds_cptr p) { return __builtin_bit_cast(s16x4, __builtin_amdgcn_ds_read_tr16_b64_v4i16((LAS v4i16_t*)p)); }
DI bf16x8 cat8(s16x4 a, s16x4 b) { return __builtin_shufflevector(a, b, 0, 1, 2, 3, 4, 5, 6, 7); }
DI bf16x8 ldsr128(lds_cptr p) { return *(const LAS bf16x8*)p; }
DI bf16x8 pack8(float a0, float a1, float a2, float a3, float a4, float a5, float a6, float a7) {
    u32x4 w = {cvtpk(a0, a1), cvtpk(a2, a3), cvtpk(a4, a5), cvtpk(a6, a7)}; return __builtin_bit_cast(bf16x8, w); }
#define PACK8(X, B) pack8((X)[(B)], (X)[(B) + 1], (X)[(B) + 2], (X)[(B) + 3], (X)[(B) + 4], (X)[(B) + 5], (X)[(B) + 6], (X)[(B) + 7])
DI float swap32_max(float v) { auto rr = __builtin_amdgcn_permlane32_swap(__float_as_uint(v), __float_as_uint(v), false, false); return fmaxf(__uint_as_float(rr[0]), __uint_as_float(rr[1])); }
DI float swap32_add(float v) { auto rr = __builtin_amdgcn_permlane32_swap(__float_as_uint(v), __float_as_uint(v), false, false); return __uint_as_float(rr[0]) + __uint_as_float(rr[1]); }
DI float lo32_bcast(float v) { auto rr = __builtin_amdgcn_permlane32_swap(__float_as_uint(v), __float_as_uint(v), false, false); return __uint_as_float(rr[0]); }
template <int CTRL> DI float dpp(float v) { return __uint_as_float(__builtin_amdgcn_update_dpp(0u, __float_as_uint(v), CTRL, 0xF, 0xF, true)); }
DI float row16_max(float v) { v = fmaxf(v, dpp<0xB1>(v)); v = fmaxf(v, dpp<0x4E>(v)); v = fmaxf(v, dpp<0x141>(v)); v = fmaxf(v, dpp<0x140>(v)); return v; }
DI float row16_add(float v) { v += dpp<0xB1>(v); v += dpp<0x4E>(v); v += dpp<0x141>(v); v += dpp<0x140>(v); return v; }

constexpr float QSCALE = 0.08838834764831845f * 1.4426950408889634f;
constexpr float THR = 8.f;
constexpr int L_V = 98304;
constexpr int L_WE = 65536, L_GATE = 139264;
#define PIN(x) asm volatile("" : "+v"(x))
#define MX3(a, b, c) __builtin_fmaxf(__builtin_fmaxf((a), (b)), (c))
#define EX2(v) __builtin_amdgcn_exp2f(v)
#define RLX_AGENT __ATOMIC_RELAXED, __HIP_MEMORY_SCOPE_AGENT
#define ST_PL(base, voff, imm, v) asm volatile("s_nop 4\n\tglobal_store_dwordx4 %0, %1, %2 offset:%3\n\ts_nop 1" :: "v"(voff), "v"(v), "s"(base), "i"(imm) : "memory")
#define ST_SC1(base, voff, imm, v) asm volatile("s_nop 4\n\tglobal_store_dwordx4 %0, %1, %2 offset:%3 sc1\n\ts_nop 1" :: "v"(voff), "v"(v), "s"(base), "i"(imm) : "memory")
DI int fresh_lane() { int l; asm volatile("v_mbcnt_lo_u32_b32 %0, -1, 0\n\tv_mbcnt_hi_u32_b32 %0, -1, %0" : "=v"(l)); return l; }
DI void wait_cnt(const unsigned* p, unsigned want) {
    unsigned spins = 0;
    while (rfl(__hip_atomic_load(p, RLX_AGENT)) < want) { __builtin_amdgcn_s_sleep(2); if (++spins > (1u << 20)) break; }
}
DI void handshake_finish(char* ws, int bid, unsigned token, bool ok, int lane) {
    unsigned* CNT = (unsigned*)(ws + WS_CNT); unsigned spins = 0;
    if (bid == 0) {
        for (;;) {
            const unsigned a = __hip_atomic_load(CNT + (80 + (lane & 15)) * 64, RLX_AGENT);
            if (__all(a >= ((lane & 15) ? 16u : 15u))) break;
#pragma unroll
            for (int i = 0; i < 4; ++i) { const unsigned q = __hip_atomic_load((const unsigned*)(ws + WS_HQ) + i * 64 + lane, RLX_AGENT); __hip_atomic_store((unsigned*)(ws + WS_HR) + i * 64 + lane, q, RLX_AGENT); }
            __builtin_amdgcn_s_sleep(8); if (++spins > (1u << 18)) break; }
    } else if (!ok) {
        while (rfl(__hip_atomic_load((const unsigned*)(ws + WS_HR) + bid, RLX_AGENT)) != token) { __builtin_amdgcn_s_sleep(8); if (++spins > (1u << 20)) break; }
        if (lane == 0) __hip_atomic_fetch_add(CNT + (80 + (bid & 15)) * 64, 1u, RLX_AGENT);
    }
}
DI void dual_softmax(f32x16& p) {
#pragma unroll
    for (int hf = 0; hf < 2; ++hf) {
        float mx = -INFINITY;
#pragma unroll
        for (int r = 0; r < 8; ++r) { p[8 * hf + r] = bfround(p[8 * hf + r]); mx = fmaxf(mx, p[8 * hf + r]); }
        mx = swap32_max(mx); float s = 0.f;
#pragma unroll
        for (int r = 0; r < 8; ++r) { p[8 * hf + r] = EX2((p[8 * hf + r] - mx) * 1.4426950408889634f); s += p[8 * hf + r]; }
        s = __builtin_amdgcn_rcpf(swap32_add(s));
#pragma unroll
        for (int r = 0; r < 8; ++r) p[8 * hf + r] *= s;
    }
}
DI void mask_half(f32x16& sc, int dqw) {
    if (dqw < 0) {
#pragma unroll
        for (int r = 0; r < 16; ++r) sc[r] = -INFINITY;
    } else if (dqw == 0) {
        const int ln = fresh_lane(); const int lr = (ln & 31) - 4 * (ln >> 5); const float ninf = -INFINITY;
#pragma unroll
        for (int r = 0; r < 16; ++r)
            asm volatile("s_nop 0\n\tv_cmp_gt_i32 vcc, %c3, %2\n\tv_cndmask_b32 %0, %0, %1, vcc" : "+v"(sc[r]) : "v"(ninf), "v"(lr), "i"(16 * ((r >> 2) & 1) + 8 * (r >> 3) + (r & 3)) : "vcc");
    }
}
DI void mask_tile(f32x16& s0, f32x16& s1, int dqw) {
    asm volatile("s_nop 7\n\ts_nop 7" ::: "memory");
    mask_half(s0, dqw); mask_half(s1, dqw - 32); }

__global__ __launch_bounds__(512, 2) void kf_all(const u16* __restrict__ x, const u16* __restrict__ ckv, const u16* __restrict__ wq, const u16* __restrict__ wuk,
                                                 const u16* __restrict__ wuv, const u16* __restrict__ wo_a, const u16* __restrict__ wqkv,
                                                 const u16* __restrict__ wphi, const u16* __restrict__ wo_b, const u16* __restrict__ gatew,
                                                 char* ws, u16* __restrict__ out) {
    extern __shared__ __attribute__((aligned(16))) char shm[];
    const lds_ptr lds = (lds_ptr)shm; const unsigned lds0 = (unsigned)(uintptr_t)shm;
    const int wid = rfl(threadIdx.x >> 6), G = wid >> 2, w4 = wid & 3;
    const int bid = blockIdx.x, hh_ = 4 * (bid & 7) + (bid >> 6), jj = (bid >> 3) & 7;
#define LANE_VARS() int tid = threadIdx.x; asm volatile("" : "+v"(tid)); const int lane = tid & 63, r32 = lane & 31, hi = lane >> 5, blk = (lane >> 4) & 1, \
        q4 = (lane & 15) >> 2, p4 = lane & 3; const unsigned vlin = (unsigned)lane * 16u; (void)r32; (void)hi; (void)blk; (void)q4; (void)p4; (void)vlin
    unsigned* const CNT = (unsigned*)(ws + WS_CNT);
    char* const KVh = ws + WS_KV + (size_t)hh_ * 32 * 32768;
    char* const A3 = ws + WS_A3;
    STAMP_DECL(); STAMP_RT(); STAMP();
    u32x4 pkv[16];
    unsigned token = 0u;
    const int bA = 15 - jj, bB = jj;
    const char* gX = (const char*)x + (size_t)((G ? bB : bA) * 128 + 32 * w4) * 1024; const char* gW = (const char*)wq + hh_ * 256; const char* gE = (const char*)wqkv + hh_ * 32;
    const unsigned lx = lds0 + wid * 4096, lw = lds0 + L_WE + wid * 1024;
    unsigned voffX, voffW, voffE;
    { const int lane = threadIdx.x & 63;
        { const int row = lane >> 3, ch = (lane & 7) ^ ((row >> 1) & 3); voffX = (unsigned)(row * 1024 + ch * 16); }
        { const int row = 4 * wid + (lane >> 4), j = lane & 15, c = (j >> 2) ^ (row & 3); voffW = (unsigned)(row * 8192 + c * 64 + (j & 3) * 16); }
        { const int row = 8 * wid + (lane >> 3), sl = lane & 7, hf = (sl >> 2) ^ ((row >> 1) & 1), part = hf ? 2 : ((sl >> 1) & 1);
          voffE = (unsigned)(row * 3072 + part * 1024 + (sl & 1) * 16); }
        if (wid == 0) glds16(gatew, (unsigned)lane * 16u, lds0 + L_GATE);
        if (bid == 0 && wid == 1) {
            const unsigned z = 0u;
            asm volatile("global_store_dword %0, %1, %2 sc1\n\tglobal_store_dword %0, %1, %3 sc1\n\ts_nop 1" :: "v"((unsigned)lane * 256u), "v"(z), "s"(ws + WS_CNT), "s"(ws + WS_CNT + 16384) : "memory");
            asm volatile("global_store_dword %0, %1, %2 offset:4 sc1\n\ts_nop 1" :: "v"((unsigned)lane * 256u), "v"(z), "s"(ws + WS_CNT + 16384) : "memory"); }
        PIN(voffX); PIN(voffW); PIN(voffE);
    }
#define XISS(c) do { _Pragma("unroll") for (int e = 0; e < 4; ++e) glds16(gX + e * 8192 + (c) * 128, voffX ^ ((e & 1) << 6), lx + ((c) & 1) * 32768 + e * 1024); } while (0)
#define WISS(c) do { glds16(gW + (size_t)(c) * 524288, voffW, lw + ((c) % 3) * 24576); glds16(gW + (size_t)(c) * 524288 + 262144, voffW, lw + ((c) % 3) * 24576 + 8192); \
                     glds16(gE + (size_t)(c) * 196608, voffE, lw + ((c) % 3) * 24576 + 16384); } while (0)
    {
        LANE_VARS();
        const int wr = wid >> 2, wc = wid & 3, fr = lane & 15, fq = lane >> 4;
        constexpr int HTB = 16384;
#define SA(b, h) (((b) * 2 + (h)) * HTB)
#define SB(b, h) ((4 + (b) * 2 + (h)) * HTB)
        unsigned voffA, voffB;
        { const int row = 8 * wid + (lane >> 3), ch = (lane & 7) ^ ((row >> 1) & 7); voffA = (unsigned)(row * 1024 + ch * 16); }
        { const int row = 4 * wid + (lane >> 4), j = lane & 15, s = ((row >> 3) & 1) * 4 + (row & 3), c = (j >> 1) ^ s; voffB = (unsigned)(row * 8192 + c * 32 + (j & 1) * 16); }
        const char* gA = (const char*)ckv + (size_t)jj * 256 * 1024;
        const char* gB0 = (const char*)wuk + hh_ * 256; const char* gB1 = (const char*)wuv + hh_ * 256;
        const unsigned ldsw = rfl(lds0 + wid * 1024);
#define STAGE_A(b, h, kt) do { const char* g_ = gA + (h) * 131072 + (kt) * 128; glds16(g_, voffA, ldsw + SA(b, h)); glds16(g_ + 65536, voffA, ldsw + SA(b, h) + 8192); } while (0)
#define STAGE_B(b, h, kt) do { const char* g_ = ((h) ? gB1 : gB0) + (size_t)(kt) * 524288; glds16(g_, voffB, ldsw + SB(b, h)); glds16(g_ + 262144, voffB, ldsw + SB(b, h) + 8192); } while (0)
        lds_cptr pa[2], pb[2];
#pragma unroll
        for (int ks = 0; ks < 2; ++ks) pa[ks] = lds + (wr * 64 + fr) * 128 + 16 * ((4 * ks + fq) ^ ((fr >> 1) & 7));
        { const int q = fr >> 2, p = fr & 3, s = (fq & 1) * 4 + q;
#pragma unroll
          for (int n = 0; n < 2; ++n) pb[n] = lds + (8 * fq + q) * 256 + 32 * ((2 * wc + n) ^ s) + 8 * p; }
#define LDA(dst, b, h) do { _Pragma("unroll") for (int m = 0; m < 4; ++m) _Pragma("unroll") for (int k = 0; k < 2; ++k) dst[m][k] = ldsr128(pa[k] + SA(b, h) + m * 2048); } while (0)
#define LDB(dst, b, h) do { _Pragma("unroll") for (int n = 0; n < 2; ++n) _Pragma("unroll") for (int k = 0; k < 2; ++k) \
        dst[n][k] = cat8(trrd(pb[n] + SB(b, h) + k * 8192), trrd(pb[n] + SB(b, h) + k * 8192 + 1024)); } while (0)
#define MMA(ai, bj, At, Bt) do { __builtin_amdgcn_s_setprio(1); _Pragma("unroll") for (int m = 0; m < 4; ++m) _Pragma("unroll") for (int n = 0; n < 2; ++n) _Pragma("unroll") for (int k = 0; k < 2; ++k) \
        acc[ai][bj][m][n] = (bj) ? MFMA16(At[m][k], Bt[n][k], acc[ai][bj][m][n]) : MFMA16(Bt[n][k], At[m][k], acc[ai][bj][m][n]); __builtin_amdgcn_s_setprio(0); } while (0)
        const unsigned scr = lds0 + 143360 + wid * 256; const size_t gw = (size_t)(bid * 8 + wid);
        auto warm = [&](int slot) {
            if (slot == 0 && wid == 0) { token = rfl(*(const LAS unsigned*)(lds + 148480)) + 1u; const unsigned z = 0u, tk = token;
                asm volatile("s_nop 4\n\tglobal_store_dword %0, %1, %2 sc1\n\ts_nop 1" :: "v"(z), "v"(tk), "s"(ws + WS_HQ + bid * 4) : "memory"); }
            if (bid == 0 && wid == 1) {
                if (slot > 0) { const u32x4 rq = *(const LAS u32x4*)(lds + 147456 + lane * 16); ST_SC1(ws + WS_HR, (unsigned)lane * 16u, 0, rq); WAIT_L(0); }
                glds16c(ws + WS_HQ, (unsigned)lane * 16u, lds0 + 147456); }
            const unsigned vo = (unsigned)lane * 128u;
            if (slot == 0) { if (lane < 8) touch8k((const char*)x + gw * 1024, vo, scr); }
            else if (slot == 1) { if (lane < 16) touch8k((const char*)wq + gw * 2048, vo, scr); }
            else if (slot == 2) { if (lane < 6) touch8k((const char*)wqkv + gw * 768, vo, scr); }
            else if (slot == 3) { if (lane < 16) touch8k((const char*)wo_a + gw * 2048, vo, scr); }
            else if (slot == 4) { if (lane < 2) touch8k((const char*)wo_b + gw * 256, vo, scr); }
        };
        f32x4 acc[2][2][4][2] = {};
        bf16x8 At[4][2], B0[2][2], B1[2][2];
        constexpr int nt = 8;
        STAGE_B(0, 0, 0); STAGE_A(0, 0, 0); STAGE_B(0, 1, 0); STAGE_A(0, 1, 0);
        if (wid == 0) glds4c(ws + WS_HR + bid * 4, lds0 + 148480);
        if (wr == 1) BAR();
        if (wid == 0) { WAIT_V(5); } else { WAIT_V(4); } BAR();
        STAGE_B(1, 0, 1); STAGE_A(1, 0, 1); STAGE_B(1, 1, 1);
        if (wid == 0) { WAIT_V(7); } else { WAIT_V(6); } BAR();
        STAMP();
        for (int t = 0; t < nt - 2; t += 2) {
            LDB(B0, 0, 0); SBAR(); LDA(At, 0, 0); STAGE_A(1, 1, t + 1);
            WAIT_L(8); BAR(); WAIT_L(0); MMA(0, 0, At, B0); BAR(); SBAR();
            LDB(B1, 0, 1); STAGE_B(0, 0, t + 2);
            BAR(); WAIT_L(0); MMA(0, 1, At, B1); BAR();
            LDA(At, 0, 1); STAGE_A(0, 0, t + 2);
            BAR(); WAIT_L(0); MMA(1, 0, At, B0); BAR(); SBAR();
            STAGE_B(0, 1, t + 2);
            WAIT_V(6); BAR(); MMA(1, 1, At, B1); BAR();
            warm(t);
            LDB(B0, 1, 0); SBAR(); LDA(At, 1, 0); STAGE_A(0, 1, t + 2);
            WAIT_L(8); BAR(); WAIT_L(0); MMA(0, 0, At, B0); BAR(); SBAR();
            LDB(B1, 1, 1); STAGE_B(1, 0, t + 3);
            BAR(); WAIT_L(0); MMA(0, 1, At, B1); BAR();
            LDA(At, 1, 1); STAGE_A(1, 0, t + 3);
            BAR(); WAIT_L(0); MMA(1, 0, At, B0); BAR(); SBAR();
            STAGE_B(1, 1, t + 3);
            WAIT_V(6); BAR(); MMA(1, 1, At, B1); BAR();
            warm(t + 1);
        }
        { LDB(B0, 0, 0); WAIT_V(0); LDA(At, 0, 0); STAGE_A(1, 1, nt - 1);
          BAR(); WAIT_L(0); MMA(0, 0, At, B0); BAR();
          LDB(B1, 0, 1); BAR(); WAIT_L(0); MMA(0, 1, At, B1); BAR();
          LDA(At, 0, 1); BAR(); WAIT_L(0); MMA(1, 0, At, B0); MMA(1, 1, At, B1); BAR(); }
        { LDB(B0, 1, 0); LDA(At, 1, 0); BAR(); WAIT_L(0); MMA(0, 0, At, B0); BAR();
          LDB(B1, 1, 1); WAIT_V(0);
          WISS(0); XISS(0);
          BAR(); WAIT_L(0); MMA(0, 1, At, B1); BAR();
          LDA(At, 1, 1); BAR(); WAIT_L(0); MMA(1, 0, At, B0); MMA(1, 1, At, B1); BAR(); }
        if (wr == 0) BAR();
        WISS(1); XISS(1);
        if (wid == 0) glds4c(ws + WS_HR + bid * 4, lds0 + 148480);
        SBAR();
#undef SA
#undef SB
#undef STAGE_A
#undef STAGE_B
#undef LDA
#undef LDB
#undef MMA
#pragma unroll
        for (int ai = 0; ai < 2; ++ai) {
#pragma unroll
            for (int m = 0; m < 4; ++m) { const f32x4 a = acc[ai][0][m][0], b = acc[ai][0][m][1];
                pkv[ai * 4 + m] = (u32x4){cvtpk(a[0], a[1]), cvtpk(a[2], a[3]), cvtpk(b[0], b[1]), cvtpk(b[2], b[3])}; }
#pragma unroll
            for (int u = 0; u < 2; ++u)
#pragma unroll
                for (int n = 0; n < 2; ++n) { const f32x4 a = acc[ai][1][2 * u][n], b = acc[ai][1][2 * u + 1][n];
                    pkv[8 + ai * 4 + u * 2 + n] = (u32x4){cvtpk(a[0], a[1]), cvtpk(a[2], a[3]), cvtpk(b[0], b[1]), cvtpk(b[2], b[3])}; }
        }
    }
    STAMP();
    const char* kvw = KVh + w4 * 4096;
    const unsigned ldw = lds0 + w4 * 4096;
    char* const qbp = ws + WS_QB + (size_t)(bid * 4 + w4) * 8192;
    char* const pqp = ws + WS_PQ + (size_t)(bid * 8 + wid) * 2048;
    bf16x8 qf[8]; float gate;
    {
        LANE_VARS();
        u16 wph[8];
#pragma unroll
        for (int j = 0; j < 8; ++j) wph[j] = wphi[hh_ * 256 + (8 * (j >> 2) + 4 * hi + (j & 3)) * 16 + (lane & 15)];
        const char* tb0 = KVh + (size_t)(4 * jj + G) * 32768;
        const unsigned vok = (unsigned)((4 * w4 + (lane >> 4)) * 1024 + (lane & 15) * 16), vov = (unsigned)(16384 + (lane >> 4) * 2048 + (w4 * 32 + (lane & 15)) * 16);
#define KVST(i) do { if ((i) < 8) ST_SC1(tb0 + ((i) >> 2) * 65536, vok, ((i) & 3) * 256, pkv[i]); \
                     else ST_SC1(tb0 + (((i) >> 2) & 1) * 65536 + (((i) >> 1) & 1) * 8192, vov, ((i) & 1) * 256, pkv[i]); } while (0)
        f32x16 qa[5] = {}, d1 = {};
        unsigned xo[2], wo_[4];
#pragma unroll
        for (int s = 0; s < 2; ++s) xo[s] = (unsigned)((32 * wid + r32) * 128 + 16 * ((2 * s + hi) ^ ((r32 >> 1) & 7)));
#pragma unroll
        for (int c = 0; c < 4; ++c) wo_[c] = (unsigned)(L_WE + (8 * hi + q4) * 256 + 64 * (c ^ q4) + 32 * (p4 >> 1) + 16 * blk + 8 * (p4 & 1));
        const unsigned eo = (unsigned)(L_WE + 16384 + (8 * hi + q4) * 128 + 64 * ((q4 >> 1) & 1) + 32 * blk + 8 * p4);
        const unsigned vo_ = (unsigned)(L_WE + 16384 + (8 * hi + q4) * 128 + 64 * (((q4 >> 1) & 1) ^ 1) + 8 * p4);
        bf16x8 fx[2], fw[2][6], fg[2];
#define QR_(buf, c, ks, u) do { \
        if ((u) == 0) { fx[buf] = ldsr128(lds + ((c) & 1) * 32768 + (xo[(ks) & 1] ^ (((ks) >> 1) << 6))); fg[buf] = ldsr128(lds + L_GATE + ((c) * 64 + (ks) * 16 + 8 * hi) * 2); } \
        else if ((u) < 5) { fw[buf][(u) - 1] = cat8(trrd(lds + wo_[(u) - 1] + ((c) % 3) * 24576 + (ks) * 4096), trrd(lds + wo_[(u) - 1] + ((c) % 3) * 24576 + (ks) * 4096 + 1024)); } \
        else { fw[buf][4] = cat8(trrd(lds + eo + ((c) % 3) * 24576 + (ks) * 2048), trrd(lds + eo + ((c) % 3) * 24576 + (ks) * 2048 + 512)); \
               fw[buf][5] = cat8(trrd(lds + vo_ + ((c) % 3) * 24576 + (ks) * 2048), trrd(lds + vo_ + ((c) % 3) * 24576 + (ks) * 2048 + 512)); } } while (0)
#define QM_(buf, u) do { if ((u) < 5) qa[u] = MFMA32(fw[buf][u], fx[buf], qa[u]); else d1 = MFMA32(fx[buf], blk ? fg[buf] : fw[buf][5], d1); } while (0)
#define QRD(buf, c, ks) do { _Pragma("unroll") for (int u_ = 0; u_ < 6; ++u_) QR_(buf, c, ks, u_); } while (0)
#define QSTEP(buf, c, ks, rd) do { _Pragma("unroll") for (int u_ = 0; u_ < 6; ++u_) { if (rd) QR_((buf) ^ 1, c, ks, u_); QM_(buf, u_); SBAR(); } } while (0)
#define WV_(n) asm volatile("s_waitcnt vmcnt(%0)" :: "i"(n) : "memory")
#define NST(c) ((c) < 0 ? 0 : (c) < 4 ? 3 : (c) < 6 ? 2 : 0)
#define ST0(c) ((c) < 4 ? 3 * (c) : 12 + 2 * ((c) - 4))
#define QCHUNK(c) do { \
        SBAR(); QSTEP(0, c, 1, true); QSTEP(1, c, 2, true); \
        if ((c) < 7) { WV_(4 + NST((c) - 1)); } BAR(); asm volatile("" ::: "memory"); \
        if ((c) == 7 && wid == 0) { handshake_finish(ws, bid, token, hs_ok, lane); if (lane == 0) __hip_atomic_fetch_add(CNT + hh_ * 64, 1u, RLX_AGENT); } \
        if ((c) < 6) WISS((c) + 2); \
        _Pragma("unroll") for (int i_ = 0; i_ < NST(c); ++i_) KVST(ST0(c) + i_); \
        SBAR(); QSTEP(0, c, 3, true); \
        if ((c) < 7) { WV_(((c) < 6 ? 3 : 0) + NST(c)); } \
        SBAR(); QSTEP(1, (c) + 1, 0, (c) < 7); \
        if ((c) < 6) XISS((c) + 2); } while (0)
        if (wid == 0) { WAIT_V(8); } else { WAIT_V(7); } BAR(); asm volatile("" ::: "memory");
        bool hs_ok = true;
        bf16x8 wpf;
#pragma unroll
        for (int j = 0; j < 8; ++j) wpf[j] = (short)(blk ? (wph[j] ^ 0x8000) : wph[j]);
        PIN(wpf);
        STAMP();
        QRD(0, 0, 0);
        QCHUNK(0);
        {
            unsigned t0, t1, seen, em;
            asm volatile("v_mov_b32 %0, %5\n\tds_read_b32 %0, %0\n\tv_mov_b32 %1, 1\n\ts_waitcnt lgkmcnt(0)\n\tv_readfirstlane_b32 %2, %0\n\tv_mov_b32 %0, 0\n\ts_nop 3\n\t"
                         "s_min_u32 %3, %8, 1\n\ts_cmp_eq_u32 %7, 0\n\ts_cselect_b32 %3, %3, 0\n\t"
                         "s_cmp_eq_u32 %2, %6\n\ts_cselect_b32 %2, 1, 0\n\ts_and_b32 %3, %3, %2\n\ts_mov_b32 exec_lo, %3\n\ts_mov_b32 exec_hi, 0\n\t"
                         "global_atomic_add %0, %1, %4\n\ts_mov_b64 exec, -1"
                         : "=&v"(t0), "=&v"(t1), "=&s"(seen), "=&s"(em) : "s"(CNT + (80 + (bid & 15)) * 64), "s"(lds0 + 148480), "s"(token), "s"(wid), "s"(bid) : "memory", "scc");
            hs_ok = seen != 0u; }
        QCHUNK(1); QCHUNK(2); QCHUNK(3); QCHUNK(4); QCHUNK(5); QCHUNK(6); QCHUNK(7);
#undef QRD
#undef QR_
#undef QM_
#undef QSTEP
#undef QCHUNK
#undef XISS
#undef WISS
#undef KVST
#undef NST
#undef ST0
        STAMP();
        BAR(); asm volatile("" ::: "memory");
        unsigned f1 = __hip_atomic_load(CNT + hh_ * 64, RLX_AGENT);
#pragma unroll
        for (int cc = 0; cc < 4; ++cc) {
#pragma unroll
            for (int r = 0; r < 16; ++r) qa[cc][r] *= QSCALE;
            qf[2 * cc] = PACK8(qa[cc], 0); qf[2 * cc + 1] = PACK8(qa[cc], 8);
        }
        if (rfl(f1) < 8u) wait_cnt(CNT + hh_ * 64, 8u);
        asm volatile("" ::: "memory");
        STAMP();
#pragma unroll
        for (int e = 0; e < 4; ++e) glds16c(kvw + (size_t)(e >> 1) * 32768 + (2 * G + (e & 1)) * 1024, vlin, ldw + (e >> 1) * 16384 + (2 * G + (e & 1)) * 1024);
        LAS float* red = (LAS float*)(lds + L_V + G * 32768 + w4 * 4096);
        if (r32 == 16) {
#pragma unroll
            for (int r = 0; r < 16; ++r) red[(r & 3) + 8 * (r >> 2) + 4 * hi] = d1[r];
        }
        WAIT_L(0);
        { const float gz = bfround(red[r32]); gate = bfround(1.f / (1.f + __expf(-gz))); }
        f32x16 pq = MFMA32(wpf, PACK8(qa[4], 0), f32x16{});
        f32x16 pk = MFMA32(wpf, PACK8(qa[4], 8), f32x16{});
        dual_softmax(pq); dual_softmax(pk);
        f32x16 d3;
        { bf16x8 id0, id1;
#pragma unroll
          for (int j = 0; j < 8; ++j) { const int f = 8 * (j >> 2) + 4 * hi + (j & 3); id0[j] = (short)(r32 == f ? 0x3F80 : 0); id1[j] = (short)(r32 == f + 16 ? 0x3F80 : 0); }
          d3 = MFMA32(PACK8(pk, 0), id0, f32x16{}); d3 = MFMA32(PACK8(pk, 8), id1, d3); }
        { const bf16x8 t0 = PACK8(pq, 0), t1 = PACK8(pq, 8); ST_PL(pqp, vlin, 0, t0); ST_PL(pqp, vlin, 1024, t1); }
        if (G == 0) {
#pragma unroll
            for (int s = 0; s < 8; ++s) ST_PL(qbp + (s >> 2) * 4096, vlin, (s & 3) * 1024, qf[s]);
        }
        const bf16x8 ones = {0x3F80, 0x3F80, 0x3F80, 0x3F80, 0x3F80, 0x3F80, 0x3F80, 0x3F80};
        const bf16x8 v0 = PACK8(d1, 0), v1 = PACK8(d1, 8);
        f32x16 d4 = MFMA32(PACK8(d3, 0), blk ? ones : v0, f32x16{});
        d4 = MFMA32(PACK8(d3, 8), blk ? ones : v1, d4);
#pragma unroll
        for (int r = 0; r < 16; ++r) red[((r & 3) + 8 * (r >> 2) + 4 * hi) * 32 + r32] = d4[r];
        WAIT_L(0); BAR(); asm volatile("" ::: "memory");
        if (tid < 256) {
            f32x4 s = {0.f, 0.f, 0.f, 0.f};
#pragma unroll
            for (int w = 0; w < 8; ++w) s += *(const LAS f32x4*)(lds + L_V + (w >> 2) * 32768 + (w & 3) * 4096 + tid * 16);
            ST_SC1(ws + WS_KVP + (size_t)(hh_ * 8 + jj) * 4096, (unsigned)tid * 16u, 0, s);
        }
        if (G) { WAIT_BAR(2); } else { WAIT_BAR(11); }
    }
    STAMP();
    {
    LANE_VARS();
    const int prow = (r32 & 7) | ((r32 & 8) << 1) | ((r32 & 16) >> 1);
    const unsigned kro = (unsigned)(prow * 16 + hi * 1024), vro = (unsigned)(L_V + r32 * 16 + hi * 2048);
    const int dq0 = 32 * w4;
    const int T = 2 * jj + 2;
    unsigned ks0 = 0u, ks1 = 16384u, ks2 = 32768u;
    int rho = 0; const int TG = G ? T : 99;
    f32x16 o[4], negm, sA0, sA1, sB0, sB1; bf16x8 kf[16], vf[16]; u32x4 pw[4]; float m, l;
#define KRD(i, kp) kf[i] = ldsr128((kp) + ((i) >> 1) * 2048 + ((i) & 1) * 512)
#define VRD(i, vp) vf[i] = ldsr128((vp) + ((i) >> 2) * 4096 + ((i) & 3) * 512)
#define PF(k) __builtin_bit_cast(bf16x8, pw[k])
#define DMA1(i) do { if ((i) < 2) glds16c(gk_ + (i) * 1024, vlin, dk_ + (i) * 1024); else if ((i) < 4) glds16c_unless(gk_ + (i) * 1024, vlin, dk_ + (i) * 1024, rk_, T); \
                     else if ((i) < 6) glds16c(gv_ + ((i) - 4) * 1024, vlin, dv_ + ((i) - 4) * 1024); else glds16c_unless(gv_ + ((i) - 4) * 1024, vlin, dv_ + ((i) - 4) * 1024, rho, T); } while (0)
    const char* gk_; const char* gv_; unsigned dk_, dv_, ko_, vo_; int rk_;
    unsigned go1, go2;
#define GOFF(r) ((r) < T ? (unsigned)(r) * 32768u + G * 2048u : (unsigned)(2 * (r) - T + G) * 32768u)
#define PREP_A(r, k0) do { ko_ = ((r) >= TG ? 49152u : 0u) + (k0); vo_ = ((r) - 1 >= TG ? 32768u : 0u) + (((r) - 1) & 1) * 16384u; } while (0)
#define PREP_B(r, gcur) do { gv_ = kvw + ((gcur) + 16384u); dv_ = ldw + L_V + ((r) & 1) * 16384u + ((r) < T ? G * 2048u : G * 32768u); } while (0)
#define PREP_C(r) do { rk_ = (r) + 2 <= 16 ? (r) + 2 : 16; go1 = go2; go2 = GOFF(rk_); } while (0)
#define PREP_D(k2) do { gk_ = kvw + go2; dk_ = ldw + (k2) + (rk_ < T ? G * 2048u : G * 49152u); } while (0)
#define ROT() do { const unsigned t_ = ks0; ks0 = ks1; ks1 = ks2; ks2 = t_; ++rho; } while (0)
#define GAPA(i, C0, C1, P0, P1) do { \
        if ((i) + 4 < 16) { KRD((i) + 4, kp_); } else { VRD((i) - 12, vp_); } \
        if (((i) & 1) == 0) DMA1((i) >> 1); \
        if ((i) & 1) C1 = MFMA32(kf[i], qf[(i) >> 1], (i) < 2 ? negm : C1); else C0 = MFMA32(kf[i], qf[(i) >> 1], (i) < 2 ? negm : C0); \
        if ((i) < 8) { P1[2 * (i)] = EX2(P1[2 * (i)]); P1[2 * (i) + 1] = EX2(P1[2 * (i) + 1]); pw[(i) >> 2][(i) & 3] = cvtpk(P0[2 * (i)], P0[2 * (i) + 1]); PIN(P1); PIN(pw[(i) >> 2]); } \
        else { l += P1[2 * (i) - 16]; l2 += P1[2 * (i) - 15]; pw[(i) >> 2][(i) & 3] = cvtpk(P1[2 * (i) - 16], P1[2 * (i) - 15]); PIN(l); PIN(l2); PIN(pw[(i) >> 2]); } \
        SBAR(); } while (0)
#define PVM(i) o[(i) & 3] = MFMA32(vf[i], PF((i) >> 2), o[(i) & 3])
#define RDB(i) do { if ((i) + 4 < 16) { VRD((i) + 4, vp_); } else { KRD((i) - 12, kn_); } } while (0)
#define ROUND(C0, C1, P0, P1) do { \
        const lds_cptr kp_ = lds + ko_ + kro; const lds_cptr vp_ = lds + vo_ + vro; \
        float l2 = 0.f; lds_cptr kn_ = kp_; SBAR(); \
        GAPA(0, C0, C1, P0, P1); GAPA(1, C0, C1, P0, P1); GAPA(2, C0, C1, P0, P1); GAPA(3, C0, C1, P0, P1); \
        GAPA(4, C0, C1, P0, P1); GAPA(5, C0, C1, P0, P1); GAPA(6, C0, C1, P0, P1); GAPA(7, C0, C1, P0, P1); \
        GAPA(8, C0, C1, P0, P1); GAPA(9, C0, C1, P0, P1); GAPA(10, C0, C1, P0, P1); GAPA(11, C0, C1, P0, P1); \
        GAPA(12, C0, C1, P0, P1); GAPA(13, C0, C1, P0, P1); GAPA(14, C0, C1, P0, P1); GAPA(15, C0, C1, P0, P1); \
        l += l2; \
        ldq_unless(qf, qbp, vlin, (unsigned)(rho + 1 - TG));        \
        RDB(0); PVM(0); SBAR(); RDB(1); PVM(1); SBAR(); \
        if (__builtin_expect(rho == 16 || (rho >= TG - 2 && rho < TG), 0)) mask_tile(C0, C1, dq0 - (rho == 16 ? 64 * G : 64 * (rho - TG + 2))); \
        float ma, mb; \
        RDB(2); PVM(2); ma = MX3(C0[0], C0[1], C1[0]); mb = MX3(C0[2], C0[3], C1[1]); ma = MX3(ma, C1[2], C1[3]); mb = MX3(mb, C0[4], C0[5]); PIN(ma); PIN(mb); SBAR(); \
        RDB(3); PVM(3); ma = MX3(ma, C0[6], C0[7]); mb = MX3(mb, C1[4], C1[5]); ma = MX3(ma, C1[6], C1[7]); mb = MX3(mb, C0[8], C0[9]); PIN(ma); PIN(mb); SBAR(); \
        RDB(4); PVM(4); ma = MX3(ma, C0[10], C0[11]); mb = MX3(mb, C1[8], C1[9]); ma = MX3(ma, C1[10], C1[11]); mb = MX3(mb, C0[12], C0[13]); PIN(ma); PIN(mb); SBAR(); \
        RDB(5); PVM(5); ma = MX3(ma, C0[14], C0[15]); mb = MX3(mb, C1[12], C1[13]); ma = MX3(ma, C1[14], C1[15]); \
        const float rm = swap32_max(fmaxf(ma, mb)); bool pend = false; float fsc = 1.f; \
        if (__builtin_expect(__any(rm > THR) || rho == TG, 0)) { float dl; pend = true; \
            if (rho == TG) { dl = fmaxf(rm, -1e30f); m = dl; fsc = gate / swap32_add(l); l = 0.f; }        \
            else { dl = fmaxf(rm, 0.f); m += dl; fsc = EX2(-dl); l *= fsc; } \
            _Pragma("unroll") for (int e = 0; e < 16; ++e) { C0[e] -= dl; C1[e] -= dl; negm[e] = -m; } } \
        SBAR(); \
        RDB(6); PVM(6); C0[0] = EX2(C0[0]); C0[1] = EX2(C0[1]); PIN(C0); SBAR(); \
        RDB(7); PVM(7); C0[2] = EX2(C0[2]); C0[3] = EX2(C0[3]); l += C0[0]; l2 = C0[1]; PIN(C0); PIN(l); SBAR(); \
        RDB(8); PVM(8); PREP_A(rho + 1, ks1); kn_ = lds + ko_ + kro; C0[4] = EX2(C0[4]); C0[5] = EX2(C0[5]); l += C0[2]; l2 += C0[3]; PIN(C0); PIN(l); PIN(l2); SBAR(); \
        RDB(9); PVM(9); PREP_B(rho + 1, go1); C0[6] = EX2(C0[6]); C0[7] = EX2(C0[7]); l += C0[4]; l2 += C0[5]; PIN(C0); PIN(l); PIN(l2); SBAR(); \
        RDB(10); PVM(10); PREP_C(rho + 1); C0[8] = EX2(C0[8]); C0[9] = EX2(C0[9]); l += C0[6]; l2 += C0[7]; PIN(C0); PIN(l); PIN(l2); SBAR(); \
        RDB(11); PVM(11); PREP_D(ks0); C0[10] = EX2(C0[10]); C0[11] = EX2(C0[11]); l += C0[8]; l2 += C0[9]; PIN(C0); PIN(l); PIN(l2); SBAR(); \
        RDB(12); PVM(12); C0[12] = EX2(C0[12]); C0[13] = EX2(C0[13]); l += C0[10]; l2 += C0[11]; PIN(C0); PIN(l); PIN(l2); SBAR(); \
        RDB(13); PVM(13); C0[14] = EX2(C0[14]); C0[15] = EX2(C0[15]); l += C0[12]; l2 += C0[13]; PIN(C0); PIN(l); PIN(l2); SBAR(); \
        RDB(14); PVM(14); l += C0[14]; l2 += C0[15]; PIN(l); PIN(l2); SBAR(); \
        RDB(15); PVM(15); l += l2; PIN(l); SBAR(); \
        if (__builtin_expect(pend || rho + 1 == TG, 0)) { \
            if (pend) { _Pragma("unroll") for (int c = 0; c < 4; ++c) _Pragma("unroll") for (int e = 0; e < 16; ++e) o[c][e] *= fsc; } \
            if (rho + 1 == TG) negm = f32x16{}; \
            if (rho == TG) { const int ln = fresh_lane(); const unsigned vod = (unsigned)((ln >> 5) * 2048 + (32 * w4 + (ln & 31)) * 16); const char* dst = A3 + (size_t)(bB * 576 + hh_ * 16) * 2048; \
                _Pragma("unroll") for (int c = 0; c < 4; ++c) { const bf16x8 t0 = PACK8(o[c], 0), t1 = PACK8(o[c], 8); ST_SC1(dst + c * 8192, vod, 0, t0); ST_SC1(dst + c * 8192 + 4096, vod, 0, t1); o[c] = f32x16{}; } } } \
        asm volatile("s_cmp_eq_u32 %0, %1\n\ts_cbranch_scc1 .Lw%=\n\ts_waitcnt vmcnt(0)\n.Lw%=:\n\ts_waitcnt vmcnt(8)\n\ts_barrier" :: "s"(rho), "s"(TG) : "memory", "scc");     \
        if (__builtin_expect(rho == TG + 1 && w4 == 0, 0)) { if (fresh_lane() == 0) __hip_atomic_fetch_or(CNT + (64 + bB) * 64, 1u << hh_, RLX_AGENT); }        \
        ROT(); } while (0)

#define ROUND0(C0, C1) do { \
        const lds_cptr kp_ = lds + ks0 + kro; const lds_cptr kn_ = lds + ks1 + kro; \
        go2 = GOFF(1); PREP_B(0, GOFF(0)); PREP_C(0); PREP_D(ks2); \
        _Pragma("unroll") for (int i = 0; i < 6; ++i) KRD(i, kp_); \
        _Pragma("unroll") for (int i = 0; i < 8; ++i) DMA1(i); \
        _Pragma("unroll") for (int c = 0; c < 4; ++c) o[c] = f32x16{}; \
        PREP_A(1, ks1); PREP_B(1, go1); PREP_C(1); PREP_D(ks0); SBAR(); \
        _Pragma("unroll") for (int i = 0; i < 16; ++i) { \
            if (i + 6 < 16) KRD(i + 6, kp_); \
            if (i & 1) C1 = MFMA32(kf[i], qf[i >> 1], i < 2 ? f32x16{} : C1); else C0 = MFMA32(kf[i], qf[i >> 1], i < 2 ? f32x16{} : C0); \
            SBAR(); } \
        if (TG == 2) mask_tile(C0, C1, dq0); \
        float ma = MX3(C0[0], C0[1], C1[0]), mb = MX3(C0[2], C0[3], C1[1]); ma = MX3(ma, C1[2], C1[3]); \
        _Pragma("unroll") for (int r = 4; r < 16; r += 4) { ma = MX3(ma, C0[r], C0[r + 1]); mb = MX3(mb, C0[r + 2], C0[r + 3]); ma = MX3(ma, C1[r], C1[r + 1]); mb = MX3(mb, C1[r + 2], C1[r + 3]); } \
        m = fmaxf(swap32_max(fmaxf(ma, mb)), -1e30f); l = 0.f; \
        _Pragma("unroll") for (int i = 0; i < 4; ++i) KRD(i, kn_); \
        _Pragma("unroll") for (int e = 0; e < 16; ++e) { C0[e] = EX2(C0[e] - m); l += C0[e]; C1[e] -= m; negm[e] = -m; } \
        asm volatile("s_waitcnt vmcnt(0)\n\ts_barrier" ::: "memory"); ROT(); } while (0)
#define DRAIN(P0, P1, MID) do { \
        const lds_cptr vp_ = lds + G * 32768 + vro; \
        _Pragma("unroll") for (int i = 0; i < 8; ++i) VRD(i, vp_); \
        _Pragma("unroll") for (int i = 0; i < 8; ++i) pw[i >> 2][i & 3] = cvtpk(P0[2 * i], P0[2 * i + 1]); \
        SBAR(); \
        _Pragma("unroll") for (int i = 0; i < 16; ++i) { \
            if (i + 8 < 16) VRD(i + 8, vp_); \
            if (i == 8) { MID } \
            PVM(i); \
            if (i < 8) { P1[2 * i] = EX2(P1[2 * i]); P1[2 * i + 1] = EX2(P1[2 * i + 1]); PIN(P1); } \
            if (i >= 1 && i < 9) { l += P1[2 * i - 2]; l += P1[2 * i - 1]; pw[2 + ((i - 1) >> 2)][(i - 1) & 3] = cvtpk(P1[2 * i - 2], P1[2 * i - 1]); PIN(l); PIN(pw[2 + ((i - 1) >> 2)]); } \
            SBAR(); } \
        l = swap32_add(l); } while (0)
    ROUND0(sA0, sA1);
    if (wid == 0 && fresh_lane() == 0) __hip_atomic_fetch_add(CNT + (32 + hh_) * 64, 1u, RLX_AGENT);
    STAMP();
#pragma clang loop unroll(disable)
    for (int pr = 0; pr < 8; ++pr) { ROUND(sB0, sB1, sA0, sA1); ROUND(sA0, sA1, sB0, sB1); }
    STAMP();
    {
        f32x2 ps[8]; bf16x8 phq[2];
        LANE_VARS();
        const unsigned f2 = __hip_atomic_load(CNT + (32 + hh_) * 64, RLX_AGENT);
        DRAIN(sA0, sA1,
            if (rfl(f2) < 8u) wait_cnt(CNT + (32 + hh_) * 64, 8u);
            _Pragma("unroll") for (int w = 0; w < 8; ++w) ps[w] = __builtin_bit_cast(f32x2, __hip_atomic_load((const unsigned long long*)(ws + WS_KVP + (size_t)(hh_ * 8) * 4096 + (unsigned)(w * 4096 + tid * 8)), RLX_AGENT));
            phq[0] = *(const bf16x8*)(pqp + vlin); phq[1] = *(const bf16x8*)(pqp + (vlin + 1024)); );
        const unsigned vod = (unsigned)(hi * 2048 + (32 * w4 + r32) * 16);
        BAR(); asm volatile("" ::: "memory");
        const unsigned st0 = ks2;
        { const lds_ptr xp = lds + L_V + wid * 8192 + lane * 16;
#pragma unroll
          for (int c = 0; c < 2; ++c)
#pragma unroll
              for (int g = 0; g < 4; ++g) { f32x4 t;
#pragma unroll
                  for (int e = 0; e < 4; ++e) t[e] = G ? o[c][4 * g + e] : o[2 + c][4 * g + e];
                  *(LAS f32x4*)(xp + (c * 4 + g) * 1024) = t; }
          *(LAS f32x4*)(lds + st0 + wid * 1024 + lane * 16) = (f32x4){m, l, gate, 0.f};
          f32x2 s = ps[0];
#pragma unroll
          for (int w = 1; w < 8; ++w) s += ps[w];
          *(LAS f32x2*)(lds + st0 + 8192 + tid * 8) = s; }
        WAIT_L(0); BAR(); asm volatile("" ::: "memory");
        { const int ow = wid ^ 4;
          const f32x4 st = *(const LAS f32x4*)(lds + st0 + ow * 1024 + lane * 16);
          const float mn = fmaxf(m, st[0]), f0 = EX2(m - mn), f1 = EX2(st[0] - mn);
          const float gg = G ? st[2] : gate;
          const float inv = gg / (l * f0 + st[1] * f1); const float c0 = f0 * inv, c1 = f1 * inv;
          const lds_cptr xp = lds + L_V + ow * 8192 + lane * 16;
          const char* dst = A3 + (size_t)(bA * 576 + hh_ * 16 + G * 8) * 2048;
#pragma unroll
          for (int c = 0; c < 2; ++c) {
              float v[16];
#pragma unroll
              for (int g = 0; g < 4; ++g) { const f32x4 t = *(const LAS f32x4*)(xp + (c * 4 + g) * 1024);
#pragma unroll
                  for (int e = 0; e < 4; ++e) v[4 * g + e] = (G ? o[2 + c][4 * g + e] : o[c][4 * g + e]) * c0 + t[e] * c1; }
              { const bf16x8 t0 = PACK8(v, 0), t1 = PACK8(v, 8); ST_SC1(dst + c * 8192, vod, 0, t0); ST_SC1(dst + c * 8192 + 4096, vod, 0, t1); }
          }
          float kvv[2][8];
          const int col = r32 < 16 ? r32 : 16;
#pragma unroll
          for (int s = 0; s < 2; ++s)
#pragma unroll
              for (int j = 0; j < 8; ++j) { const int f = 16 * s + 8 * (j >> 2) + 4 * hi + (j & 3); const float v = *(const LAS float*)(lds + st0 + 8192 + (f * 32 + col) * 4); kvv[s][j] = r32 <= 16 ? v : 0.f; }
          f32x16 nm = MFMA32(PACK8(kvv[0], 0), phq[0], f32x16{});
          nm = MFMA32(PACK8(kvv[1], 0), phq[1], nm);
          const float den = bfround(bfround(lo32_bcast(nm[8])) + 1e-6f); const float sc = (1.f - gate) / den;
          float ov[8];
#pragma unroll
          for (int r = 0; r < 8; ++r) ov[r] = bfround(nm[r]) * sc;
          const bf16x8 t0 = PACK8(ov, 0);
          ST_SC1(A3 + (size_t)((G ? bB : bA) * 576 + 512 + 2 * hh_) * 2048, vod, 0, t0);
        }
        WAIT_L(0); BAR(); asm volatile("" ::: "memory");
        STAMP();
    }
#undef KRD
#undef VRD
#undef PF
#undef DMA1
#undef ROT
#undef GOFF
#undef PREP_A
#undef PREP_B
#undef PREP_C
#undef PREP_D
#undef GAPA
#undef PVM
#undef RDB
#undef ROUND
#undef ROUND0
#undef DRAIN
    }
    {
        LANE_VARS();
        const int rt = 4 * (bid & 7) + (bid >> 6), ct = (bid >> 3) & 7;
        unsigned voffW; { const int row = lane >> 3, sl = lane & 7, nb = (sl >> 2) ^ ((row >> 1) & 1); voffW = (unsigned)(row * 1024 + nb * 64 + (sl & 3) * 16); }
        const char* gA = A3 + (size_t)((rt >> 1) * 576 + 2 * wid) * 2048 + (rt & 1) * 1024;
        const char* gWa = (const char*)wo_a + (size_t)wid * 16384 + ct * 128; const char* gWb = (const char*)wo_b + (size_t)wid * 16384 + ct * 128;
        const unsigned wb = lds0 + wid * 20480;
        const unsigned* rdy = CNT + (64 + (rt >> 1)) * 64;
        auto issue = [&](int u, unsigned slot) {
            const char* ga = gA + (size_t)u * 32768; const char* gw = u < 32 ? gWa + (size_t)u * 131072 : gWb + (size_t)(u - 32) * 131072;
            glds16c(ga, vlin, wb + slot); glds16c(ga + 2048, vlin, wb + slot + 1024); glds16(gw, voffW, wb + slot + 2048); glds16(gw + 8192, voffW, wb + slot + 3072); };
        unsigned seen = 0u; int hnext = 0;
        auto next_head = [&]() -> int {
            if (hnext >= 32) { if (hnext == 32) { unsigned spins = 0; while (rfl(__hip_atomic_load(rdy + 1, RLX_AGENT)) != ~0u) { __builtin_amdgcn_s_sleep(2); if (++spins > (1u << 20)) break; }
                                             asm volatile("" ::: "memory"); } }
            else if (!((seen >> hnext) & 1u)) { unsigned spins = 0;
                for (;;) { seen = rfl(__hip_atomic_load(rdy, RLX_AGENT)); if (((seen >> hnext) & 1u) || ++spins > (1u << 20)) break; __builtin_amdgcn_s_sleep(2); }
                asm volatile("" ::: "memory"); }
            return hnext++; };
        asm volatile("s_waitcnt vmcnt(0)\n\ts_barrier" ::: "memory");
        if (tid == 0) { __hip_atomic_fetch_or(CNT + (64 + bA) * 64, 1u << hh_, RLX_AGENT); __hip_atomic_fetch_or(CNT + (64 + bB) * 64, 1u << hh_, RLX_AGENT);
                        __hip_atomic_fetch_or(CNT + (64 + bA) * 64 + 1, 1u << hh_, RLX_AGENT); __hip_atomic_fetch_or(CNT + (64 + bB) * 64 + 1, 1u << hh_, RLX_AGENT); }
        STAMP();
#pragma unroll
        for (int i = 0; i < 5; ++i) issue(next_head(), i * 4096u);
        f32x16 acc[2][2] = {};
        const unsigned ao = (unsigned)(wid * 20480 + hi * 1024 + r32 * 16);
        unsigned bo[2];
#pragma unroll
        for (int nb = 0; nb < 2; ++nb) bo[nb] = (unsigned)(wid * 20480 + 2048 + (4 * hi + q4) * 128 + 64 * (nb ^ ((q4 >> 1) & 1)) + 32 * blk + 8 * p4);
        bf16x8 af[2][2], bf[2][2];
#define K3RD(buf, sb) do { af[buf][0] = ldsr128((sb) + ao); bf[buf][0] = cat8(trrd((sb) + bo[0]), trrd((sb) + bo[0] + 1024)); \
        af[buf][1] = ldsr128((sb) + ao + 512); bf[buf][1] = cat8(trrd((sb) + bo[1]), trrd((sb) + bo[1] + 1024)); } while (0)
#define K3BODY(buf, i) do { \
        const unsigned snx = sc == 16384u ? 0u : sc + 4096u; \
        if ((i) + 1 < 36) K3RD(buf ^ 1, lds + snx); \
        SBAR(); acc[0][0] = MFMA32(bf[buf][0], af[buf][0], acc[0][0]); acc[1][0] = MFMA32(bf[buf][0], af[buf][1], acc[1][0]); SBAR(); \
        if ((i) + 5 < 36) issue(next_head(), sc); \
        SBAR(); acc[0][1] = MFMA32(bf[buf][1], af[buf][0], acc[0][1]); acc[1][1] = MFMA32(bf[buf][1], af[buf][1], acc[1][1]); SBAR(); \
        sc = snx; } while (0)
#define K3W(n) asm volatile("s_waitcnt vmcnt(" #n ") lgkmcnt(0)" ::: "memory")
        unsigned sc = 0u;
        WAIT_V(16);
        K3RD(0, lds);
        for (int i = 0; i < 32; i += 2) {
            if (G) __builtin_amdgcn_s_setprio(1); K3W(12); K3BODY(0, i);
            if (G) __builtin_amdgcn_s_setprio(0); K3W(12); K3BODY(1, i + 1); }
        K3W(8); K3BODY(0, 32); K3W(4); K3BODY(1, 33); K3W(0); K3BODY(0, 34); K3W(0); K3BODY(1, 35);
#undef K3RD
#undef K3BODY
#undef K3W
        STAMP();
        BAR(); asm volatile("" ::: "memory");
#pragma unroll
        for (int mi = 0; mi < 2; ++mi)
#pragma unroll
            for (int nb = 0; nb < 2; ++nb)
#pragma unroll
                for (int g = 0; g < 4; ++g) { const int row = 32 * mi + r32, ch = 8 * nb + 2 * g + hi;
                    *(LAS f32x4*)(lds + wid * 16384 + row * 256 + 16 * (ch ^ (row & 15))) = (f32x4){acc[mi][nb][4 * g], acc[mi][nb][4 * g + 1], acc[mi][nb][4 * g + 2], acc[mi][nb][4 * g + 3]}; }
        WAIT_L(0); BAR(); asm volatile("" ::: "memory");
        { const int row = tid >> 3, c8 = tid & 7; f32x4 s0 = {}, s1 = {};
#pragma unroll
          for (int w = 0; w < 8; ++w) { s0 += *(const LAS f32x4*)(lds + w * 16384 + row * 256 + 16 * ((2 * c8) ^ (row & 15)));
                                        s1 += *(const LAS f32x4*)(lds + w * 16384 + row * 256 + 16 * ((2 * c8 + 1) ^ (row & 15))); }
          u32x4 w = {cvtpk(s0[0], s0[1]), cvtpk(s0[2], s0[3]), cvtpk(s1[0], s1[1]), cvtpk(s1[2], s1[3])};
          *(u32x4*)(out + (size_t)(rt * 64 + row) * 512 + ct * 64 + c8 * 8) = w; }
    }
    STAMP(); STAMP_RT();
    STAMP_OUT(ws + WS_END);
}

extern "C" void kernel_launch(void* const* d_in, const int* in_sizes, int n_in, void* d_out, int out_size, void* d_ws, size_t ws_size,
                              hipStream_t stream) {
    (void)in_sizes; (void)n_in; (void)out_size; (void)ws_size;
    char* ws = (char*)d_ws;
    (void)hipFuncSetAttribute((const void*)kf_all, hipFuncAttributeMaxDynamicSharedMemorySize, LDS_BYTES);
    kf_all<<<256, 512, LDS_BYTES, stream>>>((const u16*)d_in[0], (const u16*)d_in[1], (const u16*)d_in[2], (const u16*)d_in[3], (const u16*)d_in[4], (const u16*)d_in[5],
                                            (const u16*)d_in[7], (const u16*)d_in[8], (const u16*)d_in[9], (const u16*)d_in[10], ws, (u16*)d_out);
}
```

```cpp
#define TLBSLOT 5
#include <hip/hip_runtime.h>
#include <hip/hip_bf16.h>
#include <stdint.h>
#define TLBWARM 1
#define PREFETCH 1

typedef unsigned short u16;
typedef short bf16x8 __attribute__((ext_vector_type(8)));
typedef short s16x4 __attribute__((ext_vector_type(4)));
typedef float f32x2 __attribute__((ext_vector_type(2)));
typedef float f32x4 __attribute__((ext_vector_type(4)));
typedef float f32x16 __attribute__((ext_vector_type(16)));
typedef unsigned u32x4 __attribute__((ext_vector_type(4)));
typedef unsigned u32x2 __attribute__((ext_vector_type(2)));
typedef __bf16 bf16x2_t __attribute__((ext_vector_type(2)));
#define LAS __attribute__((address_space(3)))
#define DI __device__ __forceinline__
typedef LAS const char* lds_cptr;
typedef LAS char* lds_ptr;

constexpr int SEQ = 2048, DM = 512, NH = 32, HD = 128;
constexpr size_t WS_CNT = 0;
constexpr size_t WS_HQ = 32768, WS_HR = 33792;
constexpr size_t WS_KV = 65536;
constexpr size_t WS_A3 = WS_KV + (size_t)NH * 32 * 32768;
constexpr size_t WS_KVP = WS_A3 + (size_t)576 * SEQ * 16;
constexpr size_t WS_QB = WS_KVP + (size_t)NH * 8 * 4096;
constexpr size_t WS_PQ = WS_QB + (size_t)256 * 32768;
constexpr size_t WS_END = WS_PQ + (size_t)256 * 16384;
constexpr int LDS_BYTES = 163840;

#define MFMA32(a, b, c) __builtin_amdgcn_mfma_f32_32x32x16_bf16((a), (b), (c), 0, 0, 0)
#define MFMA16(a, b, c) __builtin_amdgcn_mfma_f32_16x16x32_bf16((a), (b), (c), 0, 0, 0)
#define SBAR() __builtin_amdgcn_sched_barrier(0)
#define WAIT_V(n) asm volatile("s_waitcnt vmcnt(" #n ")" ::: "memory")
#define WAIT_L(n) asm volatile("s_waitcnt lgkmcnt(" #n ")" ::: "memory")
#define BAR() __builtin_amdgcn_s_barrier()
#define WAIT_BAR(N) asm volatile("s_waitcnt vmcnt(" #N ") lgkmcnt(0)\n\ts_barrier" ::: "memory")

#define STAMP_DECL()
#define STAMP()
#define STAMP_RT()
#define STAMP_OUT(base)
#define STAMP_PARAM
#define STAMP_ARG
DI unsigned rfl(unsigned x) { return __builtin_amdgcn_readfirstlane(x); }
DI unsigned cvtpk(float lo, float hi) { f32x2 v = {lo, hi}; bf16x2_t b = __builtin_convertvector(v, bf16x2_t); return __builtin_bit_cast(unsigned, b); }
DI float bfround(float x) { return __uint_as_float(cvtpk(x, x) << 16); }
DI void glds16(const void* sbase, unsigned voff, unsigned lds_dst) {
    unsigned sv;
    asm volatile("s_mov_b32 %0, m0\n\ts_mov_b32 m0, %3\n\ts_nop 2\n\tglobal_load_lds_dwordx4 %1, %2\n\ts_mov_b32 m0, %0"
                 : "=&s"(sv) : "v"(voff), "s"(sbase), "s"(lds_dst) : "memory");
}
DI void glds16c(const void* sbase, unsigned voff, unsigned lds_dst) {
    unsigned sv;
    asm volatile("s_mov_b32 %0, m0\n\ts_mov_b32 m0, %3\n\ts_nop 2\n\tglobal_load_lds_dwordx4 %1, %2 sc1\n\ts_mov_b32 m0, %0"
                 : "=&s"(sv) : "v"(voff), "s"(sbase), "s"(lds_dst) : "memory");
}
DI void touch8k(const void* sbase, unsigned voff, unsigned lds_dst) {
    unsigned sv;
    asm volatile("s_mov_b32 %0, m0\n\ts_mov_b32 m0, %3\n\ts_nop 2\n\tglobal_load_lds_dword %1, %2\n\ts_mov_b32 m0, %0"
                 : "=&s"(sv) : "v"(voff), "s"(sbase), "s"(lds_dst) : "memory");
}
DI void glds16c_unless(const void* sbase, unsigned voff, unsigned lds_dst, int a, int b) {
    unsigned sv;
    asm volatile("s_cmp_lt_i32 %4, %5\n\ts_cselect_b64 exec, 0, -1\n\ts_mov_b32 %0, m0\n\ts_mov_b32 m0, %3\n\ts_nop 2\n\tglobal_load_lds_dwordx4 %1, %2 sc1\n\ts_mov_b32 m0, %0\n\ts_mov_b64 exec, -1"
                 : "=&s"(sv) : "v"(voff), "s"(sbase), "s"(lds_dst), "s"(a), "s"(b) : "memory", "scc");
}
DI void ldq_unless(bf16x8* q, const void* sbase, unsigned voff, unsigned skip) {
    asm volatile("s_cmp_lg_u32 %10, 0\n\ts_cbranch_scc0 .Lrare%=\n.Lback%=:\n\t.subsection 1\n.Lrare%=:\n\ts_nop 4\n\t"
                 "global_load_dwordx4 %0, %8, %9 offset:-4096\n\tglobal_load_dwordx4 %1, %8, %9 offset:-3072\n\tglobal_load_dwordx4 %2, %8, %9 offset:-2048\n\tglobal_load_dwordx4 %3, %8, %9 offset:-1024\n\t"
                 "global_load_dwordx4 %4, %8, %9\n\tglobal_load_dwordx4 %5, %8, %9 offset:1024\n\tglobal_load_dwordx4 %6, %8, %9 offset:2048\n\tglobal_load_dwordx4 %7, %8, %9 offset:3072\n\ts_branch .Lback%=\n\t.subsection 0"
                 : "+v"(q[0]), "+v"(q[1]), "+v"(q[2]), "+v"(q[3]), "+v"(q[4]), "+v"(q[5]), "+v"(q[6]), "+v"(q[7]) : "v"(voff), "s"((const char*)sbase + 4096), "s"(skip) : "memory", "scc");
}
DI void glds4c(const void* sbase, unsigned lds_dst) {
    unsigned sv, z;
    asm volatile("v_mov_b32 %1, 0\n\ts_mov_b32 %0, m0\n\ts_mov_b32 m0, %3\n\ts_nop 2\n\tglobal_load_lds_dword %1, %2 sc1\n\ts_mov_b32 m0, %0"
                 : "=&s"(sv), "=&v"(z) : "s"(sbase), "s"(lds_dst) : "memory");
}
typedef short v4i16_t __attribute__((ext_vector_type(4)));
DI s16x4 trrd(lds_cptr p) { return __builtin_bit_cast(s16x4, __builtin_amdgcn_ds_read_tr16_b64_v4i16((LAS v4i16_t*)p)); }
DI bf16x8 cat8(s16x4 a, s16x4 b) { return __builtin_shufflevector(a, b, 0, 1, 2, 3, 4, 5, 6, 7); }
DI bf16x8 ldsr128(lds_cptr p) { return *(const LAS bf16x8*)p; }
DI bf16x8 pack8(float a0, float a1, float a2, float a3, float a4, float a5, float a6, float a7) {
    u32x4 w = {cvtpk(a0, a1), cvtpk(a2, a3), cvtpk(a4, a5), cvtpk(a6, a7)}; return __builtin_bit_cast(bf16x8, w); }
#define PACK8(X, B) pack8((X)[(B)], (X)[(B) + 1], (X)[(B) + 2], (X)[(B) + 3], (X)[(B) + 4], (X)[(B) + 5], (X)[(B) + 6], (X)[(B) + 7])
DI float swap32_max(float v) { auto rr = __builtin_amdgcn_permlane32_swap(__float_as_uint(v), __float_as_uint(v), false, false); return fmaxf(__uint_as_float(rr[0]), __uint_as_float(rr[1])); }
DI float swap32_add(float v) { auto rr = __builtin_amdgcn_permlane32_swap(__float_as_uint(v), __float_as_uint(v), false, false); return __uint_as_float(rr[0]) + __uint_as_float(rr[1]); }
DI float lo32_bcast(float v) { auto rr = __builtin_amdgcn_permlane32_swap(__float_as_uint(v), __float_as_uint(v), false, false); return __uint_as_float(rr[0]); }
template <int CTRL> DI float dpp(float v) { return __uint_as_float(__builtin_amdgcn_update_dpp(0u, __float_as_uint(v), CTRL, 0xF, 0xF, true)); }
DI float row16_max(float v) { v = fmaxf(v, dpp<0xB1>(v)); v = fmaxf(v, dpp<0x4E>(v)); v = fmaxf(v, dpp<0x141>(v)); v = fmaxf(v, dpp<0x140>(v)); return v; }
DI float row16_add(float v) { v += dpp<0xB1>(v); v += dpp<0x4E>(v); v += dpp<0x141>(v); v += dpp<0x140>(v); return v; }

constexpr float QSCALE = 0.08838834764831845f * 1.4426950408889634f;
constexpr float THR = 8.f;
constexpr int L_V = 98304;
constexpr int L_WE = 65536, L_GATE = 139264;
#define PIN(x) asm volatile("" : "+v"(x))
#define MX3(a, b, c) __builtin_fmaxf(__builtin_fmaxf((a), (b)), (c))
#define EX2(v) __builtin_amdgcn_exp2f(v)
#define RLX_AGENT __ATOMIC_RELAXED, __HIP_MEMORY_SCOPE_AGENT
#define ST_PL(base, voff, imm, v) asm volatile("s_nop 4\n\tglobal_store_dwordx4 %0, %1, %2 offset:%3\n\ts_nop 1" :: "v"(voff), "v"(v), "s"(base), "i"(imm) : "memory")
#define ST_SC1(base, voff, imm, v) asm volatile("s_nop 4\n\tglobal_store_dwordx4 %0, %1, %2 offset:%3 sc1\n\ts_nop 1" :: "v"(voff), "v"(v), "s"(base), "i"(imm) : "memory")
DI int fresh_lane() { int l; asm volatile("v_mbcnt_lo_u32_b32 %0, -1, 0\n\tv_mbcnt_hi_u32_b32 %0, -1, %0" : "=v"(l)); return l; }
DI void wait_cnt(const unsigned* p, unsigned want) {
    unsigned spins = 0;
    while (rfl(__hip_atomic_load(p, RLX_AGENT)) < want) { __builtin_amdgcn_s_sleep(2); if (++spins > (1u << 20)) break; }
}
DI void handshake_finish(char* ws, int bid, unsigned token, bool ok, int lane) {
    unsigned* CNT = (unsigned*)(ws + WS_CNT); unsigned spins = 0;
    if (bid == 0) {
        for (;;) {
            const unsigned a = __hip_atomic_load(CNT + (80 + (lane & 15)) * 64, RLX_AGENT);
            if (__all(a >= ((lane & 15) ? 16u : 15u))) break;
#pragma unroll
            for (int i = 0; i < 4; ++i) { const unsigned q = __hip_atomic_load((const unsigned*)(ws + WS_HQ) + i * 64 + lane, RLX_AGENT); __hip_atomic_store((unsigned*)(ws + WS_HR) + i * 64 + lane, q, RLX_AGENT); }
            __builtin_amdgcn_s_sleep(8); if (++spins > (1u << 18)) break; }
    } else if (!ok) {
        while (rfl(__hip_atomic_load((const unsigned*)(ws + WS_HR) + bid, RLX_AGENT)) != token) { __builtin_amdgcn_s_sleep(8); if (++spins > (1u << 20)) break; }
        if (lane == 0) __hip_atomic_fetch_add(CNT + (80 + (bid & 15)) * 64, 1u, RLX_AGENT);
    }
}
DI void dual_softmax(f32x16& p) {
#pragma unroll
    for (int hf = 0; hf < 2; ++hf) {
        float mx = -INFINITY;
#pragma unroll
        for (int r = 0; r < 8; ++r) { p[8 * hf + r] = bfround(p[8 * hf + r]); mx = fmaxf(mx, p[8 * hf + r]); }
        mx = swap32_max(mx); float s = 0.f;
#pragma unroll
        for (int r = 0; r < 8; ++r) { p[8 * hf + r] = EX2((p[8 * hf + r] - mx) * 1.4426950408889634f); s += p[8 * hf + r]; }
        s = __builtin_amdgcn_rcpf(swap32_add(s));
#pragma unroll
        for (int r = 0; r < 8; ++r) p[8 * hf + r] *= s;
    }
}
DI void mask_half(f32x16& sc, int dqw) {
    if (dqw < 0) {
#pragma unroll
        for (int r = 0; r < 16; ++r) sc[r] = -INFINITY;
    } else if (dqw == 0) {
        const int ln = fresh_lane(); const int lr = (ln & 31) - 4 * (ln >> 5); const float ninf = -INFINITY;
#pragma unroll
        for (int r = 0; r < 16; ++r)
            asm volatile("s_nop 0\n\tv_cmp_gt_i32 vcc, %c3, %2\n\tv_cndmask_b32 %0, %0, %1, vcc" : "+v"(sc[r]) : "v"(ninf), "v"(lr), "i"(16 * ((r >> 2) & 1) + 8 * (r >> 3) + (r & 3)) : "vcc");
    }
}
DI void mask_tile(f32x16& s0, f32x16& s1, int dqw) {
    asm volatile("s_nop 7\n\ts_nop 7" ::: "memory");
    mask_half(s0, dqw); mask_half(s1, dqw - 32); }

__global__ __launch_bounds__(512, 2) void kf_all(const u16* __restrict__ x, const u16* __restrict__ ckv, const u16* __restrict__ wq, const u16* __restrict__ wuk,
                                                 const u16* __restrict__ wuv, const u16* __restrict__ wo_a, const u16* __restrict__ wqkv,
                                                 const u16* __restrict__ wphi, const u16* __restrict__ wo_b, const u16* __restrict__ gatew,
                                                 char* ws, u16* __restrict__ out) {
    extern __shared__ __attribute__((aligned(16))) char shm[];
    const lds_ptr lds = (lds_ptr)shm; const unsigned lds0 = (unsigned)(uintptr_t)shm;
    const int wid = rfl(threadIdx.x >> 6), G = wid >> 2, w4 = wid & 3;
    const int bid = blockIdx.x, hh_ = 4 * (bid & 7) + (bid >> 6), jj = (bid >> 3) & 7;
#define LANE_VARS() int tid = threadIdx.x; asm volatile("" : "+v"(tid)); const int lane = tid & 63, r32 = lane & 31, hi = lane >> 5, blk = (lane >> 4) & 1, \
        q4 = (lane & 15) >> 2, p4 = lane & 3; const unsigned vlin = (unsigned)lane * 16u; (void)r32; (void)hi; (void)blk; (void)q4; (void)p4; (void)vlin
    unsigned* const CNT = (unsigned*)(ws + WS_CNT);
    char* const KVh = ws + WS_KV + (size_t)hh_ * 32 * 32768;
    char* const A3 = ws + WS_A3;
    STAMP_DECL(); STAMP_RT(); STAMP();
    u32x4 pkv[16];
    unsigned token = 0u;
    const int bA = 15 - jj, bB = jj;
    const char* gX = (const char*)x + (size_t)((G ? bB : bA) * 128 + 32 * w4) * 1024; const char* gW = (const char*)wq + hh_ * 256; const char* gE = (const char*)wqkv + hh_ * 32;
    const unsigned lx = lds0 + wid * 4096, lw = lds0 + L_WE + wid * 1024;
    unsigned voffX, voffW, voffE;
    { const int lane = threadIdx.x & 63;
        { const int row = lane >> 3, ch = (lane & 7) ^ ((row >> 1) & 3); voffX = (unsigned)(row * 1024 + ch * 16); }
        { const int row = 4 * wid + (lane >> 4), j = lane & 15, c = (j >> 2) ^ (row & 3); voffW = (unsigned)(row * 8192 + c * 64 + (j & 3) * 16); }
        { const int row = 8 * wid + (lane >> 3), sl = lane & 7, hf = (sl >> 2) ^ ((row >> 1) & 1), part = hf ? 2 : ((sl >> 1) & 1);
          voffE = (unsigned)(row * 3072 + part * 1024 + (sl & 1) * 16); }
        if (wid == 0) glds16(gatew, (unsigned)lane * 16u, lds0 + L_GATE);
        if (bid == 0 && wid == 1) {
            const unsigned z = 0u;
            asm volatile("global_store_dword %0, %1, %2 sc1\n\tglobal_store_dword %0, %1, %3 sc1\n\ts_nop 1" :: "v"((unsigned)lane * 256u), "v"(z), "s"(ws + WS_CNT), "s"(ws + WS_CNT + 16384) : "memory");
            asm volatile("global_store_dword %0, %1, %2 offset:4 sc1\n\ts_nop 1" :: "v"((unsigned)lane * 256u), "v"(z), "s"(ws + WS_CNT + 16384) : "memory"); }
        PIN(voffX); PIN(voffW); PIN(voffE);
    }
#define XISS(c) do { _Pragma("unroll") for (int e = 0; e < 4; ++e) glds16(gX + e * 8192 + (c) * 128, voffX ^ ((e & 1) << 6), lx + ((c) & 1) * 32768 + e * 1024); } while (0)
#define WISS(c) do { glds16(gW + (size_t)(c) * 524288, voffW, lw + ((c) % 3) * 24576); glds16(gW + (size_t)(c) * 524288 + 262144, voffW, lw + ((c) % 3) * 24576 + 8192); \
                     glds16(gE + (size_t)(c) * 196608, voffE, lw + ((c) % 3) * 24576 + 16384); } while (0)
    {
        LANE_VARS();
        const int wr = wid >> 2, wc = wid & 3, fr = lane & 15, fq = lane >> 4;
        constexpr int HTB = 16384;
#define SA(b, h) (((b) * 2 + (h)) * HTB)
#define SB(b, h) ((4 + (b) * 2 + (h)) * HTB)
        unsigned voffA, voffB;
        { const int row = 8 * wid + (lane >> 3), ch = (lane & 7) ^ ((row >> 1) & 7); voffA = (unsigned)(row * 1024 + ch * 16); }
        { const int row = 4 * wid + (lane >> 4), j = lane & 15, s = ((row >> 3) & 1) * 4 + (row & 3), c = (j >> 1) ^ s; voffB = (unsigned)(row * 8192 + c * 32 + (j & 1) * 16); }
        const char* gA = (const char*)ckv + (size_t)jj * 256 * 1024;
        const char* gB0 = (const char*)wuk + hh_ * 256; const char* gB1 = (const char*)wuv + hh_ * 256;
        const unsigned ldsw = rfl(lds0 + wid * 1024);
#define STAGE_A(b, h, kt) do { const char* g_ = gA + (h) * 131072 + (kt) * 128; glds16(g_, voffA, ldsw + SA(b, h)); glds16(g_ + 65536, voffA, ldsw + SA(b, h) + 8192); } while (0)
#define STAGE_B(b, h, kt) do { const char* g_ = ((h) ? gB1 : gB0) + (size_t)(kt) * 524288; glds16(g_, voffB, ldsw + SB(b, h)); glds16(g_ + 262144, voffB, ldsw + SB(b, h) + 8192); } while (0)
        lds_cptr pa[2], pb[2];
#pragma unroll
        for (int ks = 0; ks < 2; ++ks) pa[ks] = lds + (wr * 64 + fr) * 128 + 16 * ((4 * ks + fq) ^ ((fr >> 1) & 7));
        { const int q = fr >> 2, p = fr & 3, s = (fq & 1) * 4 + q;
#pragma unroll
          for (int n = 0; n < 2; ++n) pb[n] = lds + (8 * fq + q) * 256 + 32 * ((2 * wc + n) ^ s) + 8 * p; }
#define LDA(dst, b, h) do { _Pragma("unroll") for (int m = 0; m < 4; ++m) _Pragma("unroll") for (int k = 0; k < 2; ++k) dst[m][k] = ldsr128(pa[k] + SA(b, h) + m * 2048); } while (0)
#define LDB(dst, b, h) do { _Pragma("unroll") for (int n = 0; n < 2; ++n) _Pragma("unroll") for (int k = 0; k < 2; ++k) \
        dst[n][k] = cat8(trrd(pb[n] + SB(b, h) + k * 8192), trrd(pb[n] + SB(b, h) + k * 8192 + 1024)); } while (0)
#define MMA(ai, bj, At, Bt) do { __builtin_amdgcn_s_setprio(1); _Pragma("unroll") for (int m = 0; m < 4; ++m) _Pragma("unroll") for (int n = 0; n < 2; ++n) _Pragma("unroll") for (int k = 0; k < 2; ++k) \
        acc[ai][bj][m][n] = (bj) ? MFMA16(At[m][k], Bt[n][k], acc[ai][bj][m][n]) : MFMA16(Bt[n][k], At[m][k], acc[ai][bj][m][n]); __builtin_amdgcn_s_setprio(0); } while (0)
        const unsigned scr = lds0 + 143360 + wid * 256; const size_t gw = (size_t)(bid * 8 + wid);
        auto warm = [&](int slot) {
            if (slot == 0 && wid == 0) { token = rfl(*(const LAS unsigned*)(lds + 148480)) + 1u; const unsigned z = 0u, tk = token;
                asm volatile("s_nop 4\n\tglobal_store_dword %0, %1, %2 sc1\n\ts_nop 1" :: "v"(z), "v"(tk), "s"(ws + WS_HQ + bid * 4) : "memory"); }
            if (bid == 0 && wid == 1) {
                if (slot > 0) { const u32x4 rq = *(const LAS u32x4*)(lds + 147456 + lane * 16); ST_SC1(ws + WS_HR, (unsigned)lane * 16u, 0, rq); WAIT_L(0); }
                glds16c(ws + WS_HQ, (unsigned)lane * 16u, lds0 + 147456); }
            const unsigned vo = (unsigned)lane * 128u;
            if (slot == 0) { if (lane < 8) touch8k((const char*)x + gw * 1024, vo, scr); }
            else if (slot == 1) { if (lane < 16) touch8k((const char*)wq + gw * 2048, vo, scr); }
            else if (slot == 2) { if (lane < 6) touch8k((const char*)wqkv + gw * 768, vo, scr); }
            else if (slot == 3) { if (lane < 16) touch8k((const char*)wo_a + gw * 2048, vo, scr); }
            else if (slot == 4) { if (lane < 2) touch8k((const char*)wo_b + gw * 256, vo, scr); }
            if (slot == TLBSLOT) { const int xi = (bid >> 3) * 8 + wid;
                if (lane < 5) touch8k(ws + (size_t)xi * 5 * 65536, (unsigned)lane * 65536u, scr);
                else if (lane == 5 && xi < 32) touch8k((const char*)out + (size_t)xi * 65536, 0u, scr); }
        };
        f32x4 acc[2][2][4][2] = {};
        bf16x8 At[4][2], B0[2][2], B1[2][2];
        constexpr int nt = 8;
        STAGE_B(0, 0, 0); STAGE_A(0, 0, 0); STAGE_B(0, 1, 0); STAGE_A(0, 1, 0);
        if (wid == 0) glds4c(ws + WS_HR + bid * 4, lds0 + 148480);
        if (wr == 1) BAR();
        if (wid == 0) { WAIT_V(5); } else { WAIT_V(4); } BAR();
        STAGE_B(1, 0, 1); STAGE_A(1, 0, 1); STAGE_B(1, 1, 1);
        if (wid == 0) { WAIT_V(7); } else { WAIT_V(6); } BAR();
        STAMP();
        for (int t = 0; t < nt - 2; t += 2) {
            LDB(B0, 0, 0); SBAR(); LDA(At, 0, 0); STAGE_A(1, 1, t + 1);
            WAIT_L(8); BAR(); WAIT_L(0); MMA(0, 0, At, B0); BAR(); SBAR();
            LDB(B1, 0, 1); STAGE_B(0, 0, t + 2);
            BAR(); WAIT_L(0); MMA(0, 1, At, B1); BAR();
            LDA(At, 0, 1); STAGE_A(0, 0, t + 2);
            BAR(); WAIT_L(0); MMA(1, 0, At, B0); BAR(); SBAR();
            STAGE_B(0, 1, t + 2);
            WAIT_V(6); BAR(); MMA(1, 1, At, B1); BAR();
            warm(t);
            LDB(B0, 1, 0); SBAR(); LDA(At, 1, 0); STAGE_A(0, 1, t + 2);
            WAIT_L(8); BAR(); WAIT_L(0); MMA(0, 0, At, B0); BAR(); SBAR();
            LDB(B1, 1, 1); STAGE_B(1, 0, t + 3);
            BAR(); WAIT_L(0); MMA(0, 1, At, B1); BAR();
            LDA(At, 1, 1); STAGE_A(1, 0, t + 3);
            BAR(); WAIT_L(0); MMA(1, 0, At, B0); BAR(); SBAR();
            STAGE_B(1, 1, t + 3);
            WAIT_V(6); BAR(); MMA(1, 1, At, B1); BAR();
            warm(t + 1);
        }
        { LDB(B0, 0, 0); WAIT_V(0); LDA(At, 0, 0); STAGE_A(1, 1, nt - 1);
          BAR(); WAIT_L(0); MMA(0, 0, At, B0); BAR();
          LDB(B1, 0, 1); BAR(); WAIT_L(0); MMA(0, 1, At, B1); BAR();
          LDA(At, 0, 1); BAR(); WAIT_L(0); MMA(1, 0, At, B0); MMA(1, 1, At, B1); BAR(); }
        { LDB(B0, 1, 0); LDA(At, 1, 0); BAR(); WAIT_L(0); MMA(0, 0, At, B0); BAR();
          LDB(B1, 1, 1); WAIT_V(0);
          WISS(0); XISS(0);
          BAR(); WAIT_L(0); MMA(0, 1, At, B1); BAR();
          LDA(At, 1, 1); BAR(); WAIT_L(0); MMA(1, 0, At, B0); MMA(1, 1, At, B1); BAR(); }
        if (wr == 0) BAR();
        WISS(1); XISS(1);
        if (wid == 0) glds4c(ws + WS_HR + bid * 4, lds0 + 148480);
        SBAR();
#undef SA
#undef SB
#undef STAGE_A
#undef STAGE_B
#undef LDA
#undef LDB
#undef MMA
#pragma unroll
        for (int ai = 0; ai < 2; ++ai) {
#pragma unroll
            for (int m = 0; m < 4; ++m) { const f32x4 a = acc[ai][0][m][0], b = acc[ai][0][m][1];
                pkv[ai * 4 + m] = (u32x4){cvtpk(a[0], a[1]), cvtpk(a[2], a[3]), cvtpk(b[0], b[1]), cvtpk(b[2], b[3])}; }
#pragma unroll
            for (int u = 0; u < 2; ++u)
#pragma unroll
                for (int n = 0; n < 2; ++n) { const f32x4 a = acc[ai][1][2 * u][n], b = acc[ai][1][2 * u + 1][n];
                    pkv[8 + ai * 4 + u * 2 + n] = (u32x4){cvtpk(a[0], a[1]), cvtpk(a[2], a[3]), cvtpk(b[0], b[1]), cvtpk(b[2], b[3])}; }
        }
    }
    STAMP();
    const char* kvw = KVh + w4 * 4096;
    const unsigned ldw = lds0 + w4 * 4096;
    char* const qbp = ws + WS_QB + (size_t)(bid * 4 + w4) * 8192;
    char* const pqp = ws + WS_PQ + (size_t)(bid * 8 + wid) * 2048;
    bf16x8 qf[8]; float gate;
    {
        LANE_VARS();
        u16 wph[8];
#pragma unroll
        for (int j = 0; j < 8; ++j) wph[j] = wphi[hh_ * 256 + (8 * (j >> 2) + 4 * hi + (j & 3)) * 16 + (lane & 15)];
        const char* tb0 = KVh + (size_t)(4 * jj + G) * 32768;
        const unsigned vok = (unsigned)((4 * w4 + (lane >> 4)) * 1024 + (lane & 15) * 16), vov = (unsigned)(16384 + (lane >> 4) * 2048 + (w4 * 32 + (lane & 15)) * 16);
#define KVST(i) do { if ((i) < 8) ST_SC1(tb0 + ((i) >> 2) * 65536, vok, ((i) & 3) * 256, pkv[i]); \
                     else ST_SC1(tb0 + (((i) >> 2) & 1) * 65536 + (((i) >> 1) & 1) * 8192, vov, ((i) & 1) * 256, pkv[i]); } while (0)
        f32x16 qa[5] = {}, d1 = {};
        unsigned xo[2], wo_[4];
#pragma unroll
        for (int s = 0; s < 2; ++s) xo[s] = (unsigned)((32 * wid + r32) * 128 + 16 * ((2 * s + hi) ^ ((r32 >> 1) & 7)));
#pragma unroll
        for (int c = 0; c < 4; ++c) wo_[c] = (unsigned)(L_WE + (8 * hi + q4) * 256 + 64 * (c ^ q4) + 32 * (p4 >> 1) + 16 * blk + 8 * (p4 & 1));
        const unsigned eo = (unsigned)(L_WE + 16384 + (8 * hi + q4) * 128 + 64 * ((q4 >> 1) & 1) + 32 * blk + 8 * p4);
        const unsigned vo_ = (unsigned)(L_WE + 16384 + (8 * hi + q4) * 128 + 64 * (((q4 >> 1) & 1) ^ 1) + 8 * p4);
        bf16x8 fx[2], fw[2][6], fg[2];
#define QR_(buf, c, ks, u) do { \
        if ((u) == 0) { fx[buf] = ldsr128(lds + ((c) & 1) * 32768 + (xo[(ks) & 1] ^ (((ks) >> 1) << 6))); fg[buf] = ldsr128(lds + L_GATE + ((c) * 64 + (ks) * 16 + 8 * hi) * 2); } \
        else if ((u) < 5) { fw[buf][(u) - 1] = cat8(trrd(lds + wo_[(u) - 1] + ((c) % 3) * 24576 + (ks) * 4096), trrd(lds + wo_[(u) - 1] + ((c) % 3) * 24576 + (ks) * 4096 + 1024)); } \
        else { fw[buf][4] = cat8(trrd(lds + eo + ((c) % 3) * 24576 + (ks) * 2048), trrd(lds + eo + ((c) % 3) * 24576 + (ks) * 2048 + 512)); \
               fw[buf][5] = cat8(trrd(lds + vo_ + ((c) % 3) * 24576 + (ks) * 2048), trrd(lds + vo_ + ((c) % 3) * 24576 + (ks) * 2048 + 512)); } } while (0)
#define QM_(buf, u) do { if ((u) < 5) qa[u] = MFMA32(fw[buf][u], fx[buf], qa[u]); else d1 = MFMA32(fx[buf], blk ? fg[buf] : fw[buf][5], d1); } while (0)
#define QRD(buf, c, ks) do { _Pragma("unroll") for (int u_ = 0; u_ < 6; ++u_) QR_(buf, c, ks, u_); } while (0)
#define QSTEP(buf, c, ks, rd) do { _Pragma("unroll") for (int u_ = 0; u_ < 6; ++u_) { if (rd) QR_((buf) ^ 1, c, ks, u_); QM_(buf, u_); SBAR(); } } while (0)
#define WV_(n) asm volatile("s_waitcnt vmcnt(%0)" :: "i"(n) : "memory")
#define NST(c) ((c) < 0 ? 0 : (c) < 4 ? 3 : (c) < 6 ? 2 : 0)
#define ST0(c) ((c) < 4 ? 3 * (c) : 12 + 2 * ((c) - 4))
#define QCHUNK(c) do { \
        SBAR(); QSTEP(0, c, 1, true); QSTEP(1, c, 2, true); \
        if ((c) < 7) { WV_(4 + NST((c) - 1)); } BAR(); asm volatile("" ::: "memory"); \
        if ((c) == 7 && wid == 0) { handshake_finish(ws, bid, token, hs_ok, lane); if (lane == 0) __hip_atomic_fetch_add(CNT + hh_ * 64, 1u, RLX_AGENT); } \
        if ((c) < 6) WISS((c) + 2); \
        _Pragma("unroll") for (int i_ = 0; i_ < NST(c); ++i_) KVST(ST0(c) + i_); \
        SBAR(); QSTEP(0, c, 3, true); \
        if ((c) < 7) { WV_(((c) < 6 ? 3 : 0) + NST(c)); } \
        SBAR(); QSTEP(1, (c) + 1, 0, (c) < 7); \
        if ((c) < 6) XISS((c) + 2); } while (0)
        if (wid == 0) { WAIT_V(8); } else { WAIT_V(7); } BAR(); asm volatile("" ::: "memory");
        bool hs_ok = true;
        bf16x8 wpf;
#pragma unroll
        for (int j = 0; j < 8; ++j) wpf[j] = (short)(blk ? (wph[j] ^ 0x8000) : wph[j]);
        PIN(wpf);
        STAMP();
        QRD(0, 0, 0);
        QCHUNK(0);
        {
            unsigned t0, t1, seen, em;
            asm volatile("v_mov_b32 %0, %5\n\tds_read_b32 %0, %0\n\tv_mov_b32 %1, 1\n\ts_waitcnt lgkmcnt(0)\n\tv_readfirstlane_b32 %2, %0\n\tv_mov_b32 %0, 0\n\ts_nop 3\n\t"
                         "s_min_u32 %3, %8, 1\n\ts_cmp_eq_u32 %7, 0\n\ts_cselect_b32 %3, %3, 0\n\t"
                         "s_cmp_eq_u32 %2, %6\n\ts_cselect_b32 %2, 1, 0\n\ts_and_b32 %3, %3, %2\n\ts_mov_b32 exec_lo, %3\n\ts_mov_b32 exec_hi, 0\n\t"
                         "global_atomic_add %0, %1, %4\n\ts_mov_b64 exec, -1"
                         : "=&v"(t0), "=&v"(t1), "=&s"(seen), "=&s"(em) : "s"(CNT + (80 + (bid & 15)) * 64), "s"(lds0 + 148480), "s"(token), "s"(wid), "s"(bid) : "memory", "scc");
            hs_ok = seen != 0u; }
        QCHUNK(1); QCHUNK(2); QCHUNK(3); QCHUNK(4); QCHUNK(5); QCHUNK(6); QCHUNK(7);
#undef QRD
#undef QR_
#undef QM_
#undef QSTEP
#undef QCHUNK
#undef XISS
#undef WISS
#undef KVST
#undef NST
#undef ST0
        STAMP();
        BAR(); asm volatile("" ::: "memory");
        unsigned f1 = __hip_atomic_load(CNT + hh_ * 64, RLX_AGENT);
#pragma unroll
        for (int cc = 0; cc < 4; ++cc) {
#pragma unroll
            for (int r = 0; r < 16; ++r) qa[cc][r] *= QSCALE;
            qf[2 * cc] = PACK8(qa[cc], 0); qf[2 * cc + 1] = PACK8(qa[cc], 8);
        }
        if (rfl(f1) < 8u) wait_cnt(CNT + hh_ * 64, 8u);
        asm volatile("" ::: "memory");
        STAMP();
#pragma unroll
        for (int e = 0; e < 4; ++e) glds16c(kvw + (size_t)(e >> 1) * 32768 + (2 * G + (e & 1)) * 1024, vlin, ldw + (e >> 1) * 16384 + (2 * G + (e & 1)) * 1024);
        LAS float* red = (LAS float*)(lds + L_V + G * 32768 + w4 * 4096);
        if (r32 == 16) {
#pragma unroll
            for (int r = 0; r < 16; ++r) red[(r & 3) + 8 * (r >> 2) + 4 * hi] = d1[r];
        }
        WAIT_L(0);
        { const float gz = bfround(red[r32]); gate = bfround(1.f / (1.f + __expf(-gz))); }
        f32x16 pq = MFMA32(wpf, PACK8(qa[4], 0), f32x16{});
        f32x16 pk = MFMA32(wpf, PACK8(qa[4], 8), f32x16{});
        dual_softmax(pq); dual_softmax(pk);
        f32x16 d3;
        { bf16x8 id0, id1;
#pragma unroll
          for (int j = 0; j < 8; ++j) { const int f = 8 * (j >> 2) + 4 * hi + (j & 3); id0[j] = (short)(r32 == f ? 0x3F80 : 0); id1[j] = (short)(r32 == f + 16 ? 0x3F80 : 0); }
          d3 = MFMA32(PACK8(pk, 0), id0, f32x16{}); d3 = MFMA32(PACK8(pk, 8), id1, d3); }
        { const bf16x8 t0 = PACK8(pq, 0), t1 = PACK8(pq, 8); ST_PL(pqp, vlin, 0, t0); ST_PL(pqp, vlin, 1024, t1); }
        if (G == 0) {
#pragma unroll
            for (int s = 0; s < 8; ++s) ST_PL(qbp + (s >> 2) * 4096, vlin, (s & 3) * 1024, qf[s]);
        }
        const bf16x8 ones = {0x3F80, 0x3F80, 0x3F80, 0x3F80, 0x3F80, 0x3F80, 0x3F80, 0x3F80};
        const bf16x8 v0 = PACK8(d1, 0), v1 = PACK8(d1, 8);
        f32x16 d4 = MFMA32(PACK8(d3, 0), blk ? ones : v0, f32x16{});
        d4 = MFMA32(PACK8(d3, 8), blk ? ones : v1, d4);
#pragma unroll
        for (int r = 0; r < 16; ++r) red[((r & 3) + 8 * (r >> 2) + 4 * hi) * 32 + r32] = d4[r];
        WAIT_L(0); BAR(); asm volatile("" ::: "memory");
        if (tid < 256) {
            f32x4 s = {0.f, 0.f, 0.f, 0.f};
#pragma unroll
            for (int w = 0; w < 8; ++w) s += *(const LAS f32x4*)(lds + L_V + (w >> 2) * 32768 + (w & 3) * 4096 + tid * 16);
            ST_SC1(ws + WS_KVP + (size_t)(hh_ * 8 + jj) * 4096, (unsigned)tid * 16u, 0, s);
        }
        if (G) { WAIT_BAR(2); } else { WAIT_BAR(11); }
    }
    STAMP();
    {
    LANE_VARS();
    const int prow = (r32 & 7) | ((r32 & 8) << 1) | ((r32 & 16) >> 1);
    const unsigned kro = (unsigned)(prow * 16 + hi * 1024), vro = (unsigned)(L_V + r32 * 16 + hi * 2048);
    const int dq0 = 32 * w4;
    const int T = 2 * jj + 2;
    unsigned ks0 = 0u, ks1 = 16384u, ks2 = 32768u;
    int rho = 0; const int TG = G ? T : 99;
    f32x16 o[4], negm, sA0, sA1, sB0, sB1; bf16x8 kf[16], vf[16]; u32x4 pw[4]; float m, l;
#define KRD(i, kp) kf[i] = ldsr128((kp) + ((i) >> 1) * 2048 + ((i) & 1) * 512)
#define VRD(i, vp) vf[i] = ldsr128((vp) + ((i) >> 2) * 4096 + ((i) & 3) * 512)
#define PF(k) __builtin_bit_cast(bf16x8, pw[k])
#define DMA1(i) do { if ((i) < 2) glds16c(gk_ + (i) * 1024, vlin, dk_ + (i) * 1024); else if ((i) < 4) glds16c_unless(gk_ + (i) * 1024, vlin, dk_ + (i) * 1024, rk_, T); \
                     else if ((i) < 6) glds16c(gv_ + ((i) - 4) * 1024, vlin, dv_ + ((i) - 4) * 1024); else glds16c_unless(gv_ + ((i) - 4) * 1024, vlin, dv_ + ((i) - 4) * 1024, rho, T); } while (0)
    const char* gk_; const char* gv_; unsigned dk_, dv_, ko_, vo_; int rk_;
    unsigned go1, go2;
#define GOFF(r) ((r) < T ? (unsigned)(r) * 32768u + G * 2048u : (unsigned)(2 * (r) - T + G) * 32768u)
#define PREP_A(r, k0) do { ko_ = ((r) >= TG ? 49152u : 0u) + (k0); vo_ = ((r) - 1 >= TG ? 32768u : 0u) + (((r) - 1) & 1) * 16384u; } while (0)
#define PREP_B(r, gcur) do { gv_ = kvw + ((gcur) + 16384u); dv_ = ldw + L_V + ((r) & 1) * 16384u + ((r) < T ? G * 2048u : G * 32768u); } while (0)
#define PREP_C(r) do { rk_ = (r) + 2 <= 16 ? (r) + 2 : 16; go1 = go2; go2 = GOFF(rk_); } while (0)
#define PREP_D(k2) do { gk_ = kvw + go2; dk_ = ldw + (k2) + (rk_ < T ? G * 2048u : G * 49152u); } while (0)
#define ROT() do { const unsigned t_ = ks0; ks0 = ks1; ks1 = ks2; ks2 = t_; ++rho; } while (0)
#define GAPA(i, C0, C1, P0, P1) do { \
        if ((i) + 4 < 16) { KRD((i) + 4, kp_); } else { VRD((i) - 12, vp_); } \
        if (((i) & 1) == 0) DMA1((i) >> 1); \
        if ((i) & 1) C1 = MFMA32(kf[i], qf[(i) >> 1], (i) < 2 ? negm : C1); else C0 = MFMA32(kf[i], qf[(i) >> 1], (i) < 2 ? negm : C0); \
        if ((i) < 8) { P1[2 * (i)] = EX2(P1[2 * (i)]); P1[2 * (i) + 1] = EX2(P1[2 * (i) + 1]); pw[(i) >> 2][(i) & 3] = cvtpk(P0[2 * (i)], P0[2 * (i) + 1]); PIN(P1); PIN(pw[(i) >> 2]); } \
        else { l += P1[2 * (i) - 16]; l2 += P1[2 * (i) - 15]; pw[(i) >> 2][(i) & 3] = cvtpk(P1[2 * (i) - 16], P1[2 * (i) - 15]); PIN(l); PIN(l2); PIN(pw[(i) >> 2]); } \
        SBAR(); } while (0)
#define PVM(i) o[(i) & 3] = MFMA32(vf[i], PF((i) >> 2), o[(i) & 3])
#define RDB(i) do { if ((i) + 4 < 16) { VRD((i) + 4, vp_); } else { KRD((i) - 12, kn_); } } while (0)
#define ROUND(C0, C1, P0, P1) do { \
        const lds_cptr kp_ = lds + ko_ + kro; const lds_cptr vp_ = lds + vo_ + vro; \
        float l2 = 0.f; lds_cptr kn_ = kp_; SBAR(); \
        GAPA(0, C0, C1, P0, P1); GAPA(1, C0, C1, P0, P1); GAPA(2, C0, C1, P0, P1); GAPA(3, C0, C1, P0, P1); \
        GAPA(4, C0, C1, P0, P1); GAPA(5, C0, C1, P0, P1); GAPA(6, C0, C1, P0, P1); GAPA(7, C0, C1, P0, P1); \
        GAPA(8, C0, C1, P0, P1); GAPA(9, C0, C1, P0, P1); GAPA(10, C0, C1, P0, P1); GAPA(11, C0, C1, P0, P1); \
        GAPA(12, C0, C1, P0, P1); GAPA(13, C0, C1, P0, P1); GAPA(14, C0, C1, P0, P1); GAPA(15, C0, C1, P0, P1); \
        l += l2; \
        ldq_unless(qf, qbp, vlin, (unsigned)(rho + 1 - TG));        \
        RDB(0); PVM(0); SBAR(); RDB(1); PVM(1); SBAR(); \
        if (__builtin_expect(rho == 16 || (rho >= TG - 2 && rho < TG), 0)) mask_tile(C0, C1, dq0 - (rho == 16 ? 64 * G : 64 * (rho - TG + 2))); \
        float ma, mb; \
        RDB(2); PVM(2); ma = MX3(C0[0], C0[1], C1[0]); mb = MX3(C0[2], C0[3], C1[1]); ma = MX3(ma, C1[2], C1[3]); mb = MX3(mb, C0[4], C0[5]); PIN(ma); PIN(mb); SBAR(); \
        RDB(3); PVM(3); ma = MX3(ma, C0[6], C0[7]); mb = MX3(mb, C1[4], C1[5]); ma = MX3(ma, C1[6], C1[7]); mb = MX3(mb, C0[8], C0[9]); PIN(ma); PIN(mb); SBAR(); \
        RDB(4); PVM(4); ma = MX3(ma, C0[10], C0[11]); mb = MX3(mb, C1[8], C1[9]); ma = MX3(ma, C1[10], C1[11]); mb = MX3(mb, C0[12], C0[13]); PIN(ma); PIN(mb); SBAR(); \
        RDB(5); PVM(5); ma = MX3(ma, C0[14], C0[15]); mb = MX3(mb, C1[12], C1[13]); ma = MX3(ma, C1[14], C1[15]); \
        const float rm = swap32_max(fmaxf(ma, mb)); bool pend = false; float fsc = 1.f; \
        if (__builtin_expect(__any(rm > THR) || rho == TG, 0)) { float dl; pend = true; \
            if (rho == TG) { dl = fmaxf(rm, -1e30f); m = dl; fsc = gate / swap32_add(l); l = 0.f; }        \
            else { dl = fmaxf(rm, 0.f); m += dl; fsc = EX2(-dl); l *= fsc; } \
            _Pragma("unroll") for (int e = 0; e < 16; ++e) { C0[e] -= dl; C1[e] -= dl; negm[e] = -m; } } \
        SBAR(); \
        RDB(6); PVM(6); C0[0] = EX2(C0[0]); C0[1] = EX2(C0[1]); PIN(C0); SBAR(); \
        RDB(7); PVM(7); C0[2] = EX2(C0[2]); C0[3] = EX2(C0[3]); l += C0[0]; l2 = C0[1]; PIN(C0); PIN(l); SBAR(); \
        RDB(8); PVM(8); PREP_A(rho + 1, ks1); kn_ = lds + ko_ + kro; C0[4] = EX2(C0[4]); C0[5] = EX2(C0[5]); l += C0[2]; l2 += C0[3]; PIN(C0); PIN(l); PIN(l2); SBAR(); \
        RDB(9); PVM(9); PREP_B(rho + 1, go1); C0[6] = EX2(C0[6]); C0[7] = EX2(C0[7]); l += C0[4]; l2 += C0[5]; PIN(C0); PIN(l); PIN(l2); SBAR(); \
        RDB(10); PVM(10); PREP_C(rho + 1); C0[8] = EX2(C0[8]); C0[9] = EX2(C0[9]); l += C0[6]; l2 += C0[7]; PIN(C0); PIN(l); PIN(l2); SBAR(); \
        RDB(11); PVM(11); PREP_D(ks0); C0[10] = EX2(C0[10]); C0[11] = EX2(C0[11]); l += C0[8]; l2 += C0[9]; PIN(C0); PIN(l); PIN(l2); SBAR(); \
        RDB(12); PVM(12); C0[12] = EX2(C0[12]); C0[13] = EX2(C0[13]); l += C0[10]; l2 += C0[11]; PIN(C0); PIN(l); PIN(l2); SBAR(); \
        RDB(13); PVM(13); C0[14] = EX2(C0[14]); C0[15] = EX2(C0[15]); l += C0[12]; l2 += C0[13]; PIN(C0); PIN(l); PIN(l2); SBAR(); \
        RDB(14); PVM(14); l += C0[14]; l2 += C0[15]; PIN(l); PIN(l2); SBAR(); \
        RDB(15); PVM(15); l += l2; PIN(l); SBAR(); \
        if (__builtin_expect(pend || rho + 1 == TG, 0)) { \
            if (pend) { _Pragma("unroll") for (int c = 0; c < 4; ++c) _Pragma("unroll") for (int e = 0; e < 16; ++e) o[c][e] *= fsc; } \
            if (rho + 1 == TG) negm = f32x16{}; \
            if (rho == TG) { const int ln = fresh_lane(); const unsigned vod = (unsigned)((ln >> 5) * 2048 + (32 * w4 + (ln & 31)) * 16); const char* dst = A3 + (size_t)(bB * 576 + hh_ * 16) * 2048; \
                _Pragma("unroll") for (int c = 0; c < 4; ++c) { const bf16x8 t0 = PACK8(o[c], 0), t1 = PACK8(o[c], 8); ST_SC1(dst + c * 8192, vod, 0, t0); ST_SC1(dst + c * 8192 + 4096, vod, 0, t1); o[c] = f32x16{}; } } } \
        asm volatile("s_cmp_eq_u32 %0, %1\n\ts_cbranch_scc1 .Lw%=\n\ts_waitcnt vmcnt(0)\n.Lw%=:\n\ts_waitcnt vmcnt(8)\n\ts_barrier" :: "s"(rho), "s"(TG) : "memory", "scc");     \
        if (__builtin_expect(rho == TG + 1 && w4 == 0, 0)) { if (fresh_lane() == 0) __hip_atomic_fetch_or(CNT + (64 + bB) * 64, 1u << hh_, RLX_AGENT); }        \
        ROT(); } while (0)

#define ROUND0(C0, C1) do { \
        const lds_cptr kp_ = lds + ks0 + kro; const lds_cptr kn_ = lds + ks1 + kro; \
        go2 = GOFF(1); PREP_B(0, GOFF(0)); PREP_C(0); PREP_D(ks2); \
        _Pragma("unroll") for (int i = 0; i < 6; ++i) KRD(i, kp_); \
        _Pragma("unroll") for (int i = 0; i < 8; ++i) DMA1(i); \
        _Pragma("unroll") for (int c = 0; c < 4; ++c) o[c] = f32x16{}; \
        PREP_A(1, ks1); PREP_B(1, go1); PREP_C(1); PREP_D(ks0); SBAR(); \
        _Pragma("unroll") for (int i = 0; i < 16; ++i) { \
            if (i + 6 < 16) KRD(i + 6, kp_); \
            if (i & 1) C1 = MFMA32(kf[i], qf[i >> 1], i < 2 ? f32x16{} : C1); else C0 = MFMA32(kf[i], qf[i >> 1], i < 2 ? f32x16{} : C0); \
            SBAR(); } \
        if (TG == 2) mask_tile(C0, C1, dq0); \
        float ma = MX3(C0[0], C0[1], C1[0]), mb = MX3(C0[2], C0[3], C1[1]); ma = MX3(ma, C1[2], C1[3]); \
        _Pragma("unroll") for (int r = 4; r < 16; r += 4) { ma = MX3(ma, C0[r], C0[r + 1]); mb = MX3(mb, C0[r + 2], C0[r + 3]); ma = MX3(ma, C1[r], C1[r + 1]); mb = MX3(mb, C1[r + 2], C1[r + 3]); } \
        m = fmaxf(swap32_max(fmaxf(ma, mb)), -1e30f); l = 0.f; \
        _Pragma("unroll") for (int i = 0; i < 4; ++i) KRD(i, kn_); \
        _Pragma("unroll") for (int e = 0; e < 16; ++e) { C0[e] = EX2(C0[e] - m); l += C0[e]; C1[e] -= m; negm[e] = -m; } \
        asm volatile("s_waitcnt vmcnt(0)\n\ts_barrier" ::: "memory"); ROT(); } while (0)
#define DRAIN(P0, P1, MID) do { \
        const lds_cptr vp_ = lds + G * 32768 + vro; \
        _Pragma("unroll") for (int i = 0; i < 8; ++i) VRD(i, vp_); \
        _Pragma("unroll") for (int i = 0; i < 8; ++i) pw[i >> 2][i & 3] = cvtpk(P0[2 * i], P0[2 * i + 1]); \
        SBAR(); \
        _Pragma("unroll") for (int i = 0; i < 16; ++i) { \
            if (i + 8 < 16) VRD(i + 8, vp_); \
            if (i == 8) { MID } \
            PVM(i); \
            if (i < 8) { P1[2 * i] = EX2(P1[2 * i]); P1[2 * i + 1] = EX2(P1[2 * i + 1]); PIN(P1); } \
            if (i >= 1 && i < 9) { l += P1[2 * i - 2]; l += P1[2 * i - 1]; pw[2 + ((i - 1) >> 2)][(i - 1) & 3] = cvtpk(P1[2 * i - 2], P1[2 * i - 1]); PIN(l); PIN(pw[2 + ((i - 1) >> 2)]); } \
            SBAR(); } \
        l = swap32_add(l); } while (0)
    ROUND0(sA0, sA1);
    if (wid == 0 && fresh_lane() == 0) __hip_atomic_fetch_add(CNT + (32 + hh_) * 64, 1u, RLX_AGENT);
    STAMP();
#pragma clang loop unroll(disable)
    for (int pr = 0; pr < 8; ++pr) { ROUND(sB0, sB1, sA0, sA1); ROUND(sA0, sA1, sB0, sB1); }
    STAMP();
    {
        f32x2 ps[8]; bf16x8 phq[2];
        LANE_VARS();
        const unsigned f2 = __hip_atomic_load(CNT + (32 + hh_) * 64, RLX_AGENT);
        DRAIN(sA0, sA1,
            if (rfl(f2) < 8u) wait_cnt(CNT + (32 + hh_) * 64, 8u);
            _Pragma("unroll") for (int w = 0; w < 8; ++w) ps[w] = __builtin_bit_cast(f32x2, __hip_atomic_load((const unsigned long long*)(ws + WS_KVP + (size_t)(hh_ * 8) * 4096 + (unsigned)(w * 4096 + tid * 8)), RLX_AGENT));
            phq[0] = *(const bf16x8*)(pqp + vlin); phq[1] = *(const bf16x8*)(pqp + (vlin + 1024)); );
        const unsigned vod = (unsigned)(hi * 2048 + (32 * w4 + r32) * 16);
        BAR(); asm volatile("" ::: "memory");
        const unsigned st0 = ks2;
        { const lds_ptr xp = lds + L_V + wid * 8192 + lane * 16;
#pragma unroll
          for (int c = 0; c < 2; ++c)
#pragma unroll
              for (int g = 0; g < 4; ++g) { f32x4 t;
#pragma unroll
                  for (int e = 0; e < 4; ++e) t[e] = G ? o[c][4 * g + e] : o[2 + c][4 * g + e];
                  *(LAS f32x4*)(xp + (c * 4 + g) * 1024) = t; }
          *(LAS f32x4*)(lds + st0 + wid * 1024 + lane * 16) = (f32x4){m, l, gate, 0.f};
          f32x2 s = ps[0];
#pragma unroll
          for (int w = 1; w < 8; ++w) s += ps[w];
          *(LAS f32x2*)(lds + st0 + 8192 + tid * 8) = s; }
        WAIT_L(0); BAR(); asm volatile("" ::: "memory");
        { const int ow = wid ^ 4;
          const f32x4 st = *(const LAS f32x4*)(lds + st0 + ow * 1024 + lane * 16);
          const float mn = fmaxf(m, st[0]), f0 = EX2(m - mn), f1 = EX2(st[0] - mn);
          const float gg = G ? st[2] : gate;
          const float inv = gg / (l * f0 + st[1] * f1); const float c0 = f0 * inv, c1 = f1 * inv;
          const lds_cptr xp = lds + L_V + ow * 8192 + lane * 16;
          const char* dst = A3 + (size_t)(bA * 576 + hh_ * 16 + G * 8) * 2048;
#pragma unroll
          for (int c = 0; c < 2; ++c) {
              float v[16];
#pragma unroll
              for (int g = 0; g < 4; ++g) { const f32x4 t = *(const LAS f32x4*)(xp + (c * 4 + g) * 1024);
#pragma unroll
                  for (int e = 0; e < 4; ++e) v[4 * g + e] = (G ? o[2 + c][4 * g + e] : o[c][4 * g + e]) * c0 + t[e] * c1; }
              { const bf16x8 t0 = PACK8(v, 0), t1 = PACK8(v, 8); ST_SC1(dst + c * 8192, vod, 0, t0); ST_SC1(dst + c * 8192 + 4096, vod, 0, t1); }
          }
          float kvv[2][8];
          const int col = r32 < 16 ? r32 : 16;
#pragma unroll
          for (int s = 0; s < 2; ++s)
#pragma unroll
              for (int j = 0; j < 8; ++j) { const int f = 16 * s + 8 * (j >> 2) + 4 * hi + (j & 3); const float v = *(const LAS float*)(lds + st0 + 8192 + (f * 32 + col) * 4); kvv[s][j] = r32 <= 16 ? v : 0.f; }
          f32x16 nm = MFMA32(PACK8(kvv[0], 0), phq[0], f32x16{});
          nm = MFMA32(PACK8(kvv[1], 0), phq[1], nm);
          const float den = bfround(bfround(lo32_bcast(nm[8])) + 1e-6f); const float sc = (1.f - gate) / den;
          float ov[8];
#pragma unroll
          for (int r = 0; r < 8; ++r) ov[r] = bfround(nm[r]) * sc;
          const bf16x8 t0 = PACK8(ov, 0);
          ST_SC1(A3 + (size_t)((G ? bB : bA) * 576 + 512 + 2 * hh_) * 2048, vod, 0, t0);
        }
        WAIT_L(0); BAR(); asm volatile("" ::: "memory");
        STAMP();
    }
#undef KRD
#undef VRD
#undef PF
#undef DMA1
#undef ROT
#undef GOFF
#undef PREP_A
#undef PREP_B
#undef PREP_C
#undef PREP_D
#undef GAPA
#undef PVM
#undef RDB
#undef ROUND
#undef ROUND0
#undef DRAIN
    }
    {
        LANE_VARS();
        const int rt = 4 * (bid & 7) + (bid >> 6), ct = (bid >> 3) & 7;
        unsigned voffW; { const int row = lane >> 3, sl = lane & 7, nb = (sl >> 2) ^ ((row >> 1) & 1); voffW = (unsigned)(row * 1024 + nb * 64 + (sl & 3) * 16); }
        const char* gA = A3 + (size_t)((rt >> 1) * 576 + 2 * wid) * 2048 + (rt & 1) * 1024;
        const char* gWa = (const char*)wo_a + (size_t)wid * 16384 + ct * 128; const char* gWb = (const char*)wo_b + (size_t)wid * 16384 + ct * 128;
        const unsigned wb = lds0 + wid * 20480;
        const unsigned* rdy = CNT + (64 + (rt >> 1)) * 64;
        auto issue = [&](int u, unsigned slot) {
            const char* ga = gA + (size_t)u * 32768; const char* gw = u < 32 ? gWa + (size_t)u * 131072 : gWb + (size_t)(u - 32) * 131072;
            glds16c(ga, vlin, wb + slot); glds16c(ga + 2048, vlin, wb + slot + 1024); glds16(gw, voffW, wb + slot + 2048); glds16(gw + 8192, voffW, wb + slot + 3072); };
        unsigned seen = 0u; int hnext = 0;
        auto next_head = [&]() -> int {
            if (hnext >= 32) { if (hnext == 32) { unsigned spins = 0; while (rfl(__hip_atomic_load(rdy + 1, RLX_AGENT)) != ~0u) { __builtin_amdgcn_s_sleep(2); if (++spins > (1u << 20)) break; }
                                             asm volatile("" ::: "memory"); } }
            else if (!((seen >> hnext) & 1u)) { unsigned spins = 0;
                for (;;) { seen = rfl(__hip_atomic_load(rdy, RLX_AGENT)); if (((seen >> hnext) & 1u) || ++spins > (1u << 20)) break; __builtin_amdgcn_s_sleep(2); }
                asm volatile("" ::: "memory"); }
            return hnext++; };
        asm volatile("s_waitcnt vmcnt(0)\n\ts_barrier" ::: "memory");
        if (tid == 0) { __hip_atomic_fetch_or(CNT + (64 + bA) * 64, 1u << hh_, RLX_AGENT); __hip_atomic_fetch_or(CNT + (64 + bB) * 64, 1u << hh_, RLX_AGENT);
                        __hip_atomic_fetch_or(CNT + (64 + bA) * 64 + 1, 1u << hh_, RLX_AGENT); __hip_atomic_fetch_or(CNT + (64 + bB) * 64 + 1, 1u << hh_, RLX_AGENT); }
        STAMP();
#pragma unroll
        for (int i = 0; i < 5; ++i) issue(next_head(), i * 4096u);
        f32x16 acc[2][2] = {};
        const unsigned ao = (unsigned)(wid * 20480 + hi * 1024 + r32 * 16);
        unsigned bo[2];
#pragma unroll
        for (int nb = 0; nb < 2; ++nb) bo[nb] = (unsigned)(wid * 20480 + 2048 + (4 * hi + q4) * 128 + 64 * (nb ^ ((q4 >> 1) & 1)) + 32 * blk + 8 * p4);
        bf16x8 af[2][2], bf[2][2];
#define K3RD(buf, sb) do { af[buf][0] = ldsr128((sb) + ao); bf[buf][0] = cat8(trrd((sb) + bo[0]), trrd((sb) + bo[0] + 1024)); \
        af[buf][1] = ldsr128((sb) + ao + 512); bf[buf][1] = cat8(trrd((sb) + bo[1]), trrd((sb) + bo[1] + 1024)); } while (0)
#define K3BODY(buf, i) do { \
        const unsigned snx = sc == 16384u ? 0u : sc + 4096u; \
        if ((i) + 1 < 36) K3RD(buf ^ 1, lds + snx); \
        SBAR(); acc[0][0] = MFMA32(bf[buf][0], af[buf][0], acc[0][0]); acc[1][0] = MFMA32(bf[buf][0], af[buf][1], acc[1][0]); SBAR(); \
        if ((i) + 5 < 36) issue(next_head(), sc); \
        SBAR(); acc[0][1] = MFMA32(bf[buf][1], af[buf][0], acc[0][1]); acc[1][1] = MFMA32(bf[buf][1], af[buf][1], acc[1][1]); SBAR(); \
        sc = snx; } while (0)
#define K3W(n) asm volatile("s_waitcnt vmcnt(" #n ") lgkmcnt(0)" ::: "memory")
        unsigned sc = 0u;
        WAIT_V(16);
        K3RD(0, lds);
        for (int i = 0; i < 32; i += 2) {
            if (G) __builtin_amdgcn_s_setprio(1); K3W(12); K3BODY(0, i);
            if (G) __builtin_amdgcn_s_setprio(0); K3W(12); K3BODY(1, i + 1); }
        K3W(8); K3BODY(0, 32); K3W(4); K3BODY(1, 33); K3W(0); K3BODY(0, 34); K3W(0); K3BODY(1, 35);
#undef K3RD
#undef K3BODY
#undef K3W
        STAMP();
        BAR(); asm volatile("" ::: "memory");
#pragma unroll
        for (int mi = 0; mi < 2; ++mi)
#pragma unroll
            for (int nb = 0; nb < 2; ++nb)
#pragma unroll
                for (int g = 0; g < 4; ++g) { const int row = 32 * mi + r32, ch = 8 * nb + 2 * g + hi;
                    *(LAS f32x4*)(lds + wid * 16384 + row * 256 + 16 * (ch ^ (row & 15))) = (f32x4){acc[mi][nb][4 * g], acc[mi][nb][4 * g + 1], acc[mi][nb][4 * g + 2], acc[mi][nb][4 * g + 3]}; }
        WAIT_L(0); BAR(); asm volatile("" ::: "memory");
        { const int row = tid >> 3, c8 = tid & 7; f32x4 s0 = {}, s1 = {};
#pragma unroll
          for (int w = 0; w < 8; ++w) { s0 += *(const LAS f32x4*)(lds + w * 16384 + row * 256 + 16 * ((2 * c8) ^ (row & 15)));
                                        s1 += *(const LAS f32x4*)(lds + w * 16384 + row * 256 + 16 * ((2 * c8 + 1) ^ (row & 15))); }
          u32x4 w = {cvtpk(s0[0], s0[1]), cvtpk(s0[2], s0[3]), cvtpk(s1[0], s1[1]), cvtpk(s1[2], s1[3])};
          *(u32x4*)(out + (size_t)(rt * 64 + row) * 512 + ct * 64 + c8 * 8) = w; }
    }
    STAMP(); STAMP_RT();
    STAMP_OUT(ws + WS_END);
}

extern "C" void kernel_launch(void* const* d_in, const int* in_sizes, int n_in, void* d_out, int out_size, void* d_ws, size_t ws_size,
                              hipStream_t stream) {
    (void)in_sizes; (void)n_in; (void)out_size; (void)ws_size;
    char* ws = (char*)d_ws;
    (void)hipFuncSetAttribute((const void*)kf_all, hipFuncAttributeMaxDynamicSharedMemorySize, LDS_BYTES);
    kf_all<<<256, 512, LDS_BYTES, stream>>>((const u16*)d_in[0], (const u16*)d_in[1], (const u16*)d_in[2], (const u16*)d_in[3], (const u16*)d_in[4], (const u16*)d_in[5],
                                            (const u16*)d_in[7], (const u16*)d_in[8], (const u16*)d_in[9], (const u16*)d_in[10], ws, (u16*)d_out);
}
```

```cpp
#include <hip/hip_runtime.h>
#include <hip/hip_bf16.h>
#include <stdint.h>
#define PREFETCH 1

typedef unsigned short u16;
typedef short bf16x8 __attribute__((ext_vector_type(8)));
typedef short s16x4 __attribute__((ext_vector_type(4)));
typedef float f32x2 __attribute__((ext_vector_type(2)));
typedef float f32x4 __attribute__((ext_vector_type(4)));
typedef float f32x16 __attribute__((ext_vector_type(16)));
typedef unsigned u32x4 __attribute__((ext_vector_type(4)));
typedef unsigned u32x2 __attribute__((ext_vector_type(2)));
typedef __bf16 bf16x2_t __attribute__((ext_vector_type(2)));
#define LAS __attribute__((address_space(3)))
#define DI __device__ __forceinline__
typedef LAS const char* lds_cptr;
typedef LAS char* lds_ptr;

constexpr int SEQ = 2048, DM = 512, NH = 32, HD = 128;
constexpr size_t WS_CNT = 0;
constexpr size_t WS_HQ = 32768, WS_HR = 33792;
constexpr size_t WS_KV = 65536;
constexpr size_t WS_A3 = WS_KV + (size_t)NH * 32 * 32768;
constexpr size_t WS_KVP = WS_A3 + (size_t)576 * SEQ * 16;
constexpr size_t WS_QB = WS_KVP + (size_t)NH * 8 * 4096;
constexpr size_t WS_PQ = WS_QB + (size_t)256 * 32768;
constexpr size_t WS_END = WS_PQ + (size_t)256 * 16384;
constexpr int LDS_BYTES = 163840;

#define MFMA32(a, b, c) __builtin_amdgcn_mfma_f32_32x32x16_bf16((a), (b), (c), 0, 0, 0)
#define MFMA16(a, b, c) __builtin_amdgcn_mfma_f32_16x16x32_bf16((a), (b), (c), 0, 0, 0)
#define SBAR() __builtin_amdgcn_sched_barrier(0)
#define WAIT_V(n) asm volatile("s_waitcnt vmcnt(" #n ")" ::: "memory")
#define WAIT_L(n) asm volatile("s_waitcnt lgkmcnt(" #n ")" ::: "memory")
#define BAR() __builtin_amdgcn_s_barrier()
#define WAIT_BAR(N) asm volatile("s_waitcnt vmcnt(" #N ") lgkmcnt(0)\n\ts_barrier" ::: "memory")

#define STAMP_DECL()
#define STAMP()
#define STAMP_RT()
#define STAMP_OUT(base)
#define STAMP_PARAM
#define STAMP_ARG
DI unsigned rfl(unsigned x) { return __builtin_amdgcn_readfirstlane(x); }
DI unsigned cvtpk(float lo, float hi) { f32x2 v = {lo, hi}; bf16x2_t b = __builtin_convertvector(v, bf16x2_t); return __builtin_bit_cast(unsigned, b); }
DI float bfround(float x) { return __uint_as_float(cvtpk(x, x) << 16); }
DI void glds16(const void* sbase, unsigned voff, unsigned lds_dst) {
    unsigned sv;
    asm volatile("s_mov_b32 %0, m0\n\ts_mov_b32 m0, %3\n\ts_nop 2\n\tglobal_load_lds_dwordx4 %1, %2\n\ts_mov_b32 m0, %0"
                 : "=&s"(sv) : "v"(voff), "s"(sbase), "s"(lds_dst) : "memory");
}
DI void glds16c(const void* sbase, unsigned voff, unsigned lds_dst) {
    unsigned sv;
    asm volatile("s_mov_b32 %0, m0\n\ts_mov_b32 m0, %3\n\ts_nop 2\n\tglobal_load_lds_dwordx4 %1, %2 sc1\n\ts_mov_b32 m0, %0"
                 : "=&s"(sv) : "v"(voff), "s"(sbase), "s"(lds_dst) : "memory");
}
DI void touch8k(const void* sbase, unsigned voff, unsigned lds_dst) {
    unsigned sv;
    asm volatile("s_mov_b32 %0, m0\n\ts_mov_b32 m0, %3\n\ts_nop 2\n\tglobal_load_lds_dword %1, %2\n\ts_mov_b32 m0, %0"
                 : "=&s"(sv) : "v"(voff), "s"(sbase), "s"(lds_dst) : "memory");
}
DI void glds16c_unless(const void* sbase, unsigned voff, unsigned lds_dst, int a, int b) {
    unsigned sv;
    asm volatile("s_cmp_lt_i32 %4, %5\n\ts_cselect_b64 exec, 0, -1\n\ts_mov_b32 %0, m0\n\ts_mov_b32 m0, %3\n\ts_nop 2\n\tglobal_load_lds_dwordx4 %1, %2 sc1\n\ts_mov_b32 m0, %0\n\ts_mov_b64 exec, -1"
                 : "=&s"(sv) : "v"(voff), "s"(sbase), "s"(lds_dst), "s"(a), "s"(b) : "memory", "scc");
}
DI void ldq_unless(bf16x8* q, const void* sbase, unsigned voff, unsigned skip) {
    asm volatile("s_cmp_lg_u32 %10, 0\n\ts_cbranch_scc0 .Lrare%=\n.Lback%=:\n\t.subsection 1\n.Lrare%=:\n\ts_nop 4\n\t"
                 "global_load_dwordx4 %0, %8, %9 offset:-4096\n\tglobal_load_dwordx4 %1, %8, %9 offset:-3072\n\tglobal_load_dwordx4 %2, %8, %9 offset:-2048\n\tglobal_load_dwordx4 %3, %8, %9 offset:-1024\n\t"
                 "global_load_dwordx4 %4, %8, %9\n\tglobal_load_dwordx4 %5, %8, %9 offset:1024\n\tglobal_load_dwordx4 %6, %8, %9 offset:2048\n\tglobal_load_dwordx4 %7, %8, %9 offset:3072\n\ts_branch .Lback%=\n\t.subsection 0"
                 : "+v"(q[0]), "+v"(q[1]), "+v"(q[2]), "+v"(q[3]), "+v"(q[4]), "+v"(q[5]), "+v"(q[6]), "+v"(q[7]) : "v"(voff), "s"((const char*)sbase + 4096), "s"(skip) : "memory", "scc");
}
DI void glds4c(const void* sbase, unsigned lds_dst) {
    unsigned sv, z;
    asm volatile("v_mov_b32 %1, 0\n\ts_mov_b32 %0, m0\n\ts_mov_b32 m0, %3\n\ts_nop 2\n\tglobal_load_lds_dword %1, %2 sc1\n\ts_mov_b32 m0, %0"
                 : "=&s"(sv), "=&v"(z) : "s"(sbase), "s"(lds_dst) : "memory");
}
typedef short v4i16_t __attribute__((ext_vector_type(4)));
DI s16x4 trrd(lds_cptr p) { return __builtin_bit_cast(s16x4, __builtin_amdgcn_ds_read_tr16_b64_v4i16((LAS v4i16_t*)p)); }
DI bf16x8 cat8(s16x4 a, s16x4 b) { return __builtin_shufflevector(a, b, 0, 1, 2, 3, 4, 5, 6, 7); }
DI bf16x8 ldsr128(lds_cptr p) { return *(const LAS bf16x8*)p; }
DI bf16x8 pack8(float a0, float a1, float a2, float a3, float a4, float a5, float a6, float a7) {
    u32x4 w = {cvtpk(a0, a1), cvtpk(a2, a3), cvtpk(a4, a5), cvtpk(a6, a7)}; return __builtin_bit_cast(bf16x8, w); }
#define PACK8(X, B) pack8((X)[(B)], (X)[(B) + 1], (X)[(B) + 2], (X)[(B) + 3], (X)[(B) + 4], (X)[(B) + 5], (X)[(B) + 6], (X)[(B) + 7])
DI float swap32_max(float v) { auto rr = __builtin_amdgcn_permlane32_swap(__float_as_uint(v), __float_as_uint(v), false, false); return fmaxf(__uint_as_float(rr[0]), __uint_as_float(rr[1])); }
DI float swap32_add(float v) { auto rr = __builtin_amdgcn_permlane32_swap(__float_as_uint(v), __float_as_uint(v), false, false); return __uint_as_float(rr[0]) + __uint_as_float(rr[1]); }
DI float lo32_bcast(float v) { auto rr = __builtin_amdgcn_permlane32_swap(__float_as_uint(v), __float_as_uint(v), false, false); return __uint_as_float(rr[0]); }
template <int CTRL> DI float dpp(float v) { return __uint_as_float(__builtin_amdgcn_update_dpp(0u, __float_as_uint(v), CTRL, 0xF, 0xF, true)); }
DI float row16_max(float v) { v = fmaxf(v, dpp<0xB1>(v)); v = fmaxf(v, dpp<0x4E>(v)); v = fmaxf(v, dpp<0x141>(v)); v = fmaxf(v, dpp<0x140>(v)); return v; }
DI float row16_add(float v) { v += dpp<0xB1>(v); v += dpp<0x4E>(v); v += dpp<0x141>(v); v += dpp<0x140>(v); return v; }

constexpr float QSCALE = 0.08838834764831845f * 1.4426950408889634f;
constexpr float THR = 8.f;
constexpr int L_V = 98304;
constexpr int L_WE = 65536, L_GATE = 139264;
#define PIN(x) asm volatile("" : "+v"(x))
#define MX3(a, b, c) __builtin_fmaxf(__builtin_fmaxf((a), (b)), (c))
#define EX2(v) __builtin_amdgcn_exp2f(v)
#define RLX_AGENT __ATOMIC_RELAXED, __HIP_MEMORY_SCOPE_AGENT
#define ST_PL(base, voff, imm, v) asm volatile("s_nop 4\n\tglobal_store_dwordx4 %0, %1, %2 offset:%3\n\ts_nop 1" :: "v"(voff), "v"(v), "s"(base), "i"(imm) : "memory")
#define ST_SC1(base, voff, imm, v) asm volatile("s_nop 4\n\tglobal_store_dwordx4 %0, %1, %2 offset:%3 sc1\n\ts_nop 1" :: "v"(voff), "v"(v), "s"(base), "i"(imm) : "memory")
DI int fresh_lane() { int l; asm volatile("v_mbcnt_lo_u32_b32 %0, -1, 0\n\tv_mbcnt_hi_u32_b32 %0, -1, %0" : "=v"(l)); return l; }
DI void wait_cnt(const unsigned* p, unsigned want) {
    unsigned spins = 0;
    while (rfl(__hip_atomic_load(p, RLX_AGENT)) < want) { __builtin_amdgcn_s_sleep(2); if (++spins > (1u << 20)) break; }
}
DI void handshake_finish(char* ws, int bid, unsigned token, bool ok, int lane) {
    unsigned* CNT = (unsigned*)(ws + WS_CNT); unsigned spins = 0;
    if (bid == 0) {
        for (;;) {
            const unsigned a = __hip_atomic_load(CNT + (80 + (lane & 15)) * 64, RLX_AGENT);
            if (__all(a >= ((lane & 15) ? 16u : 15u))) break;
#pragma unroll
            for (int i = 0; i < 4; ++i) { const unsigned q = __hip_atomic_load((const unsigned*)(ws + WS_HQ) + i * 64 + lane, RLX_AGENT); __hip_atomic_store((unsigned*)(ws + WS_HR) + i * 64 + lane, q, RLX_AGENT); }
            __builtin_amdgcn_s_sleep(8); if (++spins > (1u << 18)) break; }
    } else if (!ok) {
        while (rfl(__hip_atomic_load((const unsigned*)(ws + WS_HR) + bid, RLX_AGENT)) != token) { __builtin_amdgcn_s_sleep(8); if (++spins > (1u << 20)) break; }
        if (lane == 0) __hip_atomic_fetch_add(CNT + (80 + (bid & 15)) * 64, 1u, RLX_AGENT);
    }
}
DI void dual_softmax(f32x16& p) {
#pragma unroll
    for (int hf = 0; hf < 2; ++hf) {
        float mx = -INFINITY;
#pragma unroll
        for (int r = 0; r < 8; ++r) { p[8 * hf + r] = bfround(p[8 * hf + r]); mx = fmaxf(mx, p[8 * hf + r]); }
        mx = swap32_max(mx); float s = 0.f;
#pragma unroll
        for (int r = 0; r < 8; ++r) { p[8 * hf + r] = EX2((p[8 * hf + r] - mx) * 1.4426950408889634f); s += p[8 * hf + r]; }
        s = __builtin_amdgcn_rcpf(swap32_add(s));
#pragma unroll
        for (int r = 0; r < 8; ++r) p[8 * hf + r] *= s;
    }
}
DI void mask_half(f32x16& sc, int dqw) {
    if (dqw < 0) {
#pragma unroll
        for (int r = 0; r < 16; ++r) sc[r] = -INFINITY;
    } else if (dqw == 0) {
        const int ln = fresh_lane(); const int lr = (ln & 31) - 4 * (ln >> 5); const float ninf = -INFINITY;
#pragma unroll
        for (int r = 0; r < 16; ++r)
            asm volatile("s_nop 0\n\tv_cmp_gt_i32 vcc, %c3, %2\n\tv_cndmask_b32 %0, %0, %1, vcc" : "+v"(sc[r]) : "v"(ninf), "v"(lr), "i"(16 * ((r >> 2) & 1) + 8 * (r >> 3) + (r & 3)) : "vcc");
    }
}
DI void mask_tile(f32x16& s0, f32x16& s1, int dqw) {
    asm volatile("s_nop 7\n\ts_nop 7" ::: "memory");
    mask_half(s0, dqw); mask_half(s1, dqw - 32); }

__global__ __launch_bounds__(512, 2) void kf_all(const u16* __restrict__ x, const u16* __restrict__ ckv, const u16* __restrict__ wq, const u16* __restrict__ wuk,
                                                 const u16* __restrict__ wuv, const u16* __restrict__ wo_a, const u16* __restrict__ wqkv,
                                                 const u16* __restrict__ wphi, const u16* __restrict__ wo_b, const u16* __restrict__ gatew,
                                                 char* ws, u16* __restrict__ out) {
    extern __shared__ __attribute__((aligned(16))) char shm[];
    const lds_ptr lds = (lds_ptr)shm; const unsigned lds0 = (unsigned)(uintptr_t)shm;
    const int wid = rfl(threadIdx.x >> 6), G = wid >> 2, w4 = wid & 3;
    const int bid = blockIdx.x, hh_ = 4 * (bid & 7) + (bid >> 6), jj = (bid >> 3) & 7;
#define LANE_VARS() int tid = threadIdx.x; asm volatile("" : "+v"(tid)); const int lane = tid & 63, r32 = lane & 31, hi = lane >> 5, blk = (lane >> 4) & 1, \
        q4 = (lane & 15) >> 2, p4 = lane & 3; const unsigned vlin = (unsigned)lane * 16u; (void)r32; (void)hi; (void)blk; (void)q4; (void)p4; (void)vlin
    unsigned* const CNT = (unsigned*)(ws + WS_CNT);
    char* const KVh = ws + WS_KV + (size_t)hh_ * 32 * 32768;
    char* const A3 = ws + WS_A3;
    STAMP_DECL(); STAMP_RT(); STAMP();
    u32x4 pkv[16];
    unsigned token = 0u;
    const int bA = 15 - jj, bB = jj;
    const char* gX = (const char*)x + (size_t)((G ? bB : bA) * 128 + 32 * w4) * 1024; const char* gW = (const char*)wq + hh_ * 256; const char* gE = (const char*)wqkv + hh_ * 32;
    const unsigned lx = lds0 + wid * 4096, lw = lds0 + L_WE + wid * 1024;
    unsigned voffX, voffW, voffE;
    { const int lane = threadIdx.x & 63;
        { const int row = lane >> 3, ch = (lane & 7) ^ ((row >> 1) & 3); voffX = (unsigned)(row * 1024 + ch * 16); }
        { const int row = 4 * wid + (lane >> 4), j = lane & 15, c = (j >> 2) ^ (row & 3); voffW = (unsigned)(row * 8192 + c * 64 + (j & 3) * 16); }
        { const int row = 8 * wid + (lane >> 3), sl = lane & 7, hf = (sl >> 2) ^ ((row >> 1) & 1), part = hf ? 2 : ((sl >> 1) & 1);
          voffE = (unsigned)(row * 3072 + part * 1024 + (sl & 1) * 16); }
        if (wid == 0) glds16(gatew, (unsigned)lane * 16u, lds0 + L_GATE);
        if (bid == 0 && wid == 1) {
            const unsigned z = 0u;
            asm volatile("global_store_dword %0, %1, %2 sc1\n\tglobal_store_dword %0, %1, %3 sc1\n\ts_nop 1" :: "v"((unsigned)lane * 256u), "v"(z), "s"(ws + WS_CNT), "s"(ws + WS_CNT + 16384) : "memory");
            asm volatile("global_store_dword %0, %1, %2 offset:4 sc1\n\ts_nop 1" :: "v"((unsigned)lane * 256u), "v"(z), "s"(ws + WS_CNT + 16384) : "memory"); }
        PIN(voffX); PIN(voffW); PIN(voffE);
    }
#define XISS(c) do { _Pragma("unroll") for (int e = 0; e < 4; ++e) glds16(gX + e * 8192 + (c) * 128, voffX ^ ((e & 1) << 6), lx + ((c) & 1) * 32768 + e * 1024); } while (0)
#define WISS(c) do { glds16(gW + (size_t)(c) * 524288, voffW, lw + ((c) % 3) * 24576); glds16(gW + (size_t)(c) * 524288 + 262144, voffW, lw + ((c) % 3) * 24576 + 8192); \
                     glds16(gE + (size_t)(c) * 196608, voffE, lw + ((c) % 3) * 24576 + 16384); } while (0)
    {
        LANE_VARS();
        const int wr = wid >> 2, wc = wid & 3, fr = lane & 15, fq = lane >> 4;
        constexpr int HTB = 16384;
#define SA(b, h) (((b) * 2 + (h)) * HTB)
#define SB(b, h) ((4 + (b) * 2 + (h)) * HTB)
        unsigned voffA, voffB;
        { const int row = 8 * wid + (lane >> 3), ch = (lane & 7) ^ ((row >> 1) & 7); voffA = (unsigned)(row * 1024 + ch * 16); }
        { const int row = 4 * wid + (lane >> 4), j = lane & 15, s = ((row >> 3) & 1) * 4 + (row & 3), c = (j >> 1) ^ s; voffB = (unsigned)(row * 8192 + c * 32 + (j & 1) * 16); }
        const char* gA = (const char*)ckv + (size_t)jj * 256 * 1024;
        const char* gB0 = (const char*)wuk + hh_ * 256; const char* gB1 = (const char*)wuv + hh_ * 256;
        const unsigned ldsw = rfl(lds0 + wid * 1024);
#define STAGE_A(b, h, kt) do { const char* g_ = gA + (h) * 131072 + (kt) * 128; glds16(g_, voffA, ldsw + SA(b, h)); glds16(g_ + 65536, voffA, ldsw + SA(b, h) + 8192); } while (0)
#define STAGE_B(b, h, kt) do { const char* g_ = ((h) ? gB1 : gB0) + (size_t)(kt) * 524288; glds16(g_, voffB, ldsw + SB(b, h)); glds16(g_ + 262144, voffB, ldsw + SB(b, h) + 8192); } while (0)
        lds_cptr pa[2], pb[2];
#pragma unroll
        for (int ks = 0; ks < 2; ++ks) pa[ks] = lds + (wr * 64 + fr) * 128 + 16 * ((4 * ks + fq) ^ ((fr >> 1) & 7));
        { const int q = fr >> 2, p = fr & 3, s = (fq & 1) * 4 + q;
#pragma unroll
          for (int n = 0; n < 2; ++n) pb[n] = lds + (8 * fq + q) * 256 + 32 * ((2 * wc + n) ^ s) + 8 * p; }
#define LDA(dst, b, h) do { _Pragma("unroll") for (int m = 0; m < 4; ++m) _Pragma("unroll") for (int k = 0; k < 2; ++k) dst[m][k] = ldsr128(pa[k] + SA(b, h) + m * 2048); } while (0)
#define LDB(dst, b, h) do { _Pragma("unroll") for (int n = 0; n < 2; ++n) _Pragma("unroll") for (int k = 0; k < 2; ++k) \
        dst[n][k] = cat8(trrd(pb[n] + SB(b, h) + k * 8192), trrd(pb[n] + SB(b, h) + k * 8192 + 1024)); } while (0)
#define MMA(ai, bj, At, Bt) do { __builtin_amdgcn_s_setprio(1); _Pragma("unroll") for (int m = 0; m < 4; ++m) _Pragma("unroll") for (int n = 0; n < 2; ++n) _Pragma("unroll") for (int k = 0; k < 2; ++k) \
        acc[ai][bj][m][n] = (bj) ? MFMA16(At[m][k], Bt[n][k], acc[ai][bj][m][n]) : MFMA16(Bt[n][k], At[m][k], acc[ai][bj][m][n]); __builtin_amdgcn_s_setprio(0); } while (0)
        const unsigned scr = lds0 + 143360 + wid * 256; const size_t gw = (size_t)(bid * 8 + wid);
        auto warm = [&](int slot) {
            if (slot == 0 && wid == 0) { token = rfl(*(const LAS unsigned*)(lds + 148480)) + 1u; const unsigned z = 0u, tk = token;
                asm volatile("s_nop 4\n\tglobal_store_dword %0, %1, %2 sc1\n\ts_nop 1" :: "v"(z), "v"(tk), "s"(ws + WS_HQ + bid * 4) : "memory"); }
            if (bid == 0 && wid == 1) {
                if (slot > 0) { const u32x4 rq = *(const LAS u32x4*)(lds + 147456 + lane * 16); ST_SC1(ws + WS_HR, (unsigned)lane * 16u, 0, rq); WAIT_L(0); }
                glds16c(ws + WS_HQ, (unsigned)lane * 16u, lds0 + 147456); }
            const unsigned vo = (unsigned)lane * 128u;
            if (slot == 0) { if (lane < 8) touch8k((const char*)x + gw * 1024, vo, scr); }
            else if (slot == 1) { if (lane < 16) touch8k((const char*)wq + gw * 2048, vo, scr); }
            else if (slot == 2) { if (lane < 6) touch8k((const char*)wqkv + gw * 768, vo, scr); }
            else if (slot == 3) { if (lane < 16) touch8k((const char*)wo_a + gw * 2048, vo, scr); }
            else if (slot == 4) { if (lane < 2) touch8k((const char*)wo_b + gw * 256, vo, scr); }
        };
        f32x4 acc[2][2][4][2] = {};
        bf16x8 At[4][2], B0[2][2], B1[2][2];
        constexpr int nt = 8;
        STAGE_B(0, 0, 0); STAGE_A(0, 0, 0); STAGE_B(0, 1, 0); STAGE_A(0, 1, 0);
        if (wid == 0) glds4c(ws + WS_HR + bid * 4, lds0 + 148480);
        if (wr == 1) BAR();
        if (wid == 0) { WAIT_V(5); } else { WAIT_V(4); } BAR();
        STAGE_B(1, 0, 1); STAGE_A(1, 0, 1); STAGE_B(1, 1, 1);
        if (wid == 0) { WAIT_V(7); } else { WAIT_V(6); } BAR();
        { const unsigned vo = (unsigned)lane * 128u;
          if (lane < 16) { touch8k((const char*)wuk + gw * 2048, vo, scr); touch8k((const char*)wuv + gw * 2048, vo, scr); }
          if (lane < 8) touch8k((const char*)ckv + gw * 1024, vo, scr); }
        STAMP();
        for (int t = 0; t < nt - 2; t += 2) {
            LDB(B0, 0, 0); SBAR(); LDA(At, 0, 0); STAGE_A(1, 1, t + 1);
            WAIT_L(8); BAR(); WAIT_L(0); MMA(0, 0, At, B0); BAR(); SBAR();
            LDB(B1, 0, 1); STAGE_B(0, 0, t + 2);
            BAR(); WAIT_L(0); MMA(0, 1, At, B1); BAR();
            LDA(At, 0, 1); STAGE_A(0, 0, t + 2);
            BAR(); WAIT_L(0); MMA(1, 0, At, B0); BAR(); SBAR();
            STAGE_B(0, 1, t + 2);
            WAIT_V(6); BAR(); MMA(1, 1, At, B1); BAR();
            warm(t);
            LDB(B0, 1, 0); SBAR(); LDA(At, 1, 0); STAGE_A(0, 1, t + 2);
            WAIT_L(8); BAR(); WAIT_L(0); MMA(0, 0, At, B0); BAR(); SBAR();
            LDB(B1, 1, 1); STAGE_B(1, 0, t + 3);
            BAR(); WAIT_L(0); MMA(0, 1, At, B1); BAR();
            LDA(At, 1, 1); STAGE_A(1, 0, t + 3);
            BAR(); WAIT_L(0); MMA(1, 0, At, B0); BAR(); SBAR();
            STAGE_B(1, 1, t + 3);
            WAIT_V(6); BAR(); MMA(1, 1, At, B1); BAR();
            warm(t + 1);
        }
        { LDB(B0, 0, 0); WAIT_V(0); LDA(At, 0, 0); STAGE_A(1, 1, nt - 1);
          BAR(); WAIT_L(0); MMA(0, 0, At, B0); BAR();
          LDB(B1, 0, 1); BAR(); WAIT_L(0); MMA(0, 1, At, B1); BAR();
          LDA(At, 0, 1); BAR(); WAIT_L(0); MMA(1, 0, At, B0); MMA(1, 1, At, B1); BAR(); }
        { LDB(B0, 1, 0); LDA(At, 1, 0); BAR(); WAIT_L(0); MMA(0, 0, At, B0); BAR();
          LDB(B1, 1, 1); WAIT_V(0);
          WISS(0); XISS(0);
          BAR(); WAIT_L(0); MMA(0, 1, At, B1); BAR();
          LDA(At, 1, 1); BAR(); WAIT_L(0); MMA(1, 0, At, B0); MMA(1, 1, At, B1); BAR(); }
        if (wr == 0) BAR();
        WISS(1); XISS(1);
        if (wid == 0) glds4c(ws + WS_HR + bid * 4, lds0 + 148480);
        SBAR();
#undef SA
#undef SB
#undef STAGE_A
#undef STAGE_B
#undef LDA
#undef LDB
#undef MMA
#pragma unroll
        for (int ai = 0; ai < 2; ++ai) {
#pragma unroll
            for (int m = 0; m < 4; ++m) { const f32x4 a = acc[ai][0][m][0], b = acc[ai][0][m][1];
                pkv[ai * 4 + m] = (u32x4){cvtpk(a[0], a[1]), cvtpk(a[2], a[3]), cvtpk(b[0], b[1]), cvtpk(b[2], b[3])}; }
#pragma unroll
            for (int u = 0; u < 2; ++u)
#pragma unroll
                for (int n = 0; n < 2; ++n) { const f32x4 a = acc[ai][1][2 * u][n], b = acc[ai][1][2 * u + 1][n];
                    pkv[8 + ai * 4 + u * 2 + n] = (u32x4){cvtpk(a[0], a[1]), cvtpk(a[2], a[3]), cvtpk(b[0], b[1]), cvtpk(b[2], b[3])}; }
        }
    }
    STAMP();
    const char* kvw = KVh + w4 * 4096;
    const unsigned ldw = lds0 + w4 * 4096;
    char* const qbp = ws + WS_QB + (size_t)(bid * 4 + w4) * 8192;
    char* const pqp = ws + WS_PQ + (size_t)(bid * 8 + wid) * 2048;
    bf16x8 qf[8]; float gate;
    {
        LANE_VARS();
        u16 wph[8];
#pragma unroll
        for (int j = 0; j < 8; ++j) wph[j] = wphi[hh_ * 256 + (8 * (j >> 2) + 4 * hi + (j & 3)) * 16 + (lane & 15)];
        const char* tb0 = KVh + (size_t)(4 * jj + G) * 32768;
        const unsigned vok = (unsigned)((4 * w4 + (lane >> 4)) * 1024 + (lane & 15) * 16), vov = (unsigned)(16384 + (lane >> 4) * 2048 + (w4 * 32 + (lane & 15)) * 16);
#define KVST(i) do { if ((i) < 8) ST_SC1(tb0 + ((i) >> 2) * 65536, vok, ((i) & 3) * 256, pkv[i]); \
                     else ST_SC1(tb0 + (((i) >> 2) & 1) * 65536 + (((i) >> 1) & 1) * 8192, vov, ((i) & 1) * 256, pkv[i]); } while (0)
        f32x16 qa[5] = {}, d1 = {};
        unsigned xo[2], wo_[4];
#pragma unroll
        for (int s = 0; s < 2; ++s) xo[s] = (unsigned)((32 * wid + r32) * 128 + 16 * ((2 * s + hi) ^ ((r32 >> 1) & 7)));
#pragma unroll
        for (int c = 0; c < 4; ++c) wo_[c] = (unsigned)(L_WE + (8 * hi + q4) * 256 + 64 * (c ^ q4) + 32 * (p4 >> 1) + 16 * blk + 8 * (p4 & 1));
        const unsigned eo = (unsigned)(L_WE + 16384 + (8 * hi + q4) * 128 + 64 * ((q4 >> 1) & 1) + 32 * blk + 8 * p4);
        const unsigned vo_ = (unsigned)(L_WE + 16384 + (8 * hi + q4) * 128 + 64 * (((q4 >> 1) & 1) ^ 1) + 8 * p4);
        bf16x8 fx[2], fw[2][6], fg[2];
#define QR_(buf, c, ks, u) do { \
        if ((u) == 0) { fx[buf] = ldsr128(lds + ((c) & 1) * 32768 + (xo[(ks) & 1] ^ (((ks) >> 1) << 6))); fg[buf] = ldsr128(lds + L_GATE + ((c) * 64 + (ks) * 16 + 8 * hi) * 2); } \
        else if ((u) < 5) { fw[buf][(u) - 1] = cat8(trrd(lds + wo_[(u) - 1] + ((c) % 3) * 24576 + (ks) * 4096), trrd(lds + wo_[(u) - 1] + ((c) % 3) * 24576 + (ks) * 4096 + 1024)); } \
        else { fw[buf][4] = cat8(trrd(lds + eo + ((c) % 3) * 24576 + (ks) * 2048), trrd(lds + eo + ((c) % 3) * 24576 + (ks) * 2048 + 512)); \
               fw[buf][5] = cat8(trrd(lds + vo_ + ((c) % 3) * 24576 + (ks) * 2048), trrd(lds + vo_ + ((c) % 3) * 24576 + (ks) * 2048 + 512)); } } while (0)
#define QM_(buf, u) do { if ((u) < 5) qa[u] = MFMA32(fw[buf][u], fx[buf], qa[u]); else d1 = MFMA32(fx[buf], blk ? fg[buf] : fw[buf][5], d1); } while (0)
#define QRD(buf, c, ks) do { _Pragma("unroll") for (int u_ = 0; u_ < 6; ++u_) QR_(buf, c, ks, u_); } while (0)
#define QSTEP(buf, c, ks, rd) do { _Pragma("unroll") for (int u_ = 0; u_ < 6; ++u_) { if (rd) QR_((buf) ^ 1, c, ks, u_); QM_(buf, u_); SBAR(); } } while (0)
#define WV_(n) asm volatile("s_waitcnt vmcnt(%0)" :: "i"(n) : "memory")
#define NST(c) ((c) < 0 ? 0 : (c) < 4 ? 3 : (c) < 6 ? 2 : 0)
#define ST0(c) ((c) < 4 ? 3 * (c) : 12 + 2 * ((c) - 4))
#define QCHUNK(c) do { \
        SBAR(); QSTEP(0, c, 1, true); QSTEP(1, c, 2, true); \
        if ((c) < 7) { WV_(4 + NST((c) - 1)); } BAR(); asm volatile("" ::: "memory"); \
        if ((c) == 7 && wid == 0) { handshake_finish(ws, bid, token, hs_ok, lane); if (lane == 0) __hip_atomic_fetch_add(CNT + hh_ * 64, 1u, RLX_AGENT); } \
        if ((c) < 6) WISS((c) + 2); \
        _Pragma("unroll") for (int i_ = 0; i_ < NST(c); ++i_) KVST(ST0(c) + i_); \
        SBAR(); QSTEP(0, c, 3, true); \
        if ((c) < 7) { WV_(((c) < 6 ? 3 : 0) + NST(c)); } \
        SBAR(); QSTEP(1, (c) + 1, 0, (c) < 7); \
        if ((c) < 6) XISS((c) + 2); } while (0)
        if (wid == 0) { WAIT_V(8); } else { WAIT_V(7); } BAR(); asm volatile("" ::: "memory");
        bool hs_ok = true;
        bf16x8 wpf;
#pragma unroll
        for (int j = 0; j < 8; ++j) wpf[j] = (short)(blk ? (wph[j] ^ 0x8000) : wph[j]);
        PIN(wpf);
        STAMP();
        QRD(0, 0, 0);
        QCHUNK(0);
        {
            unsigned t0, t1, seen, em;
            asm volatile("v_mov_b32 %0, %5\n\tds_read_b32 %0, %0\n\tv_mov_b32 %1, 1\n\ts_waitcnt lgkmcnt(0)\n\tv_readfirstlane_b32 %2, %0\n\tv_mov_b32 %0, 0\n\ts_nop 3\n\t"
                         "s_min_u32 %3, %8, 1\n\ts_cmp_eq_u32 %7, 0\n\ts_cselect_b32 %3, %3, 0\n\t"
                         "s_cmp_eq_u32 %2, %6\n\ts_cselect_b32 %2, 1, 0\n\ts_and_b32 %3, %3, %2\n\ts_mov_b32 exec_lo, %3\n\ts_mov_b32 exec_hi, 0\n\t"
                         "global_atomic_add %0, %1, %4\n\ts_mov_b64 exec, -1"
                         : "=&v"(t0), "=&v"(t1), "=&s"(seen), "=&s"(em) : "s"(CNT + (80 + (bid & 15)) * 64), "s"(lds0 + 148480), "s"(token), "s"(wid), "s"(bid) : "memory", "scc");
            hs_ok = seen != 0u; }
        QCHUNK(1); QCHUNK(2); QCHUNK(3); QCHUNK(4); QCHUNK(5); QCHUNK(6); QCHUNK(7);
#undef QRD
#undef QR_
#undef QM_
#undef QSTEP
#undef QCHUNK
#undef XISS
#undef WISS
#undef KVST
#undef NST
#undef ST0
        STAMP();
        BAR(); asm volatile("" ::: "memory");
        unsigned f1 = __hip_atomic_load(CNT + hh_ * 64, RLX_AGENT);
#pragma unroll
        for (int cc = 0; cc < 4; ++cc) {
#pragma unroll
            for (int r = 0; r < 16; ++r) qa[cc][r] *= QSCALE;
            qf[2 * cc] = PACK8(qa[cc], 0); qf[2 * cc + 1] = PACK8(qa[cc], 8);
        }
        if (rfl(f1) < 8u) wait_cnt(CNT + hh_ * 64, 8u);
        asm volatile("" ::: "memory");
        STAMP();
#pragma unroll
        for (int e = 0; e < 4; ++e) glds16c(kvw + (size_t)(e >> 1) * 32768 + (2 * G + (e & 1)) * 1024, vlin, ldw + (e >> 1) * 16384 + (2 * G + (e & 1)) * 1024);
        LAS float* red = (LAS float*)(lds + L_V + G * 32768 + w4 * 4096);
        if (r32 == 16) {
#pragma unroll
            for (int r = 0; r < 16; ++r) red[(r & 3) + 8 * (r >> 2) + 4 * hi] = d1[r];
        }
        WAIT_L(0);
        { const float gz = bfround(red[r32]); gate = bfround(1.f / (1.f + __expf(-gz))); }
        f32x16 pq = MFMA32(wpf, PACK8(qa[4], 0), f32x16{});
        f32x16 pk = MFMA32(wpf, PACK8(qa[4], 8), f32x16{});
        dual_softmax(pq); dual_softmax(pk);
        f32x16 d3;
        { bf16x8 id0, id1;
#pragma unroll
          for (int j = 0; j < 8; ++j) { const int f = 8 * (j >> 2) + 4 * hi + (j & 3); id0[j] = (short)(r32 == f ? 0x3F80 : 0); id1[j] = (short)(r32 == f + 16 ? 0x3F80 : 0); }
          d3 = MFMA32(PACK8(pk, 0), id0, f32x16{}); d3 = MFMA32(PACK8(pk, 8), id1, d3); }
        { const bf16x8 t0 = PACK8(pq, 0), t1 = PACK8(pq, 8); ST_PL(pqp, vlin, 0, t0); ST_PL(pqp, vlin, 1024, t1); }
        if (G == 0) {
#pragma unroll
            for (int s = 0; s < 8; ++s) ST_PL(qbp + (s >> 2) * 4096, vlin, (s & 3) * 1024, qf[s]);
        }
        const bf16x8 ones = {0x3F80, 0x3F80, 0x3F80, 0x3F80, 0x3F80, 0x3F80, 0x3F80, 0x3F80};
        const bf16x8 v0 = PACK8(d1, 0), v1 = PACK8(d1, 8);
        f32x16 d4 = MFMA32(PACK8(d3, 0), blk ? ones : v0, f32x16{});
        d4 = MFMA32(PACK8(d3, 8), blk ? ones : v1, d4);
#pragma unroll
        for (int r = 0; r < 16; ++r) red[((r & 3) + 8 * (r >> 2) + 4 * hi) * 32 + r32] = d4[r];
        WAIT_L(0); BAR(); asm volatile("" ::: "memory");
        if (tid < 256) {
            f32x4 s = {0.f, 0.f, 0.f, 0.f};
#pragma unroll
            for (int w = 0; w < 8; ++w) s += *(const LAS f32x4*)(lds + L_V + (w >> 2) * 32768 + (w & 3) * 4096 + tid * 16);
            ST_SC1(ws + WS_KVP + (size_t)(hh_ * 8 + jj) * 4096, (unsigned)tid * 16u, 0, s);
        }
        if (G) { WAIT_BAR(2); } else { WAIT_BAR(11); }
    }
    STAMP();
    {
    LANE_VARS();
    const int prow = (r32 & 7) | ((r32 & 8) << 1) | ((r32 & 16) >> 1);
    const unsigned kro = (unsigned)(prow * 16 + hi * 1024), vro = (unsigned)(L_V + r32 * 16 + hi * 2048);
    const int dq0 = 32 * w4;
    const int T = 2 * jj + 2;
    unsigned ks0 = 0u, ks1 = 16384u, ks2 = 32768u;
    int rho = 0; const int TG = G ? T : 99;
    f32x16 o[4], negm, sA0, sA1, sB0, sB1; bf16x8 kf[16], vf[16]; u32x4 pw[4]; float m, l;
#define KRD(i, kp) kf[i] = ldsr128((kp) + ((i) >> 1) * 2048 + ((i) & 1) * 512)
#define VRD(i, vp) vf[i] = ldsr128((vp) + ((i) >> 2) * 4096 + ((i) & 3) * 512)
#define PF(k) __builtin_bit_cast(bf16x8, pw[k])
#define DMA1(i) do { if ((i) < 2) glds16c(gk_ + (i) * 1024, vlin, dk_ + (i) * 1024); else if ((i) < 4) glds16c_unless(gk_ + (i) * 1024, vlin, dk_ + (i) * 1024, rk_, T); \
                     else if ((i) < 6) glds16c(gv_ + ((i) - 4) * 1024, vlin, dv_ + ((i) - 4) * 1024); else glds16c_unless(gv_ + ((i) - 4) * 1024, vlin, dv_ + ((i) - 4) * 1024, rho, T); } while (0)
    const char* gk_; const char* gv_; unsigned dk_, dv_, ko_, vo_; int rk_;
    unsigned go1, go2;
#define GOFF(r) ((r) < T ? (unsigned)(r) * 32768u + G * 2048u : (unsigned)(2 * (r) - T + G) * 32768u)
#define PREP_A(r, k0) do { ko_ = ((r) >= TG ? 49152u : 0u) + (k0); vo_ = ((r) - 1 >= TG ? 32768u : 0u) + (((r) - 1) & 1) * 16384u; } while (0)
#define PREP_B(r, gcur) do { gv_ = kvw + ((gcur) + 16384u); dv_ = ldw + L_V + ((r) & 1) * 16384u + ((r) < T ? G * 2048u : G * 32768u); } while (0)
#define PREP_C(r) do { rk_ = (r) + 2 <= 16 ? (r) + 2 : 16; go1 = go2; go2 = GOFF(rk_); } while (0)
#define PREP_D(k2) do { gk_ = kvw + go2; dk_ = ldw + (k2) + (rk_ < T ? G * 2048u : G * 49152u); } while (0)
#define ROT() do { const unsigned t_ = ks0; ks0 = ks1; ks1 = ks2; ks2 = t_; ++rho; } while (0)
#define GAPA(i, C0, C1, P0, P1) do { \
        if ((i) + 4 < 16) { KRD((i) + 4, kp_); } else { VRD((i) - 12, vp_); } \
        if (((i) & 1) == 0) DMA1((i) >> 1); \
        if ((i) & 1) C1 = MFMA32(kf[i], qf[(i) >> 1], (i) < 2 ? negm : C1); else C0 = MFMA32(kf[i], qf[(i) >> 1], (i) < 2 ? negm : C0); \
        if ((i) < 8) { P1[2 * (i)] = EX2(P1[2 * (i)]); P1[2 * (i) + 1] = EX2(P1[2 * (i) + 1]); pw[(i) >> 2][(i) & 3] = cvtpk(P0[2 * (i)], P0[2 * (i) + 1]); PIN(P1); PIN(pw[(i) >> 2]); } \
        else { l += P1[2 * (i) - 16]; l2 += P1[2 * (i) - 15]; pw[(i) >> 2][(i) & 3] = cvtpk(P1[2 * (i) - 16], P1[2 * (i) - 15]); PIN(l); PIN(l2); PIN(pw[(i) >> 2]); } \
        SBAR(); } while (0)
#define PVM(i) o[(i) & 3] = MFMA32(vf[i], PF((i) >> 2), o[(i) & 3])
#define RDB(i) do { if ((i) + 4 < 16) { VRD((i) + 4, vp_); } else { KRD((i) - 12, kn_); } } while (0)
#define ROUND(C0, C1, P0, P1) do { \
        const lds_cptr kp_ = lds + ko_ + kro; const lds_cptr vp_ = lds + vo_ + vro; \
        float l2 = 0.f; lds_cptr kn_ = kp_; SBAR(); \
        GAPA(0, C0, C1, P0, P1); GAPA(1, C0, C1, P0, P1); GAPA(2, C0, C1, P0, P1); GAPA(3, C0, C1, P0, P1); \
        GAPA(4, C0, C1, P0, P1); GAPA(5, C0, C1, P0, P1); GAPA(6, C0, C1, P0, P1); GAPA(7, C0, C1, P0, P1); \
        GAPA(8, C0, C1, P0, P1); GAPA(9, C0, C1, P0, P1); GAPA(10, C0, C1, P0, P1); GAPA(11, C0, C1, P0, P1); \
        GAPA(12, C0, C1, P0, P1); GAPA(13, C0, C1, P0, P1); GAPA(14, C0, C1, P0, P1); GAPA(15, C0, C1, P0, P1); \
        l += l2; \
        ldq_unless(qf, qbp, vlin, (unsigned)(rho + 1 - TG));        \
        RDB(0); PVM(0); SBAR(); RDB(1); PVM(1); SBAR(); \
        if (__builtin_expect(rho == 16 || (rho >= TG - 2 && rho < TG), 0)) mask_tile(C0, C1, dq0 - (rho == 16 ? 64 * G : 64 * (rho - TG + 2))); \
        float ma, mb; \
        RDB(2); PVM(2); ma = MX3(C0[0], C0[1], C1[0]); mb = MX3(C0[2], C0[3], C1[1]); ma = MX3(ma, C1[2], C1[3]); mb = MX3(mb, C0[4], C0[5]); PIN(ma); PIN(mb); SBAR(); \
        RDB(3); PVM(3); ma = MX3(ma, C0[6], C0[7]); mb = MX3(mb, C1[4], C1[5]); ma = MX3(ma, C1[6], C1[7]); mb = MX3(mb, C0[8], C0[9]); PIN(ma); PIN(mb); SBAR(); \
        RDB(4); PVM(4); ma = MX3(ma, C0[10], C0[11]); mb = MX3(mb, C1[8], C1[9]); ma = MX3(ma, C1[10], C1[11]); mb = MX3(mb, C0[12], C0[13]); PIN(ma); PIN(mb); SBAR(); \
        RDB(5); PVM(5); ma = MX3(ma, C0[14], C0[15]); mb = MX3(mb, C1[12], C1[13]); ma = MX3(ma, C1[14], C1[15]); \
        const float rm = swap32_max(fmaxf(ma, mb)); bool pend = false; float fsc = 1.f; \
        if (__builtin_expect(__any(rm > THR) || rho == TG, 0)) { float dl; pend = true; \
            if (rho == TG) { dl = fmaxf(rm, -1e30f); m = dl; fsc = gate / swap32_add(l); l = 0.f; }        \
            else { dl = fmaxf(rm, 0.f); m += dl; fsc = EX2(-dl); l *= fsc; } \
            _Pragma("unroll") for (int e = 0; e < 16; ++e) { C0[e] -= dl; C1[e] -= dl; negm[e] = -m; } } \
        SBAR(); \
        RDB(6); PVM(6); C0[0] = EX2(C0[0]); C0[1] = EX2(C0[1]); PIN(C0); SBAR(); \
        RDB(7); PVM(7); C0[2] = EX2(C0[2]); C0[3] = EX2(C0[3]); l += C0[0]; l2 = C0[1]; PIN(C0); PIN(l); SBAR(); \
        RDB(8); PVM(8); PREP_A(rho + 1, ks1); kn_ = lds + ko_ + kro; C0[4] = EX2(C0[4]); C0[5] = EX2(C0[5]); l += C0[2]; l2 += C0[3]; PIN(C0); PIN(l); PIN(l2); SBAR(); \
        RDB(9); PVM(9); PREP_B(rho + 1, go1); C0[6] = EX2(C0[6]); C0[7] = EX2(C0[7]); l += C0[4]; l2 += C0[5]; PIN(C0); PIN(l); PIN(l2); SBAR(); \
        RDB(10); PVM(10); PREP_C(rho + 1); C0[8] = EX2(C0[8]); C0[9] = EX2(C0[9]); l += C0[6]; l2 += C0[7]; PIN(C0); PIN(l); PIN(l2); SBAR(); \
        RDB(11); PVM(11); PREP_D(ks0); C0[10] = EX2(C0[10]); C0[11] = EX2(C0[11]); l += C0[8]; l2 += C0[9]; PIN(C0); PIN(l); PIN(l2); SBAR(); \
        RDB(12); PVM(12); C0[12] = EX2(C0[12]); C0[13] = EX2(C0[13]); l += C0[10]; l2 += C0[11]; PIN(C0); PIN(l); PIN(l2); SBAR(); \
        RDB(13); PVM(13); C0[14] = EX2(C0[14]); C0[15] = EX2(C0[15]); l += C0[12]; l2 += C0[13]; PIN(C0); PIN(l); PIN(l2); SBAR(); \
        RDB(14); PVM(14); l += C0[14]; l2 += C0[15]; PIN(l); PIN(l2); SBAR(); \
        RDB(15); PVM(15); l += l2; PIN(l); SBAR(); \
        if (__builtin_expect(pend || rho + 1 == TG, 0)) { \
            if (pend) { _Pragma("unroll") for (int c = 0; c < 4; ++c) _Pragma("unroll") for (int e = 0; e < 16; ++e) o[c][e] *= fsc; } \
            if (rho + 1 == TG) negm = f32x16{}; \
            if (rho == TG) { const int ln = fresh_lane(); const unsigned vod = (unsigned)((ln >> 5) * 2048 + (32 * w4 + (ln & 31)) * 16); const char* dst = A3 + (size_t)(bB * 576 + hh_ * 16) * 2048; \
                _Pragma("unroll") for (int c = 0; c < 4; ++c) { const bf16x8 t0 = PACK8(o[c], 0), t1 = PACK8(o[c], 8); ST_SC1(dst + c * 8192, vod, 0, t0); ST_SC1(dst + c * 8192 + 4096, vod, 0, t1); o[c] = f32x16{}; } } } \
        asm volatile("s_cmp_eq_u32 %0, %1\n\ts_cbranch_scc1 .Lw%=\n\ts_waitcnt vmcnt(0)\n.Lw%=:\n\ts_waitcnt vmcnt(8)\n\ts_barrier" :: "s"(rho), "s"(TG) : "memory", "scc");     \
        if (__builtin_expect(rho == TG + 1 && w4 == 0, 0)) { if (fresh_lane() == 0) __hip_atomic_fetch_or(CNT + (64 + bB) * 64, 1u << hh_, RLX_AGENT); }        \
        ROT(); } while (0)

#define ROUND0(C0, C1) do { \
        const lds_cptr kp_ = lds + ks0 + kro; const lds_cptr kn_ = lds + ks1 + kro; \
        go2 = GOFF(1); PREP_B(0, GOFF(0)); PREP_C(0); PREP_D(ks2); \
        _Pragma("unroll") for (int i = 0; i < 6; ++i) KRD(i, kp_); \
        _Pragma("unroll") for (int i = 0; i < 8; ++i) DMA1(i); \
        _Pragma("unroll") for (int c = 0; c < 4; ++c) o[c] = f32x16{}; \
        PREP_A(1, ks1); PREP_B(1, go1); PREP_C(1); PREP_D(ks0); SBAR(); \
        _Pragma("unroll") for (int i = 0; i < 16; ++i) { \
            if (i + 6 < 16) KRD(i + 6, kp_); \
            if (i & 1) C1 = MFMA32(kf[i], qf[i >> 1], i < 2 ? f32x16{} : C1); else C0 = MFMA32(kf[i], qf[i >> 1], i < 2 ? f32x16{} : C0); \
            SBAR(); } \
        if (TG == 2) mask_tile(C0, C1, dq0); \
        float ma = MX3(C0[0], C0[1], C1[0]), mb = MX3(C0[2], C0[3], C1[1]); ma = MX3(ma, C1[2], C1[3]); \
        _Pragma("unroll") for (int r = 4; r < 16; r += 4) { ma = MX3(ma, C0[r], C0[r + 1]); mb = MX3(mb, C0[r + 2], C0[r + 3]); ma = MX3(ma, C1[r], C1[r + 1]); mb = MX3(mb, C1[r + 2], C1[r + 3]); } \
        m = fmaxf(swap32_max(fmaxf(ma, mb)), -1e30f); l = 0.f; \
        _Pragma("unroll") for (int i = 0; i < 4; ++i) KRD(i, kn_); \
        _Pragma("unroll") for (int e = 0; e < 16; ++e) { C0[e] = EX2(C0[e] - m); l += C0[e]; C1[e] -= m; negm[e] = -m; } \
        asm volatile("s_waitcnt vmcnt(0)\n\ts_barrier" ::: "memory"); ROT(); } while (0)
#define DRAIN(P0, P1, MID) do { \
        const lds_cptr vp_ = lds + G * 32768 + vro; \
        _Pragma("unroll") for (int i = 0; i < 8; ++i) VRD(i, vp_); \
        _Pragma("unroll") for (int i = 0; i < 8; ++i) pw[i >> 2][i & 3] = cvtpk(P0[2 * i], P0[2 * i + 1]); \
        SBAR(); \
        _Pragma("unroll") for (int i = 0; i < 16; ++i) { \
            if (i + 8 < 16) VRD(i + 8, vp_); \
            if (i == 8) { MID } \
            PVM(i); \
            if (i < 8) { P1[2 * i] = EX2(P1[2 * i]); P1[2 * i + 1] = EX2(P1[2 * i + 1]); PIN(P1); } \
            if (i >= 1 && i < 9) { l += P1[2 * i - 2]; l += P1[2 * i - 1]; pw[2 + ((i - 1) >> 2)][(i - 1) & 3] = cvtpk(P1[2 * i - 2], P1[2 * i - 1]); PIN(l); PIN(pw[2 + ((i - 1) >> 2)]); } \
            SBAR(); } \
        l = swap32_add(l); } while (0)
    ROUND0(sA0, sA1);
    if (wid == 0 && fresh_lane() == 0) __hip_atomic_fetch_add(CNT + (32 + hh_) * 64, 1u, RLX_AGENT);
    STAMP();
#pragma clang loop unroll(disable)
    for (int pr = 0; pr < 8; ++pr) { ROUND(sB0, sB1, sA0, sA1); ROUND(sA0, sA1, sB0, sB1); }
    STAMP();
    {
        f32x2 ps[8]; bf16x8 phq[2];
        LANE_VARS();
        const unsigned f2 = __hip_atomic_load(CNT + (32 + hh_) * 64, RLX_AGENT);
        DRAIN(sA0, sA1,
            if (rfl(f2) < 8u) wait_cnt(CNT + (32 + hh_) * 64, 8u);
            _Pragma("unroll") for (int w = 0; w < 8; ++w) ps[w] = __builtin_bit_cast(f32x2, __hip_atomic_load((const unsigned long long*)(ws + WS_KVP + (size_t)(hh_ * 8) * 4096 + (unsigned)(w * 4096 + tid * 8)), RLX_AGENT));
            phq[0] = *(const bf16x8*)(pqp + vlin); phq[1] = *(const bf16x8*)(pqp + (vlin + 1024)); );
        const unsigned vod = (unsigned)(hi * 2048 + (32 * w4 + r32) * 16);
        BAR(); asm volatile("" ::: "memory");
        const unsigned st0 = ks2;
        { const lds_ptr xp = lds + L_V + wid * 8192 + lane * 16;
#pragma unroll
          for (int c = 0; c < 2; ++c)
#pragma unroll
              for (int g = 0; g < 4; ++g) { f32x4 t;
#pragma unroll
                  for (int e = 0; e < 4; ++e) t[e] = G ? o[c][4 * g + e] : o[2 + c][4 * g + e];
                  *(LAS f32x4*)(xp + (c * 4 + g) * 1024) = t; }
          *(LAS f32x4*)(lds + st0 + wid * 1024 + lane * 16) = (f32x4){m, l, gate, 0.f};
          f32x2 s = ps[0];
#pragma unroll
          for (int w = 1; w < 8; ++w) s += ps[w];
          *(LAS f32x2*)(lds + st0 + 8192 + tid * 8) = s; }
        WAIT_L(0); BAR(); asm volatile("" ::: "memory");
        { const int ow = wid ^ 4;
          const f32x4 st = *(const LAS f32x4*)(lds + st0 + ow * 1024 + lane * 16);
          const float mn = fmaxf(m, st[0]), f0 = EX2(m - mn), f1 = EX2(st[0] - mn);
          const float gg = G ? st[2] : gate;
          const float inv = gg / (l * f0 + st[1] * f1); const float c0 = f0 * inv, c1 = f1 * inv;
          const lds_cptr xp = lds + L_V + ow * 8192 + lane * 16;
          const char* dst = A3 + (size_t)(bA * 576 + hh_ * 16 + G * 8) * 2048;
#pragma unroll
          for (int c = 0; c < 2; ++c) {
              float v[16];
#pragma unroll
              for (int g = 0; g < 4; ++g) { const f32x4 t = *(const LAS f32x4*)(xp + (c * 4 + g) * 1024);
#pragma unroll
                  for (int e = 0; e < 4; ++e) v[4 * g + e] = (G ? o[2 + c][4 * g + e] : o[c][4 * g + e]) * c0 + t[e] * c1; }
              { const bf16x8 t0 = PACK8(v, 0), t1 = PACK8(v, 8); ST_SC1(dst + c * 8192, vod, 0, t0); ST_SC1(dst + c * 8192 + 4096, vod, 0, t1); }
          }
          float kvv[2][8];
          const int col = r32 < 16 ? r32 : 16;
#pragma unroll
          for (int s = 0; s < 2; ++s)
#pragma unroll
              for (int j = 0; j < 8; ++j) { const int f = 16 * s + 8 * (j >> 2) + 4 * hi + (j & 3); const float v = *(const LAS float*)(lds + st0 + 8192 + (f * 32 + col) * 4); kvv[s][j] = r32 <= 16 ? v : 0.f; }
          f32x16 nm = MFMA32(PACK8(kvv[0], 0), phq[0], f32x16{});
          nm = MFMA32(PACK8(kvv[1], 0), phq[1], nm);
          const float den = bfround(bfround(lo32_bcast(nm[8])) + 1e-6f); const float sc = (1.f - gate) / den;
          float ov[8];
#pragma unroll
          for (int r = 0; r < 8; ++r) ov[r] = bfround(nm[r]) * sc;
          const bf16x8 t0 = PACK8(ov, 0);
          ST_SC1(A3 + (size_t)((G ? bB : bA) * 576 + 512 + 2 * hh_) * 2048, vod, 0, t0);
        }
        WAIT_L(0); BAR(); asm volatile("" ::: "memory");
        STAMP();
    }
#undef KRD
#undef VRD
#undef PF
#undef DMA1
#undef ROT
#undef GOFF
#undef PREP_A
#undef PREP_B
#undef PREP_C
#undef PREP_D
#undef GAPA
#undef PVM
#undef RDB
#undef ROUND
#undef ROUND0
#undef DRAIN
    }
    {
        LANE_VARS();
        const int rt = 4 * (bid & 7) + (bid >> 6), ct = (bid >> 3) & 7;
        unsigned voffW; { const int row = lane >> 3, sl = lane & 7, nb = (sl >> 2) ^ ((row >> 1) & 1); voffW = (unsigned)(row * 1024 + nb * 64 + (sl & 3) * 16); }
        const char* gA = A3 + (size_t)((rt >> 1) * 576 + 2 * wid) * 2048 + (rt & 1) * 1024;
        const char* gWa = (const char*)wo_a + (size_t)wid * 16384 + ct * 128; const char* gWb = (const char*)wo_b + (size_t)wid * 16384 + ct * 128;
        const unsigned wb = lds0 + wid * 20480;
        const unsigned* rdy = CNT + (64 + (rt >> 1)) * 64;
        auto issue = [&](int u, unsigned slot) {
            const char* ga = gA + (size_t)u * 32768; const char* gw = u < 32 ? gWa + (size_t)u * 131072 : gWb + (size_t)(u - 32) * 131072;
            glds16c(ga, vlin, wb + slot); glds16c(ga + 2048, vlin, wb + slot + 1024); glds16(gw, voffW, wb + slot + 2048); glds16(gw + 8192, voffW, wb + slot + 3072); };
        unsigned seen, seenB; int hnext = 0;
        auto poll = [&]() { const unsigned long long w = __hip_atomic_load((const unsigned long long*)rdy, RLX_AGENT); seen = rfl((unsigned)w); seenB = rfl((unsigned)(w >> 32)); };
        auto next_head = [&]() -> int {
            unsigned spins = 0;
            while (hnext < 32 ? !((seen >> hnext) & 1u) : seenB != ~0u) { poll(); if (++spins > (1u << 20)) break; if (spins > 1) __builtin_amdgcn_s_sleep(2); }
            asm volatile("" ::: "memory");
            return hnext++; };
        poll();
        asm volatile("s_waitcnt vmcnt(0)\n\ts_barrier" ::: "memory");
        if (tid == 0) { __hip_atomic_fetch_or(CNT + (64 + bA) * 64, 1u << hh_, RLX_AGENT); __hip_atomic_fetch_or(CNT + (64 + bB) * 64, 1u << hh_, RLX_AGENT);
                        __hip_atomic_fetch_or(CNT + (64 + bA) * 64 + 1, 1u << hh_, RLX_AGENT); __hip_atomic_fetch_or(CNT + (64 + bB) * 64 + 1, 1u << hh_, RLX_AGENT); }
        STAMP();
#pragma unroll
        for (int i = 0; i < 5; ++i) issue(next_head(), i * 4096u);
        f32x16 acc[2][2] = {};
        const unsigned ao = (unsigned)(wid * 20480 + hi * 1024 + r32 * 16);
        unsigned bo[2];
#pragma unroll
        for (int nb = 0; nb < 2; ++nb) bo[nb] = (unsigned)(wid * 20480 + 2048 + (4 * hi + q4) * 128 + 64 * (nb ^ ((q4 >> 1) & 1)) + 32 * blk + 8 * p4);
        bf16x8 af[2][2], bf[2][2];
#define K3RD(buf, sb) do { af[buf][0] = ldsr128((sb) + ao); bf[buf][0] = cat8(trrd((sb) + bo[0]), trrd((sb) + bo[0] + 1024)); \
        af[buf][1] = ldsr128((sb) + ao + 512); bf[buf][1] = cat8(trrd((sb) + bo[1]), trrd((sb) + bo[1] + 1024)); } while (0)
#define K3BODY(buf, i) do { \
        const unsigned snx = sc == 16384u ? 0u : sc + 4096u; \
        if ((i) + 1 < 36) K3RD(buf ^ 1, lds + snx); \
        SBAR(); acc[0][0] = MFMA32(bf[buf][0], af[buf][0], acc[0][0]); acc[1][0] = MFMA32(bf[buf][0], af[buf][1], acc[1][0]); SBAR(); \
        if ((i) + 5 < 36) issue(next_head(), sc); \
        SBAR(); acc[0][1] = MFMA32(bf[buf][1], af[buf][0], acc[0][1]); acc[1][1] = MFMA32(bf[buf][1], af[buf][1], acc[1][1]); SBAR(); \
        sc = snx; } while (0)
#define K3W(n) asm volatile("s_waitcnt vmcnt(" #n ") lgkmcnt(0)" ::: "memory")
        unsigned sc = 0u;
        WAIT_V(16);
        K3RD(0, lds);
        for (int i = 0; i < 32; i += 2) {
            if (G) __builtin_amdgcn_s_setprio(1); K3W(12); K3BODY(0, i);
            if (G) __builtin_amdgcn_s_setprio(0); K3W(12); K3BODY(1, i + 1); }
        K3W(8); K3BODY(0, 32); K3W(4); K3BODY(1, 33); K3W(0); K3BODY(0, 34); K3W(0); K3BODY(1, 35);
#undef K3RD
#undef K3BODY
#undef K3W
        STAMP();
        BAR(); asm volatile("" ::: "memory");
#pragma unroll
        for (int mi = 0; mi < 2; ++mi)
#pragma unroll
            for (int nb = 0; nb < 2; ++nb)
#pragma unroll
                for (int g = 0; g < 4; ++g) { const int row = 32 * mi + r32, ch = 8 * nb + 2 * g + hi;
                    *(LAS f32x4*)(lds + wid * 16384 + row * 256 + 16 * (ch ^ (row & 15))) = (f32x4){acc[mi][nb][4 * g], acc[mi][nb][4 * g + 1], acc[mi][nb][4 * g + 2], acc[mi][nb][4 * g + 3]}; }
        WAIT_L(0); BAR(); asm volatile("" ::: "memory");
        { const int row = tid >> 3, c8 = tid & 7; f32x4 s0 = {}, s1 = {};
#pragma unroll
          for (int w = 0; w < 8; ++w) { s0 += *(const LAS f32x4*)(lds + w * 16384 + row * 256 + 16 * ((2 * c8) ^ (row & 15)));
                                        s1 += *(const LAS f32x4*)(lds + w * 16384 + row * 256 + 16 * ((2 * c8 + 1) ^ (row & 15))); }
          u32x4 w = {cvtpk(s0[0], s0[1]), cvtpk(s0[2], s0[3]), cvtpk(s1[0], s1[1]), cvtpk(s1[2], s1[3])};
          *(u32x4*)(out + (size_t)(rt * 64 + row) * 512 + ct * 64 + c8 * 8) = w; }
    }
    STAMP(); STAMP_RT();
    STAMP_OUT(ws + WS_END);
}

extern "C" void kernel_launch(void* const* d_in, const int* in_sizes, int n_in, void* d_out, int out_size, void* d_ws, size_t ws_size,
                              hipStream_t stream) {
    (void)in_sizes; (void)n_in; (void)out_size; (void)ws_size;
    char* ws = (char*)d_ws;
    (void)hipFuncSetAttribute((const void*)kf_all, hipFuncAttributeMaxDynamicSharedMemorySize, LDS_BYTES);
    kf_all<<<256, 512, LDS_BYTES, stream>>>((const u16*)d_in[0], (const u16*)d_in[1], (const u16*)d_in[2], (const u16*)d_in[3], (const u16*)d_in[4], (const u16*)d_in[5],
                                            (const u16*)d_in[7], (const u16*)d_in[8], (const u16*)d_in[9], (const u16*)d_in[10], ws, (u16*)d_out);
}
```

```cpp
#include <hip/hip_runtime.h>
#include <hip/hip_bf16.h>
#include <stdint.h>
#define PREFETCH 1

typedef unsigned short u16;
typedef short bf16x8 __attribute__((ext_vector_type(8)));
typedef short s16x4 __attribute__((ext_vector_type(4)));
typedef float f32x2 __attribute__((ext_vector_type(2)));
typedef float f32x4 __attribute__((ext_vector_type(4)));
typedef float f32x16 __attribute__((ext_vector_type(16)));
typedef unsigned u32x4 __attribute__((ext_vector_type(4)));
typedef unsigned u32x2 __attribute__((ext_vector_type(2)));
typedef __bf16 bf16x2_t __attribute__((ext_vector_type(2)));
#define LAS __attribute__((address_space(3)))
#define DI __device__ __forceinline__
typedef LAS const char* lds_cptr;
typedef LAS char* lds_ptr;

constexpr int SEQ = 2048, DM = 512, NH = 32, HD = 128;
constexpr size_t WS_CNT = 0;
constexpr size_t WS_HQ = 32768, WS_HR = 33792;
constexpr size_t WS_KV = 65536;
constexpr size_t WS_A3 = WS_KV + (size_t)NH * 32 * 32768;
constexpr size_t WS_KVP = WS_A3 + (size_t)576 * SEQ * 16;
constexpr size_t WS_QB = WS_KVP + (size_t)NH * 8 * 4096;
constexpr size_t WS_PQ = WS_QB + (size_t)256 * 32768;
constexpr size_t WS_END = WS_PQ + (size_t)256 * 16384;
constexpr int LDS_BYTES = 163840;

#define MFMA32(a, b, c) __builtin_amdgcn_mfma_f32_32x32x16_bf16((a), (b), (c), 0, 0, 0)
#define MFMA16(a, b, c) __builtin_amdgcn_mfma_f32_16x16x32_bf16((a), (b), (c), 0, 0, 0)
#define SBAR() __builtin_amdgcn_sched_barrier(0)
#define WAIT_V(n) asm volatile("s_waitcnt vmcnt(" #n ")" ::: "memory")
#define WAIT_L(n) asm volatile("s_waitcnt lgkmcnt(" #n ")" ::: "memory")
#define BAR() __builtin_amdgcn_s_barrier()
#define WAIT_BAR(N) asm volatile("s_waitcnt vmcnt(" #N ") lgkmcnt(0)\n\ts_barrier" ::: "memory")

#define STAMP_DECL()
#define STAMP()
#define STAMP_RT()
#define STAMP_OUT(base)
#define STAMP_PARAM
#define STAMP_ARG
DI unsigned rfl(unsigned x) { return __builtin_amdgcn_readfirstlane(x); }
DI unsigned cvtpk(float lo, float hi) { f32x2 v = {lo, hi}; bf16x2_t b = __builtin_convertvector(v, bf16x2_t); return __builtin_bit_cast(unsigned, b); }
DI float bfround(float x) { return __uint_as_float(cvtpk(x, x) << 16); }
DI void glds16(const void* sbase, unsigned voff, unsigned lds_dst) {
    unsigned sv;
    asm volatile("s_mov_b32 %0, m0\n\ts_mov_b32 m0, %3\n\ts_nop 2\n\tglobal_load_lds_dwordx4 %1, %2\n\ts_mov_b32 m0, %0"
                 : "=&s"(sv) : "v"(voff), "s"(sbase), "s"(lds_dst) : "memory");
}
DI void glds16c(const void* sbase, unsigned voff, unsigned lds_dst) {
    unsigned sv;
    asm volatile("s_mov_b32 %0, m0\n\ts_mov_b32 m0, %3\n\ts_nop 2\n\tglobal_load_lds_dwordx4 %1, %2 sc1\n\ts_mov_b32 m0, %0"
                 : "=&s"(sv) : "v"(voff), "s"(sbase), "s"(lds_dst) : "memory");
}
DI void touch8k(const void* sbase, unsigned voff, unsigned lds_dst) {
    unsigned sv;
    asm volatile("s_mov_b32 %0, m0\n\ts_mov_b32 m0, %3\n\ts_nop 2\n\tglobal_load_lds_dword %1, %2\n\ts_mov_b32 m0, %0"
                 : "=&s"(sv) : "v"(voff), "s"(sbase), "s"(lds_dst) : "memory");
}
DI void glds16c_unless(const void* sbase, unsigned voff, unsigned lds_dst, int a, int b) {
    unsigned sv;
    asm volatile("s_cmp_lt_i32 %4, %5\n\ts_cselect_b64 exec, 0, -1\n\ts_mov_b32 %0, m0\n\ts_mov_b32 m0, %3\n\ts_nop 2\n\tglobal_load_lds_dwordx4 %1, %2 sc1\n\ts_mov_b32 m0, %0\n\ts_mov_b64 exec, -1"
                 : "=&s"(sv) : "v"(voff), "s"(sbase), "s"(lds_dst), "s"(a), "s"(b) : "memory", "scc");
}
DI void ldq_unless(bf16x8* q, const void* sbase, unsigned voff, unsigned skip) {
    asm volatile("s_cmp_lg_u32 %10, 0\n\ts_cbranch_scc0 .Lrare%=\n.Lback%=:\n\t.subsection 1\n.Lrare%=:\n\ts_nop 4\n\t"
                 "global_load_dwordx4 %0, %8, %9 offset:-4096\n\tglobal_load_dwordx4 %1, %8, %9 offset:-3072\n\tglobal_load_dwordx4 %2, %8, %9 offset:-2048\n\tglobal_load_dwordx4 %3, %8, %9 offset:-1024\n\t"
                 "global_load_dwordx4 %4, %8, %9\n\tglobal_load_dwordx4 %5, %8, %9 offset:1024\n\tglobal_load_dwordx4 %6, %8, %9 offset:2048\n\tglobal_load_dwordx4 %7, %8, %9 offset:3072\n\ts_branch .Lback%=\n\t.subsection 0"
                 : "+v"(q[0]), "+v"(q[1]), "+v"(q[2]), "+v"(q[3]), "+v"(q[4]), "+v"(q[5]), "+v"(q[6]), "+v"(q[7]) : "v"(voff), "s"((const char*)sbase + 4096), "s"(skip) : "memory", "scc");
}
DI void glds4c(const void* sbase, unsigned lds_dst) {
    unsigned sv, z;
    asm volatile("v_mov_b32 %1, 0\n\ts_mov_b32 %0, m0\n\ts_mov_b32 m0, %3\n\ts_nop 2\n\tglobal_load_lds_dword %1, %2 sc1\n\ts_mov_b32 m0, %0"
                 : "=&s"(sv), "=&v"(z) : "s"(sbase), "s"(lds_dst) : "memory");
}
typedef short v4i16_t __attribute__((ext_vector_type(4)));
DI s16x4 trrd(lds_cptr p) { return __builtin_bit_cast(s16x4, __builtin_amdgcn_ds_read_tr16_b64_v4i16((LAS v4i16_t*)p)); }
DI bf16x8 cat8(s16x4 a, s16x4 b) { return __builtin_shufflevector(a, b, 0, 1, 2, 3, 4, 5, 6, 7); }
DI bf16x8 ldsr128(lds_cptr p) { return *(const LAS bf16x8*)p; }
DI bf16x8 pack8(float a0, float a1, float a2, float a3, float a4, float a5, float a6, float a7) {
    u32x4 w = {cvtpk(a0, a1), cvtpk(a2, a3), cvtpk(a4, a5), cvtpk(a6, a7)}; return __builtin_bit_cast(bf16x8, w); }
#define PACK8(X, B) pack8((X)[(B)], (X)[(B) + 1], (X)[(B) + 2], (X)[(B) + 3], (X)[(B) + 4], (X)[(B) + 5], (X)[(B) + 6], (X)[(B) + 7])
DI float swap32_max(float v) { auto rr = __builtin_amdgcn_permlane32_swap(__float_as_uint(v), __float_as_uint(v), false, false); return fmaxf(__uint_as_float(rr[0]), __uint_as_float(rr[1])); }
DI float swap32_add(float v) { auto rr = __builtin_amdgcn_permlane32_swap(__float_as_uint(v), __float_as_uint(v), false, false); return __uint_as_float(rr[0]) + __uint_as_float(rr[1]); }
DI float lo32_bcast(float v) { auto rr = __builtin_amdgcn_permlane32_swap(__float_as_uint(v), __float_as_uint(v), false, false); return __uint_as_float(rr[0]); }
template <int CTRL> DI float dpp(float v) { return __uint_as_float(__builtin_amdgcn_update_dpp(0u, __float_as_uint(v), CTRL, 0xF, 0xF, true)); }
DI float row16_max(float v) { v = fmaxf(v, dpp<0xB1>(v)); v = fmaxf(v, dpp<0x4E>(v)); v = fmaxf(v, dpp<0x141>(v)); v = fmaxf(v, dpp<0x140>(v)); return v; }
DI float row16_add(float v) { v += dpp<0xB1>(v); v += dpp<0x4E>(v); v += dpp<0x141>(v); v += dpp<0x140>(v); return v; }

constexpr float QSCALE = 0.08838834764831845f * 1.4426950408889634f;
constexpr float THR = 8.f;
constexpr int L_V = 98304;
constexpr int L_WE = 65536, L_GATE = 139264;
#define PIN(x) asm volatile("" : "+v"(x))
#define MX3(a, b, c) __builtin_fmaxf(__builtin_fmaxf((a), (b)), (c))
#define EX2(v) __builtin_amdgcn_exp2f(v)
#define RLX_AGENT __ATOMIC_RELAXED, __HIP_MEMORY_SCOPE_AGENT
#define ST_PL(base, voff, imm, v) asm volatile("s_nop 4\n\tglobal_store_dwordx4 %0, %1, %2 offset:%3\n\ts_nop 1" :: "v"(voff), "v"(v), "s"(base), "i"(imm) : "memory")
#define ST_SC1(base, voff, imm, v) asm volatile("s_nop 4\n\tglobal_store_dwordx4 %0, %1, %2 offset:%3 sc1\n\ts_nop 1" :: "v"(voff), "v"(v), "s"(base), "i"(imm) : "memory")
DI int fresh_lane() { int l; asm volatile("v_mbcnt_lo_u32_b32 %0, -1, 0\n\tv_mbcnt_hi_u32_b32 %0, -1, %0" : "=v"(l)); return l; }
DI void wait_cnt(const unsigned* p, unsigned want) {
    unsigned spins = 0;
    while (rfl(__hip_atomic_load(p, RLX_AGENT)) < want) { __builtin_amdgcn_s_sleep(2); if (++spins > (1u << 20)) break; }
}
DI void handshake_finish(char* ws, int bid, unsigned token, bool ok, int lane) {
    unsigned* CNT = (unsigned*)(ws + WS_CNT); unsigned spins = 0;
    if (bid == 0) {
        for (;;) {
            const unsigned a = __hip_atomic_load(CNT + (80 + (lane & 15)) * 64, RLX_AGENT);
            if (__all(a >= ((lane & 15) ? 16u : 15u))) break;
#pragma unroll
            for (int i = 0; i < 4; ++i) { const unsigned q = __hip_atomic_load((const unsigned*)(ws + WS_HQ) + i * 64 + lane, RLX_AGENT); __hip_atomic_store((unsigned*)(ws + WS_HR) + i * 64 + lane, q, RLX_AGENT); }
            __builtin_amdgcn_s_sleep(8); if (++spins > (1u << 18)) break; }
    } else if (!ok) {
        while (rfl(__hip_atomic_load((const unsigned*)(ws + WS_HR) + bid, RLX_AGENT)) != token) { __builtin_amdgcn_s_sleep(8); if (++spins > (1u << 20)) break; }
        if (lane == 0) __hip_atomic_fetch_add(CNT + (80 + (bid & 15)) * 64, 1u, RLX_AGENT);
    }
}
DI void dual_softmax(f32x16& p) {
#pragma unroll
    for (int hf = 0; hf < 2; ++hf) {
        float mx = -INFINITY;
#pragma unroll
        for (int r = 0; r < 8; ++r) { p[8 * hf + r] = bfround(p[8 * hf + r]); mx = fmaxf(mx, p[8 * hf + r]); }
        mx = swap32_max(mx); float s = 0.f;
#pragma unroll
        for (int r = 0; r < 8; ++r) { p[8 * hf + r] = EX2((p[8 * hf + r] - mx) * 1.4426950408889634f); s += p[8 * hf + r]; }
        s = __builtin_amdgcn_rcpf(swap32_add(s));
#pragma unroll
        for (int r = 0; r < 8; ++r) p[8 * hf + r] *= s;
    }
}
DI void mask_half(f32x16& sc, int dqw) {
    if (dqw < 0) {
#pragma unroll
        for (int r = 0; r < 16; ++r) sc[r] = -INFINITY;
    } else if (dqw == 0) {
        const int ln = fresh_lane(); const int lr = (ln & 31) - 4 * (ln >> 5); const float ninf = -INFINITY;
#pragma unroll
        for (int r = 0; r < 16; ++r)
            asm volatile("s_nop 0\n\tv_cmp_gt_i32 vcc, %c3, %2\n\tv_cndmask_b32 %0, %0, %1, vcc" : "+v"(sc[r]) : "v"(ninf), "v"(lr), "i"(16 * ((r >> 2) & 1) + 8 * (r >> 3) + (r & 3)) : "vcc");
    }
}
DI void mask_tile(f32x16& s0, f32x16& s1, int dqw) {
    asm volatile("s_nop 7\n\ts_nop 7" ::: "memory");
    mask_half(s0, dqw); mask_half(s1, dqw - 32); }

__global__ __launch_bounds__(512, 2) void kf_all(const u16* __restrict__ x, const u16* __restrict__ ckv, const u16* __restrict__ wq, const u16* __restrict__ wuk,
                                                 const u16* __restrict__ wuv, const u16* __restrict__ wo_a, const u16* __restrict__ wqkv,
                                                 const u16* __restrict__ wphi, const u16* __restrict__ wo_b, const u16* __restrict__ gatew,
                                                 char* ws, u16* __restrict__ out) {
    extern __shared__ __attribute__((aligned(16))) char shm[];
    const lds_ptr lds = (lds_ptr)shm; const unsigned lds0 = (unsigned)(uintptr_t)shm;
    const int wid = rfl(threadIdx.x >> 6), G = wid >> 2, w4 = wid & 3;
    const int bid = blockIdx.x, hh_ = 4 * (bid & 7) + (bid >> 6), jj = (bid >> 3) & 7;
#define LANE_VARS() int tid = threadIdx.x; asm volatile("" : "+v"(tid)); const int lane = tid & 63, r32 = lane & 31, hi = lane >> 5, blk = (lane >> 4) & 1, \
        q4 = (lane & 15) >> 2, p4 = lane & 3; const unsigned vlin = (unsigned)lane * 16u; (void)r32; (void)hi; (void)blk; (void)q4; (void)p4; (void)vlin
    unsigned* const CNT = (unsigned*)(ws + WS_CNT);
    char* const KVh = ws + WS_KV + (size_t)hh_ * 32 * 32768;
    char* const A3 = ws + WS_A3;
    STAMP_DECL(); STAMP_RT(); STAMP();
    u32x4 pkv[16];
    unsigned token = 0u;
    const int bA = 15 - jj, bB = jj;
    const char* gX = (const char*)x + (size_t)((G ? bB : bA) * 128 + 32 * w4) * 1024; const char* gW = (const char*)wq + hh_ * 256; const char* gE = (const char*)wqkv + hh_ * 32;
    const unsigned lx = lds0 + wid * 4096, lw = lds0 + L_WE + wid * 1024;
    unsigned voffX, voffW, voffE;
    { const int lane = threadIdx.x & 63;
        { const int row = lane >> 3, ch = (lane & 7) ^ ((row >> 1) & 3); voffX = (unsigned)(row * 1024 + ch * 16); }
        { const int row = 4 * wid + (lane >> 4), j = lane & 15, c = (j >> 2) ^ (row & 3); voffW = (unsigned)(row * 8192 + c * 64 + (j & 3) * 16); }
        { const int row = 8 * wid + (lane >> 3), sl = lane & 7, hf = (sl >> 2) ^ ((row >> 1) & 1), part = hf ? 2 : ((sl >> 1) & 1);
          voffE = (unsigned)(row * 3072 + part * 1024 + (sl & 1) * 16); }
        if (wid == 0) glds16(gatew, (unsigned)lane * 16u, lds0 + L_GATE);
        if (bid == 0 && wid == 1) {
            const unsigned z = 0u;
            asm volatile("global_store_dword %0, %1, %2 sc1\n\tglobal_store_dword %0, %1, %3 sc1\n\ts_nop 1" :: "v"((unsigned)lane * 256u), "v"(z), "s"(ws + WS_CNT), "s"(ws + WS_CNT + 16384) : "memory");
            asm volatile("global_store_dword %0, %1, %2 offset:4 sc1\n\ts_nop 1" :: "v"((unsigned)lane * 256u), "v"(z), "s"(ws + WS_CNT + 16384) : "memory"); }
        PIN(voffX); PIN(voffW); PIN(voffE);
    }
#define XISS(c) do { _Pragma("unroll") for (int e = 0; e < 4; ++e) glds16(gX + e * 8192 + (c) * 128, voffX ^ ((e & 1) << 6), lx + ((c) & 1) * 32768 + e * 1024); } while (0)
#define WISS(c) do { glds16(gW + (size_t)(c) * 524288, voffW, lw + ((c) % 3) * 24576); glds16(gW + (size_t)(c) * 524288 + 262144, voffW, lw + ((c) % 3) * 24576 + 8192); \
                     glds16(gE + (size_t)(c) * 196608, voffE, lw + ((c) % 3) * 24576 + 16384); } while (0)
    {
        LANE_VARS();
        const int wr = wid >> 2, wc = wid & 3, fr = lane & 15, fq = lane >> 4;
        constexpr int HTB = 16384;
#define SA(b, h) (((b) * 2 + (h)) * HTB)
#define SB(b, h) ((4 + (b) * 2 + (h)) * HTB)
        unsigned voffA, voffB;
        { const int row = 8 * wid + (lane >> 3), ch = (lane & 7) ^ ((row >> 1) & 7); voffA = (unsigned)(row * 1024 + ch * 16); }
        { const int row = 4 * wid + (lane >> 4), j = lane & 15, s = ((row >> 3) & 1) * 4 + (row & 3), c = (j >> 1) ^ s; voffB = (unsigned)(row * 8192 + c * 32 + (j & 1) * 16); }
        const char* gA = (const char*)ckv + (size_t)jj * 256 * 1024;
        const char* gB0 = (const char*)wuk + hh_ * 256; const char* gB1 = (const char*)wuv + hh_ * 256;
        const unsigned ldsw = rfl(lds0 + wid * 1024);
#define STAGE_A(b, h, kt) do { const char* g_ = gA + (h) * 131072 + (kt) * 128; glds16(g_, voffA, ldsw + SA(b, h)); glds16(g_ + 65536, voffA, ldsw + SA(b, h) + 8192); } while (0)
#define STAGE_B(b, h, kt) do { const char* g_ = ((h) ? gB1 : gB0) + (size_t)(kt) * 524288; glds16(g_, voffB, ldsw + SB(b, h)); glds16(g_ + 262144, voffB, ldsw + SB(b, h) + 8192); } while (0)
        lds_cptr pa[2], pb[2];
#pragma unroll
        for (int ks = 0; ks < 2; ++ks) pa[ks] = lds + (wr * 64 + fr) * 128 + 16 * ((4 * ks + fq) ^ ((fr >> 1) & 7));
        { const int q = fr >> 2, p = fr & 3, s = (fq & 1) * 4 + q;
#pragma unroll
          for (int n = 0; n < 2; ++n) pb[n] = lds + (8 * fq + q) * 256 + 32 * ((2 * wc + n) ^ s) + 8 * p; }
#define LDA(dst, b, h) do { _Pragma("unroll") for (int m = 0; m < 4; ++m) _Pragma("unroll") for (int k = 0; k < 2; ++k) dst[m][k] = ldsr128(pa[k] + SA(b, h) + m * 2048); } while (0)
#define LDB(dst, b, h) do { _Pragma("unroll") for (int n = 0; n < 2; ++n) _Pragma("unroll") for (int k = 0; k < 2; ++k) \
        dst[n][k] = cat8(trrd(pb[n] + SB(b, h) + k * 8192), trrd(pb[n] + SB(b, h) + k * 8192 + 1024)); } while (0)
#define MMA(ai, bj, At, Bt) do { __builtin_amdgcn_s_setprio(1); _Pragma("unroll") for (int m = 0; m < 4; ++m) _Pragma("unroll") for (int n = 0; n < 2; ++n) _Pragma("unroll") for (int k = 0; k < 2; ++k) \
        acc[ai][bj][m][n] = (bj) ? MFMA16(At[m][k], Bt[n][k], acc[ai][bj][m][n]) : MFMA16(Bt[n][k], At[m][k], acc[ai][bj][m][n]); __builtin_amdgcn_s_setprio(0); } while (0)
        const unsigned scr = lds0 + 143360 + wid * 256; const size_t gw = (size_t)(bid * 8 + wid);
        auto warm = [&](int slot) {
            if (slot == 0 && wid == 0) { token = rfl(*(const LAS unsigned*)(lds + 148480)) + 1u; const unsigned z = 0u, tk = token;
                asm volatile("s_nop 4\n\tglobal_store_dword %0, %1, %2 sc1\n\ts_nop 1" :: "v"(z), "v"(tk), "s"(ws + WS_HQ + bid * 4) : "memory"); }
            if (bid == 0 && wid == 1) {
                if (slot > 0) { const u32x4 rq = *(const LAS u32x4*)(lds + 147456 + lane * 16); ST_SC1(ws + WS_HR, (unsigned)lane * 16u, 0, rq); WAIT_L(0); }
                glds16c(ws + WS_HQ, (unsigned)lane * 16u, lds0 + 147456); }
            const unsigned vo = (unsigned)lane * 128u;
            if (slot == 0) { if (lane < 8) touch8k((const char*)x + gw * 1024, vo, scr); }
            else if (slot == 1) { if (lane < 16) touch8k((const char*)wq + gw * 2048, vo, scr); }
            else if (slot == 2) { if (lane < 6) touch8k((const char*)wqkv + gw * 768, vo, scr); }
            else if (slot == 3) { if (lane < 16) touch8k((const char*)wo_a + gw * 2048, vo, scr); }
            else if (slot == 4) { if (lane < 2) touch8k((const char*)wo_b + gw * 256, vo, scr); }
        };
        f32x4 acc[2][2][4][2] = {};
        bf16x8 At[4][2], B0[2][2], B1[2][2];
        constexpr int nt = 8;
        STAGE_B(0, 0, 0); STAGE_A(0, 0, 0); STAGE_B(0, 1, 0); STAGE_A(0, 1, 0);
        if (wid == 0) glds4c(ws + WS_HR + bid * 4, lds0 + 148480);
        if (wr == 1) BAR();
        if (wid == 0) { WAIT_V(5); } else { WAIT_V(4); } BAR();
        STAGE_B(1, 0, 1); STAGE_A(1, 0, 1); STAGE_B(1, 1, 1);
        if (wid == 0) { WAIT_V(7); } else { WAIT_V(6); } BAR();
        { const unsigned vo = (unsigned)lane * 128u;
          if (lane < 16) { touch8k((const char*)wuk + gw * 2048, vo, scr); touch8k((const char*)wuv + gw * 2048, vo, scr); }
          if (lane < 8) touch8k((const char*)ckv + gw * 1024, vo, scr); }
        STAMP();
        for (int t = 0; t < nt - 2; t += 2) {
            LDB(B0, 0, 0); SBAR(); LDA(At, 0, 0); STAGE_A(1, 1, t + 1);
            WAIT_L(8); BAR(); WAIT_L(0); MMA(0, 0, At, B0); BAR(); SBAR();
            LDB(B1, 0, 1); STAGE_B(0, 0, t + 2);
            BAR(); WAIT_L(0); MMA(0, 1, At, B1); BAR();
            LDA(At, 0, 1); STAGE_A(0, 0, t + 2);
            BAR(); WAIT_L(0); MMA(1, 0, At, B0); BAR(); SBAR();
            STAGE_B(0, 1, t + 2);
            WAIT_V(6); BAR(); MMA(1, 1, At, B1); BAR();
            warm(t);
            LDB(B0, 1, 0); SBAR(); LDA(At, 1, 0); STAGE_A(0, 1, t + 2);
            WAIT_L(8); BAR(); WAIT_L(0); MMA(0, 0, At, B0); BAR(); SBAR();
            LDB(B1, 1, 1); STAGE_B(1, 0, t + 3);
            BAR(); WAIT_L(0); MMA(0, 1, At, B1); BAR();
            LDA(At, 1, 1); STAGE_A(1, 0, t + 3);
            BAR(); WAIT_L(0); MMA(1, 0, At, B0); BAR(); SBAR();
            STAGE_B(1, 1, t + 3);
            WAIT_V(6); BAR(); MMA(1, 1, At, B1); BAR();
            warm(t + 1);
        }
        { LDB(B0, 0, 0); WAIT_V(0); LDA(At, 0, 0); STAGE_A(1, 1, nt - 1);
          BAR(); WAIT_L(0); MMA(0, 0, At, B0); BAR();
          LDB(B1, 0, 1); BAR(); WAIT_L(0); MMA(0, 1, At, B1); BAR();
          LDA(At, 0, 1); BAR(); WAIT_L(0); MMA(1, 0, At, B0); MMA(1, 1, At, B1); BAR(); }
        { LDB(B0, 1, 0); LDA(At, 1, 0); BAR(); WAIT_L(0); MMA(0, 0, At, B0); BAR();
          LDB(B1, 1, 1); WAIT_V(0);
          WISS(0); XISS(0);
          BAR(); WAIT_L(0); MMA(0, 1, At, B1); BAR();
          LDA(At, 1, 1); BAR(); WAIT_L(0); MMA(1, 0, At, B0); MMA(1, 1, At, B1); BAR(); }
        if (wr == 0) BAR();
        WISS(1); XISS(1);
        if (wid == 0) glds4c(ws + WS_HR + bid * 4, lds0 + 148480);
        SBAR();
#undef SA
#undef SB
#undef STAGE_A
#undef STAGE_B
#undef LDA
#undef LDB
#undef MMA
#pragma unroll
        for (int ai = 0; ai < 2; ++ai) {
#pragma unroll
            for (int m = 0; m < 4; ++m) { const f32x4 a = acc[ai][0][m][0], b = acc[ai][0][m][1];
                pkv[ai * 4 + m] = (u32x4){cvtpk(a[0], a[1]), cvtpk(a[2], a[3]), cvtpk(b[0], b[1]), cvtpk(b[2], b[3])}; }
#pragma unroll
            for (int u = 0; u < 2; ++u)
#pragma unroll
                for (int n = 0; n < 2; ++n) { const f32x4 a = acc[ai][1][2 * u][n], b = acc[ai][1][2 * u + 1][n];
                    pkv[8 + ai * 4 + u * 2 + n] = (u32x4){cvtpk(a[0], a[1]), cvtpk(a[2], a[3]), cvtpk(b[0], b[1]), cvtpk(b[2], b[3])}; }
        }
    }
    STAMP();
    const char* kvw = KVh + w4 * 4096;
    const unsigned ldw = lds0 + w4 * 4096;
    char* const qbp = ws + WS_QB + (size_t)(bid * 4 + w4) * 8192;
    char* const pqp = ws + WS_PQ + (size_t)(bid * 8 + wid) * 2048;
    bf16x8 qf[8]; float gate;
    {
        LANE_VARS();
        u16 wph[8];
#pragma unroll
        for (int j = 0; j < 8; ++j) wph[j] = wphi[hh_ * 256 + (8 * (j >> 2) + 4 * hi + (j & 3)) * 16 + (lane & 15)];
        const char* tb0 = KVh + (size_t)(4 * jj + G) * 32768;
        const unsigned vok = (unsigned)((4 * w4 + (lane >> 4)) * 1024 + (lane & 15) * 16), vov = (unsigned)(16384 + (lane >> 4) * 2048 + (w4 * 32 + (lane & 15)) * 16);
#define KVST(i) do { if ((i) < 8) ST_SC1(tb0 + ((i) >> 2) * 65536, vok, ((i) & 3) * 256, pkv[i]); \
                     else ST_SC1(tb0 + (((i) >> 2) & 1) * 65536 + (((i) >> 1) & 1) * 8192, vov, ((i) & 1) * 256, pkv[i]); } while (0)
        f32x16 qa[5] = {}, d1 = {};
        unsigned xo[2], wo_[4];
#pragma unroll
        for (int s = 0; s < 2; ++s) xo[s] = (unsigned)((32 * wid + r32) * 128 + 16 * ((2 * s + hi) ^ ((r32 >> 1) & 7)));
#pragma unroll
        for (int c = 0; c < 4; ++c) wo_[c] = (unsigned)(L_WE + (8 * hi + q4) * 256 + 64 * (c ^ q4) + 32 * (p4 >> 1) + 16 * blk + 8 * (p4 & 1));
        const unsigned eo = (unsigned)(L_WE + 16384 + (8 * hi + q4) * 128 + 64 * ((q4 >> 1) & 1) + 32 * blk + 8 * p4);
        const unsigned vo_ = (unsigned)(L_WE + 16384 + (8 * hi + q4) * 128 + 64 * (((q4 >> 1) & 1) ^ 1) + 8 * p4);
        bf16x8 fx[2], fw[2][6], fg[2];
#define QR_(buf, c, ks, u) do { \
        if ((u) == 0) { fx[buf] = ldsr128(lds + ((c) & 1) * 32768 + (xo[(ks) & 1] ^ (((ks) >> 1) << 6))); fg[buf] = ldsr128(lds + L_GATE + ((c) * 64 + (ks) * 16 + 8 * hi) * 2); } \
        else if ((u) < 5) { fw[buf][(u) - 1] = cat8(trrd(lds + wo_[(u) - 1] + ((c) % 3) * 24576 + (ks) * 4096), trrd(lds + wo_[(u) - 1] + ((c) % 3) * 24576 + (ks) * 4096 + 1024)); } \
        else { fw[buf][4] = cat8(trrd(lds + eo + ((c) % 3) * 24576 + (ks) * 2048), trrd(lds + eo + ((c) % 3) * 24576 + (ks) * 2048 + 512)); \
               fw[buf][5] = cat8(trrd(lds + vo_ + ((c) % 3) * 24576 + (ks) * 2048), trrd(lds + vo_ + ((c) % 3) * 24576 + (ks) * 2048 + 512)); } } while (0)
#define QM_(buf, u) do { if ((u) < 5) qa[u] = MFMA32(fw[buf][u], fx[buf], qa[u]); else d1 = MFMA32(fx[buf], blk ? fg[buf] : fw[buf][5], d1); } while (0)
#define QRD(buf, c, ks) do { _Pragma("unroll") for (int u_ = 0; u_ < 6; ++u_) QR_(buf, c, ks, u_); } while (0)
#define QSTEP(buf, c, ks, rd) do { _Pragma("unroll") for (int u_ = 0; u_ < 6; ++u_) { if (rd) QR_((buf) ^ 1, c, ks, u_); QM_(buf, u_); SBAR(); } } while (0)
#define WV_(n) asm volatile("s_waitcnt vmcnt(%0)" :: "i"(n) : "memory")
#define NST(c) ((c) < 0 ? 0 : 2)
#define ST0(c) (2 * (c))
#define QCHUNK(c) do { \
        SBAR(); QSTEP(0, c, 1, true); QSTEP(1, c, 2, true); \
        if ((c) < 7) { WV_(4 + NST((c) - 1)); } BAR(); asm volatile("" ::: "memory"); \
        if ((c) < 6) WISS((c) + 2); \
        _Pragma("unroll") for (int i_ = 0; i_ < NST(c); ++i_) KVST(ST0(c) + i_); \
        SBAR(); QSTEP(0, c, 3, true); \
        if ((c) < 7) { WV_(((c) < 6 ? 3 : 0) + NST(c)); } \
        SBAR(); QSTEP(1, (c) + 1, 0, (c) < 7); \
        if ((c) < 6) XISS((c) + 2); } while (0)
        if (wid == 0) { WAIT_V(8); } else { WAIT_V(7); } BAR(); asm volatile("" ::: "memory");
        bool hs_ok = true;
        bf16x8 wpf;
#pragma unroll
        for (int j = 0; j < 8; ++j) wpf[j] = (short)(blk ? (wph[j] ^ 0x8000) : wph[j]);
        PIN(wpf);
        STAMP();
        QRD(0, 0, 0);
        QCHUNK(0);
        {
            unsigned t0, t1, seen, em;
            asm volatile("v_mov_b32 %0, %5\n\tds_read_b32 %0, %0\n\tv_mov_b32 %1, 1\n\ts_waitcnt lgkmcnt(0)\n\tv_readfirstlane_b32 %2, %0\n\tv_mov_b32 %0, 0\n\ts_nop 3\n\t"
                         "s_min_u32 %3, %8, 1\n\ts_cmp_eq_u32 %7, 0\n\ts_cselect_b32 %3, %3, 0\n\t"
                         "s_cmp_eq_u32 %2, %6\n\ts_cselect_b32 %2, 1, 0\n\ts_and_b32 %3, %3, %2\n\ts_mov_b32 exec_lo, %3\n\ts_mov_b32 exec_hi, 0\n\t"
                         "global_atomic_add %0, %1, %4\n\ts_mov_b64 exec, -1"
                         : "=&v"(t0), "=&v"(t1), "=&s"(seen), "=&s"(em) : "s"(CNT + (80 + (bid & 15)) * 64), "s"(lds0 + 148480), "s"(token), "s"(wid), "s"(bid) : "memory", "scc");
            hs_ok = seen != 0u; }
        QCHUNK(1); QCHUNK(2); QCHUNK(3); QCHUNK(4); QCHUNK(5); QCHUNK(6); QCHUNK(7);
#undef QRD
#undef QR_
#undef QM_
#undef QSTEP
#undef QCHUNK
#undef XISS
#undef WISS
#undef KVST
#undef NST
#undef ST0
        STAMP();
#pragma unroll
        for (int cc = 0; cc < 4; ++cc) {
#pragma unroll
            for (int r = 0; r < 16; ++r) qa[cc][r] *= QSCALE;
            qf[2 * cc] = PACK8(qa[cc], 0); qf[2 * cc + 1] = PACK8(qa[cc], 8);
        }
        f32x16 pq = MFMA32(wpf, PACK8(qa[4], 0), f32x16{});
        f32x16 pk = MFMA32(wpf, PACK8(qa[4], 8), f32x16{});
        dual_softmax(pq); dual_softmax(pk);
        f32x16 d3;
        { bf16x8 id0, id1;
#pragma unroll
          for (int j = 0; j < 8; ++j) { const int f = 8 * (j >> 2) + 4 * hi + (j & 3); id0[j] = (short)(r32 == f ? 0x3F80 : 0); id1[j] = (short)(r32 == f + 16 ? 0x3F80 : 0); }
          d3 = MFMA32(PACK8(pk, 0), id0, f32x16{}); d3 = MFMA32(PACK8(pk, 8), id1, d3); }
        const bf16x8 ones = {0x3F80, 0x3F80, 0x3F80, 0x3F80, 0x3F80, 0x3F80, 0x3F80, 0x3F80};
        const bf16x8 v0 = PACK8(d1, 0), v1 = PACK8(d1, 8);
        f32x16 d4 = MFMA32(PACK8(d3, 0), blk ? ones : v0, f32x16{});
        d4 = MFMA32(PACK8(d3, 8), blk ? ones : v1, d4);
        bf16x8 pq0 = PACK8(pq, 0), pq1 = PACK8(pq, 8); PIN(pq0); PIN(pq1); PIN(d4);
        if (wid == 0) handshake_finish(ws, bid, token, hs_ok, lane);
        WAIT_BAR(0);
        if (tid == 0) __hip_atomic_fetch_add(CNT + hh_ * 64, 1u, RLX_AGENT);
        STAMP();
        LAS float* red = (LAS float*)(lds + L_V + G * 32768 + w4 * 4096);
        if (r32 == 16) {
#pragma unroll
            for (int r = 0; r < 16; ++r) red[(r & 3) + 8 * (r >> 2) + 4 * hi] = d1[r];
        }
        WAIT_L(0);
        { const float gz = bfround(red[r32]); gate = bfround(1.f / (1.f + __expf(-gz))); }
        PIN(gate);
#pragma unroll
        for (int r = 0; r < 16; ++r) red[((r & 3) + 8 * (r >> 2) + 4 * hi) * 32 + r32] = d4[r];
        ST_PL(pqp, vlin, 0, pq0); ST_PL(pqp, vlin, 1024, pq1);
        if (G == 0) {
#pragma unroll
            for (int s = 0; s < 8; ++s) ST_PL(qbp + (s >> 2) * 4096, vlin, (s & 3) * 1024, qf[s]);
        }
        WAIT_L(0); BAR(); asm volatile("" ::: "memory");
        if (tid < 256) {
            f32x4 s = {0.f, 0.f, 0.f, 0.f};
#pragma unroll
            for (int w = 0; w < 8; ++w) s += *(const LAS f32x4*)(lds + L_V + (w >> 2) * 32768 + (w & 3) * 4096 + tid * 16);
            ST_SC1(ws + WS_KVP + (size_t)(hh_ * 8 + jj) * 4096, (unsigned)tid * 16u, 0, s);
        }
        wait_cnt(CNT + hh_ * 64, 8u);
        asm volatile("" ::: "memory");
#pragma unroll
        for (int e = 0; e < 4; ++e) glds16c(kvw + (size_t)(e >> 1) * 32768 + (2 * G + (e & 1)) * 1024, vlin, ldw + (e >> 1) * 16384 + (2 * G + (e & 1)) * 1024);
        WAIT_BAR(0);
    }
    STAMP();
    {
    LANE_VARS();
    const int prow = (r32 & 7) | ((r32 & 8) << 1) | ((r32 & 16) >> 1);
    const unsigned kro = (unsigned)(prow * 16 + hi * 1024), vro = (unsigned)(L_V + r32 * 16 + hi * 2048);
    const int dq0 = 32 * w4;
    const int T = 2 * jj + 2;
    unsigned ks0 = 0u, ks1 = 16384u, ks2 = 32768u;
    int rho = 0; const int TG = G ? T : 99;
    f32x16 o[4], negm, sA0, sA1, sB0, sB1; bf16x8 kf[16], vf[16]; u32x4 pw[4]; float m, l;
#define KRD(i, kp) kf[i] = ldsr128((kp) + ((i) >> 1) * 2048 + ((i) & 1) * 512)
#define VRD(i, vp) vf[i] = ldsr128((vp) + ((i) >> 2) * 4096 + ((i) & 3) * 512)
#define PF(k) __builtin_bit_cast(bf16x8, pw[k])
#define DMA1(i) do { if ((i) < 2) glds16c(gk_ + (i) * 1024, vlin, dk_ + (i) * 1024); else if ((i) < 4) glds16c_unless(gk_ + (i) * 1024, vlin, dk_ + (i) * 1024, rk_, T); \
                     else if ((i) < 6) glds16c(gv_ + ((i) - 4) * 1024, vlin, dv_ + ((i) - 4) * 1024); else glds16c_unless(gv_ + ((i) - 4) * 1024, vlin, dv_ + ((i) - 4) * 1024, rho, T); } while (0)
    const char* gk_; const char* gv_; unsigned dk_, dv_, ko_, vo_; int rk_;
    unsigned go1, go2;
#define GOFF(r) ((r) < T ? (unsigned)(r) * 32768u + G * 2048u : (unsigned)(2 * (r) - T + G) * 32768u)
#define PREP_A(r, k0) do { ko_ = ((r) >= TG ? 49152u : 0u) + (k0); vo_ = ((r) - 1 >= TG ? 32768u : 0u) + (((r) - 1) & 1) * 16384u; } while (0)
#define PREP_B(r, gcur) do { gv_ = kvw + ((gcur) + 16384u); dv_ = ldw + L_V + ((r) & 1) * 16384u + ((r) < T ? G * 2048u : G * 32768u); } while (0)
#define PREP_C(r) do { rk_ = (r) + 2 <= 16 ? (r) + 2 : 16; go1 = go2; go2 = GOFF(rk_); } while (0)
#define PREP_D(k2) do { gk_ = kvw + go2; dk_ = ldw + (k2) + (rk_ < T ? G * 2048u : G * 49152u); } while (0)
#define ROT() do { const unsigned t_ = ks0; ks0 = ks1; ks1 = ks2; ks2 = t_; ++rho; } while (0)
#define GAPA(i, C0, C1, P0, P1) do { \
        if ((i) + 4 < 16) { KRD((i) + 4, kp_); } else { VRD((i) - 12, vp_); } \
        if (((i) & 1) == 0) DMA1((i) >> 1); \
        if ((i) & 1) C1 = MFMA32(kf[i], qf[(i) >> 1], (i) < 2 ? negm : C1); else C0 = MFMA32(kf[i], qf[(i) >> 1], (i) < 2 ? negm : C0); \
        if ((i) < 8) { P1[2 * (i)] = EX2(P1[2 * (i)]); P1[2 * (i) + 1] = EX2(P1[2 * (i) + 1]); pw[(i) >> 2][(i) & 3] = cvtpk(P0[2 * (i)], P0[2 * (i) + 1]); PIN(P1); PIN(pw[(i) >> 2]); } \
        else { l += P1[2 * (i) - 16]; l2 += P1[2 * (i) - 15]; pw[(i) >> 2][(i) & 3] = cvtpk(P1[2 * (i) - 16], P1[2 * (i) - 15]); PIN(l); PIN(l2); PIN(pw[(i) >> 2]); } \
        SBAR(); } while (0)
#define PVM(i) o[(i) & 3] = MFMA32(vf[i], PF((i) >> 2), o[(i) & 3])
#define RDB(i) do { if ((i) + 4 < 16) { VRD((i) + 4, vp_); } else { KRD((i) - 12, kn_); } } while (0)
#define ROUND(C0, C1, P0, P1) do { \
        const lds_cptr kp_ = lds + ko_ + kro; const lds_cptr vp_ = lds + vo_ + vro; \
        float l2 = 0.f; lds_cptr kn_ = kp_; SBAR(); \
        GAPA(0, C0, C1, P0, P1); GAPA(1, C0, C1, P0, P1); GAPA(2, C0, C1, P0, P1); GAPA(3, C0, C1, P0, P1); \
        GAPA(4, C0, C1, P0, P1); GAPA(5, C0, C1, P0, P1); GAPA(6, C0, C1, P0, P1); GAPA(7, C0, C1, P0, P1); \
        GAPA(8, C0, C1, P0, P1); GAPA(9, C0, C1, P0, P1); GAPA(10, C0, C1, P0, P1); GAPA(11, C0, C1, P0, P1); \
        GAPA(12, C0, C1, P0, P1); GAPA(13, C0, C1, P0, P1); GAPA(14, C0, C1, P0, P1); GAPA(15, C0, C1, P0, P1); \
        l += l2; \
        ldq_unless(qf, qbp, vlin, (unsigned)(rho + 1 - TG));        \
        RDB(0); PVM(0); SBAR(); RDB(1); PVM(1); SBAR(); \
        if (__builtin_expect(rho == 16 || (rho >= TG - 2 && rho < TG), 0)) mask_tile(C0, C1, dq0 - (rho == 16 ? 64 * G : 64 * (rho - TG + 2))); \
        float ma, mb; \
        RDB(2); PVM(2); ma = MX3(C0[0], C0[1], C1[0]); mb = MX3(C0[2], C0[3], C1[1]); ma = MX3(ma, C1[2], C1[3]); mb = MX3(mb, C0[4], C0[5]); PIN(ma); PIN(mb); SBAR(); \
        RDB(3); PVM(3); ma = MX3(ma, C0[6], C0[7]); mb = MX3(mb, C1[4], C1[5]); ma = MX3(ma, C1[6], C1[7]); mb = MX3(mb, C0[8], C0[9]); PIN(ma); PIN(mb); SBAR(); \
        RDB(4); PVM(4); ma = MX3(ma, C0[10], C0[11]); mb = MX3(mb, C1[8], C1[9]); ma = MX3(ma, C1[10], C1[11]); mb = MX3(mb, C0[12], C0[13]); PIN(ma); PIN(mb); SBAR(); \
        RDB(5); PVM(5); ma = MX3(ma, C0[14], C0[15]); mb = MX3(mb, C1[12], C1[13]); ma = MX3(ma, C1[14], C1[15]); \
        const float rm = swap32_max(fmaxf(ma, mb)); bool pend = false; float fsc = 1.f; \
        if (__builtin_expect(__any(rm > THR) || rho == TG, 0)) { float dl; pend = true; \
            if (rho == TG) { dl = fmaxf(rm, -1e30f); m = dl; fsc = gate / swap32_add(l); l = 0.f; }        \
            else { dl = fmaxf(rm, 0.f); m += dl; fsc = EX2(-dl); l *= fsc; } \
            _Pragma("unroll") for (int e = 0; e < 16; ++e) { C0[e] -= dl; C1[e] -= dl; negm[e] = -m; } } \
        SBAR(); \
        RDB(6); PVM(6); C0[0] = EX2(C0[0]); C0[1] = EX2(C0[1]); PIN(C0); SBAR(); \
        RDB(7); PVM(7); C0[2] = EX2(C0[2]); C0[3] = EX2(C0[3]); l += C0[0]; l2 = C0[1]; PIN(C0); PIN(l); SBAR(); \
        RDB(8); PVM(8); PREP_A(rho + 1, ks1); kn_ = lds + ko_ + kro; C0[4] = EX2(C0[4]); C0[5] = EX2(C0[5]); l += C0[2]; l2 += C0[3]; PIN(C0); PIN(l); PIN(l2); SBAR(); \
        RDB(9); PVM(9); PREP_B(rho + 1, go1); C0[6] = EX2(C0[6]); C0[7] = EX2(C0[7]); l += C0[4]; l2 += C0[5]; PIN(C0); PIN(l); PIN(l2); SBAR(); \
        RDB(10); PVM(10); PREP_C(rho + 1); C0[8] = EX2(C0[8]); C0[9] = EX2(C0[9]); l += C0[6]; l2 += C0[7]; PIN(C0); PIN(l); PIN(l2); SBAR(); \
        RDB(11); PVM(11); PREP_D(ks0); C0[10] = EX2(C0[10]); C0[11] = EX2(C0[11]); l += C0[8]; l2 += C0[9]; PIN(C0); PIN(l); PIN(l2); SBAR(); \
        RDB(12); PVM(12); C0[12] = EX2(C0[12]); C0[13] = EX2(C0[13]); l += C0[10]; l2 += C0[11]; PIN(C0); PIN(l); PIN(l2); SBAR(); \
        RDB(13); PVM(13); C0[14] = EX2(C0[14]); C0[15] = EX2(C0[15]); l += C0[12]; l2 += C0[13]; PIN(C0); PIN(l); PIN(l2); SBAR(); \
        RDB(14); PVM(14); l += C0[14]; l2 += C0[15]; PIN(l); PIN(l2); SBAR(); \
        RDB(15); PVM(15); l += l2; PIN(l); SBAR(); \
        if (__builtin_expect(pend || rho + 1 == TG, 0)) { \
            if (pend) { _Pragma("unroll") for (int c = 0; c < 4; ++c) _Pragma("unroll") for (int e = 0; e < 16; ++e) o[c][e] *= fsc; } \
            if (rho + 1 == TG) negm = f32x16{}; \
            if (rho == TG) { const int ln = fresh_lane(); const unsigned vod = (unsigned)((ln >> 5) * 2048 + (32 * w4 + (ln & 31)) * 16); const char* dst = A3 + (size_t)(bB * 576 + hh_ * 16) * 2048; \
                _Pragma("unroll") for (int c = 0; c < 4; ++c) { const bf16x8 t0 = PACK8(o[c], 0), t1 = PACK8(o[c], 8); ST_SC1(dst + c * 8192, vod, 0, t0); ST_SC1(dst + c * 8192 + 4096, vod, 0, t1); o[c] = f32x16{}; } } } \
        asm volatile("s_cmp_eq_u32 %0, %1\n\ts_cbranch_scc1 .Lw%=\n\ts_waitcnt vmcnt(0)\n.Lw%=:\n\ts_waitcnt vmcnt(8)\n\ts_barrier" :: "s"(rho), "s"(TG) : "memory", "scc");     \
        if (__builtin_expect(rho == TG + 1 && w4 == 0, 0)) { if (fresh_lane() == 0) __hip_atomic_fetch_or(CNT + (64 + bB) * 64, 1u << hh_, RLX_AGENT); }        \
        ROT(); } while (0)

#define ROUND0(C0, C1) do { \
        const lds_cptr kp_ = lds + ks0 + kro; const lds_cptr kn_ = lds + ks1 + kro; \
        go2 = GOFF(1); PREP_B(0, GOFF(0)); PREP_C(0); PREP_D(ks2); \
        _Pragma("unroll") for (int i = 0; i < 6; ++i) KRD(i, kp_); \
        _Pragma("unroll") for (int i = 0; i < 8; ++i) DMA1(i); \
        _Pragma("unroll") for (int c = 0; c < 4; ++c) o[c] = f32x16{}; \
        PREP_A(1, ks1); PREP_B(1, go1); PREP_C(1); PREP_D(ks0); SBAR(); \
        _Pragma("unroll") for (int i = 0; i < 16; ++i) { \
            if (i + 6 < 16) KRD(i + 6, kp_); \
            if (i & 1) C1 = MFMA32(kf[i], qf[i >> 1], i < 2 ? f32x16{} : C1); else C0 = MFMA32(kf[i], qf[i >> 1], i < 2 ? f32x16{} : C0); \
            SBAR(); } \
        if (TG == 2) mask_tile(C0, C1, dq0); \
        float ma = MX3(C0[0], C0[1], C1[0]), mb = MX3(C0[2], C0[3], C1[1]); ma = MX3(ma, C1[2], C1[3]); \
        _Pragma("unroll") for (int r = 4; r < 16; r += 4) { ma = MX3(ma, C0[r], C0[r + 1]); mb = MX3(mb, C0[r + 2], C0[r + 3]); ma = MX3(ma, C1[r], C1[r + 1]); mb = MX3(mb, C1[r + 2], C1[r + 3]); } \
        m = fmaxf(swap32_max(fmaxf(ma, mb)), -1e30f); l = 0.f; \
        _Pragma("unroll") for (int i = 0; i < 4; ++i) KRD(i, kn_); \
        _Pragma("unroll") for (int e = 0; e < 16; ++e) { C0[e] = EX2(C0[e] - m); l += C0[e]; C1[e] -= m; negm[e] = -m; } \
        asm volatile("s_waitcnt vmcnt(0)\n\ts_barrier" ::: "memory"); ROT(); } while (0)
#define DRAIN(P0, P1, MID) do { \
        const lds_cptr vp_ = lds + G * 32768 + vro; \
        _Pragma("unroll") for (int i = 0; i < 8; ++i) VRD(i, vp_); \
        _Pragma("unroll") for (int i = 0; i < 8; ++i) pw[i >> 2][i & 3] = cvtpk(P0[2 * i], P0[2 * i + 1]); \
        SBAR(); \
        _Pragma("unroll") for (int i = 0; i < 16; ++i) { \
            if (i + 8 < 16) VRD(i + 8, vp_); \
            if (i == 8) { MID } \
            PVM(i); \
            if (i < 8) { P1[2 * i] = EX2(P1[2 * i]); P1[2 * i + 1] = EX2(P1[2 * i + 1]); PIN(P1); } \
            if (i >= 1 && i < 9) { l += P1[2 * i - 2]; l += P1[2 * i - 1]; pw[2 + ((i - 1) >> 2)][(i - 1) & 3] = cvtpk(P1[2 * i - 2], P1[2 * i - 1]); PIN(l); PIN(pw[2 + ((i - 1) >> 2)]); } \
            SBAR(); } \
        l = swap32_add(l); } while (0)
    ROUND0(sA0, sA1);
    if (wid == 0 && fresh_lane() == 0) __hip_atomic_fetch_add(CNT + (32 + hh_) * 64, 1u, RLX_AGENT);
    STAMP();
#pragma clang loop unroll(disable)
    for (int pr = 0; pr < 8; ++pr) { ROUND(sB0, sB1, sA0, sA1); ROUND(sA0, sA1, sB0, sB1); }
    STAMP();
    {
        f32x2 ps[8]; bf16x8 phq[2];
        LANE_VARS();
        const unsigned f2 = __hip_atomic_load(CNT + (32 + hh_) * 64, RLX_AGENT);
        DRAIN(sA0, sA1,
            if (rfl(f2) < 8u) wait_cnt(CNT + (32 + hh_) * 64, 8u);
            _Pragma("unroll") for (int w = 0; w < 8; ++w) ps[w] = __builtin_bit_cast(f32x2, __hip_atomic_load((const unsigned long long*)(ws + WS_KVP + (size_t)(hh_ * 8) * 4096 + (unsigned)(w * 4096 + tid * 8)), RLX_AGENT));
            phq[0] = *(const bf16x8*)(pqp + vlin); phq[1] = *(const bf16x8*)(pqp + (vlin + 1024)); );
        const unsigned vod = (unsigned)(hi * 2048 + (32 * w4 + r32) * 16);
        BAR(); asm volatile("" ::: "memory");
        const unsigned st0 = ks2;
        { const lds_ptr xp = lds + L_V + wid * 8192 + lane * 16;
#pragma unroll
          for (int c = 0; c < 2; ++c)
#pragma unroll
              for (int g = 0; g < 4; ++g) { f32x4 t;
#pragma unroll
                  for (int e = 0; e < 4; ++e) t[e] = G ? o[c][4 * g + e] : o[2 + c][4 * g + e];
                  *(LAS f32x4*)(xp + (c * 4 + g) * 1024) = t; }
          *(LAS f32x4*)(lds + st0 + wid * 1024 + lane * 16) = (f32x4){m, l, gate, 0.f};
          f32x2 s = ps[0];
#pragma unroll
          for (int w = 1; w < 8; ++w) s += ps[w];
          *(LAS f32x2*)(lds + st0 + 8192 + tid * 8) = s; }
        WAIT_L(0); BAR(); asm volatile("" ::: "memory");
        { const int ow = wid ^ 4;
          const f32x4 st = *(const LAS f32x4*)(lds + st0 + ow * 1024 + lane * 16);
          const float mn = fmaxf(m, st[0]), f0 = EX2(m - mn), f1 = EX2(st[0] - mn);
          const float gg = G ? st[2] : gate;
          const float inv = gg / (l * f0 + st[1] * f1); const float c0 = f0 * inv, c1 = f1 * inv;
          const lds_cptr xp = lds + L_V + ow * 8192 + lane * 16;
          const char* dst = A3 + (size_t)(bA * 576 + hh_ * 16 + G * 8) * 2048;
#pragma unroll
          for (int c = 0; c < 2; ++c) {
              float v[16];
#pragma unroll
              for (int g = 0; g < 4; ++g) { const f32x4 t = *(const LAS f32x4*)(xp + (c * 4 + g) * 1024);
#pragma unroll
                  for (int e = 0; e < 4; ++e) v[4 * g + e] = (G ? o[2 + c][4 * g + e] : o[c][4 * g + e]) * c0 + t[e] * c1; }
              { const bf16x8 t0 = PACK8(v, 0), t1 = PACK8(v, 8); ST_SC1(dst + c * 8192, vod, 0, t0); ST_SC1(dst + c * 8192 + 4096, vod, 0, t1); }
          }
          float kvv[2][8];
          const int col = r32 < 16 ? r32 : 16;
#pragma unroll
          for (int s = 0; s < 2; ++s)
#pragma unroll
              for (int j = 0; j < 8; ++j) { const int f = 16 * s + 8 * (j >> 2) + 4 * hi + (j & 3); const float v = *(const LAS float*)(lds + st0 + 8192 + (f * 32 + col) * 4); kvv[s][j] = r32 <= 16 ? v : 0.f; }
          f32x16 nm = MFMA32(PACK8(kvv[0], 0), phq[0], f32x16{});
          nm = MFMA32(PACK8(kvv[1], 0), phq[1], nm);
          const float den = bfround(bfround(lo32_bcast(nm[8])) + 1e-6f); const float sc = (1.f - gate) / den;
          float ov[8];
#pragma unroll
          for (int r = 0; r < 8; ++r) ov[r] = bfround(nm[r]) * sc;
          const bf16x8 t0 = PACK8(ov, 0);
          ST_SC1(A3 + (size_t)((G ? bB : bA) * 576 + 512 + 2 * hh_) * 2048, vod, 0, t0);
        }
        WAIT_L(0); BAR(); asm volatile("" ::: "memory");
        STAMP();
    }
#undef KRD
#undef VRD
#undef PF
#undef DMA1
#undef ROT
#undef GOFF
#undef PREP_A
#undef PREP_B
#undef PREP_C
#undef PREP_D
#undef GAPA
#undef PVM
#undef RDB
#undef ROUND
#undef ROUND0
#undef DRAIN
    }
    {
        LANE_VARS();
        const int rt = 4 * (bid & 7) + (bid >> 6), ct = (bid >> 3) & 7;
        unsigned voffW; { const int row = lane >> 3, sl = lane & 7, nb = (sl >> 2) ^ ((row >> 1) & 1); voffW = (unsigned)(row * 1024 + nb * 64 + (sl & 3) * 16); }
        const char* gA = A3 + (size_t)((rt >> 1) * 576 + 2 * wid) * 2048 + (rt & 1) * 1024;
        const char* gWa = (const char*)wo_a + (size_t)wid * 16384 + ct * 128; const char* gWb = (const char*)wo_b + (size_t)wid * 16384 + ct * 128;
        const unsigned wb = lds0 + wid * 20480;
        const unsigned* rdy = CNT + (64 + (rt >> 1)) * 64;
        auto issue = [&](int u, unsigned slot) {
            const char* ga = gA + (size_t)u * 32768; const char* gw = u < 32 ? gWa + (size_t)u * 131072 : gWb + (size_t)(u - 32) * 131072;
            glds16c(ga, vlin, wb + slot); glds16c(ga + 2048, vlin, wb + slot + 1024); glds16(gw, voffW, wb + slot + 2048); glds16(gw + 8192, voffW, wb + slot + 3072); };
        unsigned seen, seenB; int hnext = 0;
        auto poll = [&]() { const unsigned long long w = __hip_atomic_load((const unsigned long long*)rdy, RLX_AGENT); seen = rfl((unsigned)w); seenB = rfl((unsigned)(w >> 32)); };
        auto next_head = [&]() -> int {
            unsigned spins = 0;
            while (hnext < 32 ? !((seen >> hnext) & 1u) : seenB != ~0u) { poll(); if (++spins > (1u << 20)) break; if (spins > 1) __builtin_amdgcn_s_sleep(2); }
            asm volatile("" ::: "memory");
            return hnext++; };
        poll();
        asm volatile("s_waitcnt vmcnt(0)\n\ts_barrier" ::: "memory");
        if (tid == 0) { __hip_atomic_fetch_or(CNT + (64 + bA) * 64, 1u << hh_, RLX_AGENT); __hip_atomic_fetch_or(CNT + (64 + bB) * 64, 1u << hh_, RLX_AGENT);
                        __hip_atomic_fetch_or(CNT + (64 + bA) * 64 + 1, 1u << hh_, RLX_AGENT); __hip_atomic_fetch_or(CNT + (64 + bB) * 64 + 1, 1u << hh_, RLX_AGENT); }
        STAMP();
#pragma unroll
        for (int i = 0; i < 5; ++i) issue(next_head(), i * 4096u);
        f32x16 acc[2][2] = {};
        const unsigned ao = (unsigned)(wid * 20480 + hi * 1024 + r32 * 16);
        unsigned bo[2];
#pragma unroll
        for (int nb = 0; nb < 2; ++nb) bo[nb] = (unsigned)(wid * 20480 + 2048 + (4 * hi + q4) * 128 + 64 * (nb ^ ((q4 >> 1) & 1)) + 32 * blk + 8 * p4);
        bf16x8 af[2][2], bf[2][2];
#define K3RD(buf, sb) do { af[buf][0] = ldsr128((sb) + ao); bf[buf][0] = cat8(trrd((sb) + bo[0]), trrd((sb) + bo[0] + 1024)); \
        af[buf][1] = ldsr128((sb) + ao + 512); bf[buf][1] = cat8(trrd((sb) + bo[1]), trrd((sb) + bo[1] + 1024)); } while (0)
#define K3BODY(buf, i) do { \
        const unsigned snx = sc == 16384u ? 0u : sc + 4096u; \
        if ((i) + 1 < 36) K3RD(buf ^ 1, lds + snx); \
        SBAR(); acc[0][0] = MFMA32(bf[buf][0], af[buf][0], acc[0][0]); acc[1][0] = MFMA32(bf[buf][0], af[buf][1], acc[1][0]); SBAR(); \
        if ((i) + 5 < 36) issue(next_head(), sc); \
        SBAR(); acc[0][1] = MFMA32(bf[buf][1], af[buf][0], acc[0][1]); acc[1][1] = MFMA32(bf[buf][1], af[buf][1], acc[1][1]); SBAR(); \
        sc = snx; } while (0)
#define K3W(n) asm volatile("s_waitcnt vmcnt(" #n ") lgkmcnt(0)" ::: "memory")
        unsigned sc = 0u;
        WAIT_V(16);
        K3RD(0, lds);
        for (int i = 0; i < 32; i += 2) {
            if (G) __builtin_amdgcn_s_setprio(1); K3W(12); K3BODY(0, i);
            if (G) __builtin_amdgcn_s_setprio(0); K3W(12); K3BODY(1, i + 1); }
        K3W(8); K3BODY(0, 32); K3W(4); K3BODY(1, 33); K3W(0); K3BODY(0, 34); K3W(0); K3BODY(1, 35);
#undef K3RD
#undef K3BODY
#undef K3W
        STAMP();
        BAR(); asm volatile("" ::: "memory");
#pragma unroll
        for (int mi = 0; mi < 2; ++mi)
#pragma unroll
            for (int nb = 0; nb < 2; ++nb)
#pragma unroll
                for (int g = 0; g < 4; ++g) { const int row = 32 * mi + r32, ch = 8 * nb + 2 * g + hi;
                    *(LAS f32x4*)(lds + wid * 16384 + row * 256 + 16 * (ch ^ (row & 15))) = (f32x4){acc[mi][nb][4 * g], acc[mi][nb][4 * g + 1], acc[mi][nb][4 * g + 2], acc[mi][nb][4 * g + 3]}; }
        WAIT_L(0); BAR(); asm volatile("" ::: "memory");
        { const int row = tid >> 3, c8 = tid & 7; f32x4 s0 = {}, s1 = {};
#pragma unroll
          for (int w = 0; w < 8; ++w) { s0 += *(const LAS f32x4*)(lds + w * 16384 + row * 256 + 16 * ((2 * c8) ^ (row & 15)));
                                        s1 += *(const LAS f32x4*)(lds + w * 16384 + row * 256 + 16 * ((2 * c8 + 1) ^ (row & 15))); }
          u32x4 w = {cvtpk(s0[0], s0[1]), cvtpk(s0[2], s0[3]), cvtpk(s1[0], s1[1]), cvtpk(s1[2], s1[3])};
          *(u32x4*)(out + (size_t)(rt * 64 + row) * 512 + ct * 64 + c8 * 8) = w; }
    }
    STAMP(); STAMP_RT();
    STAMP_OUT(ws + WS_END);
}

extern "C" void kernel_launch(void* const* d_in, const int* in_sizes, int n_in, void* d_out, int out_size, void* d_ws, size_t ws_size,
                              hipStream_t stream) {
    (void)in_sizes; (void)n_in; (void)out_size; (void)ws_size;
    char* ws = (char*)d_ws;
    (void)hipFuncSetAttribute((const void*)kf_all, hipFuncAttributeMaxDynamicSharedMemorySize, LDS_BYTES);
    kf_all<<<256, 512, LDS_BYTES, stream>>>((const u16*)d_in[0], (const u16*)d_in[1], (const u16*)d_in[2], (const u16*)d_in[3], (const u16*)d_in[4], (const u16*)d_in[5],
                                            (const u16*)d_in[7], (const u16*)d_in[8], (const u16*)d_in[9], (const u16*)d_in[10], ws, (u16*)d_out);
}
```

```cpp
#include <hip/hip_runtime.h>
#include <hip/hip_bf16.h>
#include <stdint.h>
#define PD 4
#define PREFETCH 1

typedef unsigned short u16;
typedef short bf16x8 __attribute__((ext_vector_type(8)));
typedef short s16x4 __attribute__((ext_vector_type(4)));
typedef float f32x2 __attribute__((ext_vector_type(2)));
typedef float f32x4 __attribute__((ext_vector_type(4)));
typedef float f32x16 __attribute__((ext_vector_type(16)));
typedef unsigned u32x4 __attribute__((ext_vector_type(4)));
typedef unsigned u32x2 __attribute__((ext_vector_type(2)));
typedef __bf16 bf16x2_t __attribute__((ext_vector_type(2)));
#define LAS __attribute__((address_space(3)))
#define DI __device__ __forceinline__
typedef LAS const char* lds_cptr;
typedef LAS char* lds_ptr;

constexpr int SEQ = 2048, DM = 512, NH = 32, HD = 128;
constexpr size_t WS_CNT = 0;
constexpr size_t WS_HQ = 32768, WS_HR = 33792;
constexpr size_t WS_KV = 65536;
constexpr size_t WS_A3 = WS_KV + (size_t)NH * 32 * 32768;
constexpr size_t WS_KVP = WS_A3 + (size_t)576 * SEQ * 16;
constexpr size_t WS_QB = WS_KVP + (size_t)NH * 8 * 4096;
constexpr size_t WS_PQ = WS_QB + (size_t)256 * 32768;
constexpr size_t WS_W3 = WS_PQ + (size_t)256 * 16384;
constexpr size_t WS_END = WS_W3 + (size_t)8 * 288 * 2048;
constexpr int LDS_BYTES = 163840;

#define MFMA32(a, b, c) __builtin_amdgcn_mfma_f32_32x32x16_bf16((a), (b), (c), 0, 0, 0)
#define MFMA16(a, b, c) __builtin_amdgcn_mfma_f32_16x16x32_bf16((a), (b), (c), 0, 0, 0)
#define SBAR() __builtin_amdgcn_sched_barrier(0)
#define WAIT_V(n) asm volatile("s_waitcnt vmcnt(" #n ")" ::: "memory")
#define WAIT_L(n) asm volatile("s_waitcnt lgkmcnt(" #n ")" ::: "memory")
#define BAR() __builtin_amdgcn_s_barrier()
#define WAIT_BAR(N) asm volatile("s_waitcnt vmcnt(" #N ") lgkmcnt(0)\n\ts_barrier" ::: "memory")

#define STAMP_DECL()
#define STAMP()
#define STAMP_RT()
#define STAMP_OUT(base)
#define STAMP_PARAM
#define STAMP_ARG
DI unsigned rfl(unsigned x) { return __builtin_amdgcn_readfirstlane(x); }
DI unsigned cvtpk(float lo, float hi) { f32x2 v = {lo, hi}; bf16x2_t b = __builtin_convertvector(v, bf16x2_t); return __builtin_bit_cast(unsigned, b); }
DI float bfround(float x) { return __uint_as_float(cvtpk(x, x) << 16); }
DI void glds16(const void* sbase, unsigned voff, unsigned lds_dst) {
    unsigned sv;
    asm volatile("s_mov_b32 %0, m0\n\ts_mov_b32 m0, %3\n\ts_nop 2\n\tglobal_load_lds_dwordx4 %1, %2\n\ts_mov_b32 m0, %0"
                 : "=&s"(sv) : "v"(voff), "s"(sbase), "s"(lds_dst) : "memory");
}
DI void glds16c(const void* sbase, unsigned voff, unsigned lds_dst) {
    unsigned sv;
    asm volatile("s_mov_b32 %0, m0\n\ts_mov_b32 m0, %3\n\ts_nop 2\n\tglobal_load_lds_dwordx4 %1, %2 sc1\n\ts_mov_b32 m0, %0"
                 : "=&s"(sv) : "v"(voff), "s"(sbase), "s"(lds_dst) : "memory");
}
DI void touch8k(const void* sbase, unsigned voff, unsigned lds_dst) {
    unsigned sv;
    asm volatile("s_mov_b32 %0, m0\n\ts_mov_b32 m0, %3\n\ts_nop 2\n\tglobal_load_lds_dword %1, %2\n\ts_mov_b32 m0, %0"
                 : "=&s"(sv) : "v"(voff), "s"(sbase), "s"(lds_dst) : "memory");
}
DI void glds16c_unless(const void* sbase, unsigned voff, unsigned lds_dst, int a, int b) {
    unsigned sv;
    asm volatile("s_cmp_lt_i32 %4, %5\n\ts_cselect_b64 exec, 0, -1\n\ts_mov_b32 %0, m0\n\ts_mov_b32 m0, %3\n\ts_nop 2\n\tglobal_load_lds_dwordx4 %1, %2 sc1\n\ts_mov_b32 m0, %0\n\ts_mov_b64 exec, -1"
                 : "=&s"(sv) : "v"(voff), "s"(sbase), "s"(lds_dst), "s"(a), "s"(b) : "memory", "scc");
}
DI void ldq_unless(bf16x8* q, const void* sbase, unsigned voff, unsigned skip) {
    asm volatile("s_cmp_lg_u32 %10, 0\n\ts_cbranch_scc0 .Lrare%=\n.Lback%=:\n\t.subsection 1\n.Lrare%=:\n\ts_nop 4\n\t"
                 "global_load_dwordx4 %0, %8, %9 offset:-4096\n\tglobal_load_dwordx4 %1, %8, %9 offset:-3072\n\tglobal_load_dwordx4 %2, %8, %9 offset:-2048\n\tglobal_load_dwordx4 %3, %8, %9 offset:-1024\n\t"
                 "global_load_dwordx4 %4, %8, %9\n\tglobal_load_dwordx4 %5, %8, %9 offset:1024\n\tglobal_load_dwordx4 %6, %8, %9 offset:2048\n\tglobal_load_dwordx4 %7, %8, %9 offset:3072\n\ts_branch .Lback%=\n\t.subsection 0"
                 : "+v"(q[0]), "+v"(q[1]), "+v"(q[2]), "+v"(q[3]), "+v"(q[4]), "+v"(q[5]), "+v"(q[6]), "+v"(q[7]) : "v"(voff), "s"((const char*)sbase + 4096), "s"(skip) : "memory", "scc");
}
DI void glds4c(const void* sbase, unsigned lds_dst) {
    unsigned sv, z;
    asm volatile("v_mov_b32 %1, 0\n\ts_mov_b32 %0, m0\n\ts_mov_b32 m0, %3\n\ts_nop 2\n\tglobal_load_lds_dword %1, %2 sc1\n\ts_mov_b32 m0, %0"
                 : "=&s"(sv), "=&v"(z) : "s"(sbase), "s"(lds_dst) : "memory");
}
#define GLDS_IMM(sbase, voff, lds_dst, IMM) asm volatile("s_mov_b32 m0, %2\n\ts_nop 3\n\tglobal_load_lds_dwordx4 %0, %1 offset:%3 sc1" :: "v"(voff), "s"(sbase), "s"(lds_dst), "i"(IMM) : "memory")
#define GLDS_IMM_UNLESS(sbase, voff, lds_dst, IMM, a, b) asm volatile("s_cmp_lt_i32 %4, %5\n\ts_cselect_b64 exec, 0, -1\n\ts_mov_b32 m0, %2\n\ts_nop 1\n\tglobal_load_lds_dwordx4 %0, %1 offset:%3 sc1\n\ts_mov_b64 exec, -1" \
        :: "v"(voff), "s"(sbase), "s"(lds_dst), "i"(IMM), "s"(a), "s"(b) : "memory", "scc")
typedef short v4i16_t __attribute__((ext_vector_type(4)));
DI s16x4 trrd(lds_cptr p) { return __builtin_bit_cast(s16x4, __builtin_amdgcn_ds_read_tr16_b64_v4i16((LAS v4i16_t*)p)); }
DI bf16x8 cat8(s16x4 a, s16x4 b) { return __builtin_shufflevector(a, b, 0, 1, 2, 3, 4, 5, 6, 7); }
DI bf16x8 ldsr128(lds_cptr p) { return *(const LAS bf16x8*)p; }
DI bf16x8 pack8(float a0, float a1, float a2, float a3, float a4, float a5, float a6, float a7) {
    u32x4 w = {cvtpk(a0, a1), cvtpk(a2, a3), cvtpk(a4, a5), cvtpk(a6, a7)}; return __builtin_bit_cast(bf16x8, w); }
#define PACK8(X, B) pack8((X)[(B)], (X)[(B) + 1], (X)[(B) + 2], (X)[(B) + 3], (X)[(B) + 4], (X)[(B) + 5], (X)[(B) + 6], (X)[(B) + 7])
DI float swap32_max(float v) { auto rr = __builtin_amdgcn_permlane32_swap(__float_as_uint(v), __float_as_uint(v), false, false); return fmaxf(__uint_as_float(rr[0]), __uint_as_float(rr[1])); }
DI float swap32_add(float v) { auto rr = __builtin_amdgcn_permlane32_swap(__float_as_uint(v), __float_as_uint(v), false, false); return __uint_as_float(rr[0]) + __uint_as_float(rr[1]); }
DI float lo32_bcast(float v) { auto rr = __builtin_amdgcn_permlane32_swap(__float_as_uint(v), __float_as_uint(v), false, false); return __uint_as_float(rr[0]); }
template <int CTRL> DI float dpp(float v) { return __uint_as_float(__builtin_amdgcn_update_dpp(0u, __float_as_uint(v), CTRL, 0xF, 0xF, true)); }
DI float row16_max(float v) { v = fmaxf(v, dpp<0xB1>(v)); v = fmaxf(v, dpp<0x4E>(v)); v = fmaxf(v, dpp<0x141>(v)); v = fmaxf(v, dpp<0x140>(v)); return v; }
DI float row16_add(float v) { v += dpp<0xB1>(v); v += dpp<0x4E>(v); v += dpp<0x141>(v); v += dpp<0x140>(v); return v; }

constexpr float QSCALE = 0.08838834764831845f * 1.4426950408889634f;
constexpr float THR = 8.f;
constexpr int L_V = 98304;
constexpr int L_WE = 65536, L_GATE = 139264;
#define PIN(x) asm volatile("" : "+v"(x))
#define MX3(a, b, c) __builtin_fmaxf(__builtin_fmaxf((a), (b)), (c))
#define EX2(v) __builtin_amdgcn_exp2f(v)
#define RLX_AGENT __ATOMIC_RELAXED, __HIP_MEMORY_SCOPE_AGENT
#define ST_PL(base, voff, imm, v) asm volatile("s_nop 4\n\tglobal_store_dwordx4 %0, %1, %2 offset:%3\n\ts_nop 1" :: "v"(voff), "v"(v), "s"(base), "i"(imm) : "memory")
#define ST_SC1(base, voff, imm, v) asm volatile("s_nop 4\n\tglobal_store_dwordx4 %0, %1, %2 offset:%3 sc1\n\ts_nop 1" :: "v"(voff), "v"(v), "s"(base), "i"(imm) : "memory")
DI int fresh_lane() { int l; asm volatile("v_mbcnt_lo_u32_b32 %0, -1, 0\n\tv_mbcnt_hi_u32_b32 %0, -1, %0" : "=v"(l)); return l; }
DI void wait_cnt(const unsigned* p, unsigned want) {
    unsigned spins = 0;
    while (rfl(__hip_atomic_load(p, RLX_AGENT)) < want) { __builtin_amdgcn_s_sleep(2); if (++spins > (1u << 20)) break; }
}
DI void handshake_finish(char* ws, int bid, unsigned token, bool ok, int lane) {
    unsigned* CNT = (unsigned*)(ws + WS_CNT); unsigned spins = 0;
    if (bid == 0) {
        for (;;) {
            const unsigned a = __hip_atomic_load(CNT + (80 + (lane & 15)) * 64, RLX_AGENT);
            if (__all(a >= ((lane & 15) ? 16u : 15u))) break;
#pragma unroll
            for (int i = 0; i < 4; ++i) { const unsigned q = __hip_atomic_load((const unsigned*)(ws + WS_HQ) + i * 64 + lane, RLX_AGENT); __hip_atomic_store((unsigned*)(ws + WS_HR) + i * 64 + lane, q, RLX_AGENT); }
            __builtin_amdgcn_s_sleep(8); if (++spins > (1u << 18)) break; }
    } else if (!ok) {
        while (rfl(__hip_atomic_load((const unsigned*)(ws + WS_HR) + bid, RLX_AGENT)) != token) { __builtin_amdgcn_s_sleep(8); if (++spins > (1u << 20)) break; }
        if (lane == 0) __hip_atomic_fetch_add(CNT + (80 + (bid & 15)) * 64, 1u, RLX_AGENT);
    }
}
DI void dual_softmax(f32x16& p) {
#pragma unroll
    for (int hf = 0; hf < 2; ++hf) {
        float mx = -INFINITY;
#pragma unroll
        for (int r = 0; r < 8; ++r) { p[8 * hf + r] = bfround(p[8 * hf + r]); mx = fmaxf(mx, p[8 * hf + r]); }
        mx = swap32_max(mx); float s = 0.f;
#pragma unroll
        for (int r = 0; r < 8; ++r) { p[8 * hf + r] = EX2((p[8 * hf + r] - mx) * 1.4426950408889634f); s += p[8 * hf + r]; }
        s = __builtin_amdgcn_rcpf(swap32_add(s));
#pragma unroll
        for (int r = 0; r < 8; ++r) p[8 * hf + r] *= s;
    }
}
DI void mask_half(f32x16& sc, int dqw) {
    if (dqw < 0) {
#pragma unroll
        for (int r = 0; r < 16; ++r) sc[r] = -INFINITY;
    } else if (dqw == 0) {
        const int ln = fresh_lane(); const int lr = (ln & 31) - 4 * (ln >> 5); const float ninf = -INFINITY;
#pragma unroll
        for (int r = 0; r < 16; ++r)
            asm volatile("s_nop 0\n\tv_cmp_gt_i32 vcc, %c3, %2\n\tv_cndmask_b32 %0, %0, %1, vcc" : "+v"(sc[r]) : "v"(ninf), "v"(lr), "i"(16 * ((r >> 2) & 1) + 8 * (r >> 3) + (r & 3)) : "vcc");
    }
}
DI void mask_tile(f32x16& s0, f32x16& s1, int dqw) {
    asm volatile("s_nop 7\n\ts_nop 7" ::: "memory");
    mask_half(s0, dqw); mask_half(s1, dqw - 32); }

__global__ __launch_bounds__(512, 2) void kf_all(const u16* __restrict__ x, const u16* __restrict__ ckv, const u16* __restrict__ wq, const u16* __restrict__ wuk,
                                                 const u16* __restrict__ wuv, const u16* __restrict__ wo_a, const u16* __restrict__ wqkv,
                                                 const u16* __restrict__ wphi, const u16* __restrict__ wo_b, const u16* __restrict__ gatew,
                                                 char* ws, u16* __restrict__ out) {
    extern __shared__ __attribute__((aligned(16))) char shm[];
    const lds_ptr lds = (lds_ptr)shm; const unsigned lds0 = (unsigned)(uintptr_t)shm;
    const int wid = rfl(threadIdx.x >> 6), G = wid >> 2, w4 = wid & 3;
    const int bid = blockIdx.x, hh_ = 4 * (bid & 7) + (bid >> 6), jj = (bid >> 3) & 7;
#define LANE_VARS() int tid = threadIdx.x; asm volatile("" : "+v"(tid)); const int lane = tid & 63, r32 = lane & 31, hi = lane >> 5, blk = (lane >> 4) & 1, \
        q4 = (lane & 15) >> 2, p4 = lane & 3; const unsigned vlin = (unsigned)lane * 16u; (void)r32; (void)hi; (void)blk; (void)q4; (void)p4; (void)vlin
    unsigned* const CNT = (unsigned*)(ws + WS_CNT);
    char* const KVh = ws + WS_KV + (size_t)hh_ * 32 * 32768;
    char* const A3 = ws + WS_A3;
    STAMP_DECL(); STAMP_RT(); STAMP();
    u32x4 pkv[16];
    unsigned token = 0u;
    const int bA = 15 - jj, bB = jj;
    const char* gX = (const char*)x + (size_t)((G ? bB : bA) * 128 + 32 * w4) * 1024; const char* gW = (const char*)wq + hh_ * 256; const char* gE = (const char*)wqkv + hh_ * 32;
    const unsigned lx = lds0 + wid * 4096, lw = lds0 + L_WE + wid * 1024;
    unsigned voffX, voffW, voffE;
    { const int lane = threadIdx.x & 63;
        { const int row = lane >> 3, ch = (lane & 7) ^ ((row >> 1) & 3); voffX = (unsigned)(row * 1024 + ch * 16); }
        { const int row = 4 * wid + (lane >> 4), j = lane & 15, c = (j >> 2) ^ (row & 3); voffW = (unsigned)(row * 8192 + c * 64 + (j & 3) * 16); }
        { const int row = 8 * wid + (lane >> 3), sl = lane & 7, hf = (sl >> 2) ^ ((row >> 1) & 1), part = hf ? 2 : ((sl >> 1) & 1);
          voffE = (unsigned)(row * 3072 + part * 1024 + (sl & 1) * 16); }
        if (wid == 0) glds16(gatew, (unsigned)lane * 16u, lds0 + L_GATE);
        if (bid == 0 && wid == 1) {
            const unsigned z = 0u;
            asm volatile("global_store_dword %0, %1, %2 sc1\n\tglobal_store_dword %0, %1, %3 sc1\n\ts_nop 1" :: "v"((unsigned)lane * 256u), "v"(z), "s"(ws + WS_CNT), "s"(ws + WS_CNT + 16384) : "memory");
            asm volatile("global_store_dword %0, %1, %2 offset:4 sc1\n\ts_nop 1" :: "v"((unsigned)lane * 256u), "v"(z), "s"(ws + WS_CNT + 16384) : "memory"); }
        PIN(voffX); PIN(voffW); PIN(voffE);
    }
#define XISS(c) do { _Pragma("unroll") for (int e = 0; e < 4; ++e) glds16(gX + e * 8192 + (c) * 128, voffX ^ ((e & 1) << 6), lx + ((c) & 1) * 32768 + e * 1024); } while (0)
#define WISS(c) do { glds16(gW + (size_t)(c) * 524288, voffW, lw + ((c) % 3) * 24576); glds16(gW + (size_t)(c) * 524288 + 262144, voffW, lw + ((c) % 3) * 24576 + 8192); \
                     glds16(gE + (size_t)(c) * 196608, voffE, lw + ((c) % 3) * 24576 + 16384); } while (0)
    {
        LANE_VARS();
        const int wr = wid >> 2, wc = wid & 3, fr = lane & 15, fq = lane >> 4;
        constexpr int HTB = 16384;
#define SA(b, h) (((b) * 2 + (h)) * HTB)
#define SB(b, h) ((4 + (b) * 2 + (h)) * HTB)
        unsigned voffA, voffB;
        { const int row = 8 * wid + (lane >> 3), ch = (lane & 7) ^ ((row >> 1) & 7); voffA = (unsigned)(row * 1024 + ch * 16); }
        { const int row = 4 * wid + (lane >> 4), j = lane & 15, s = ((row >> 3) & 1) * 4 + (row & 3), c = (j >> 1) ^ s; voffB = (unsigned)(row * 8192 + c * 32 + (j & 1) * 16); }
        const char* gA = (const char*)ckv + (size_t)jj * 256 * 1024;
        const char* gB0 = (const char*)wuk + hh_ * 256; const char* gB1 = (const char*)wuv + hh_ * 256;
        const unsigned ldsw = rfl(lds0 + wid * 1024);
#define STAGE_A(b, h, kt) do { const char* g_ = gA + (h) * 131072 + (kt) * 128; glds16(g_, voffA, ldsw + SA(b, h)); glds16(g_ + 65536, voffA, ldsw + SA(b, h) + 8192); } while (0)
#define STAGE_B(b, h, kt) do { const char* g_ = ((h) ? gB1 : gB0) + (size_t)(kt) * 524288; glds16(g_, voffB, ldsw + SB(b, h)); glds16(g_ + 262144, voffB, ldsw + SB(b, h) + 8192); } while (0)
        lds_cptr pa[2], pb[2];
#pragma unroll
        for (int ks = 0; ks < 2; ++ks) pa[ks] = lds + (wr * 64 + fr) * 128 + 16 * ((4 * ks + fq) ^ ((fr >> 1) & 7));
        { const int q = fr >> 2, p = fr & 3, s = (fq & 1) * 4 + q;
#pragma unroll
          for (int n = 0; n < 2; ++n) pb[n] = lds + (8 * fq + q) * 256 + 32 * ((2 * wc + n) ^ s) + 8 * p; }
#define LDA(dst, b, h) do { _Pragma("unroll") for (int m = 0; m < 4; ++m) _Pragma("unroll") for (int k = 0; k < 2; ++k) dst[m][k] = ldsr128(pa[k] + SA(b, h) + m * 2048); } while (0)
#define LDB(dst, b, h) do { _Pragma("unroll") for (int n = 0; n < 2; ++n) _Pragma("unroll") for (int k = 0; k < 2; ++k) \
        dst[n][k] = cat8(trrd(pb[n] + SB(b, h) + k * 8192), trrd(pb[n] + SB(b, h) + k * 8192 + 1024)); } while (0)
#define MMA(ai, bj, At, Bt) do { __builtin_amdgcn_s_setprio(1); _Pragma("unroll") for (int m = 0; m < 4; ++m) _Pragma("unroll") for (int n = 0; n < 2; ++n) _Pragma("unroll") for (int k = 0; k < 2; ++k) \
        acc[ai][bj][m][n] = (bj) ? MFMA16(At[m][k], Bt[n][k], acc[ai][bj][m][n]) : MFMA16(Bt[n][k], At[m][k], acc[ai][bj][m][n]); __builtin_amdgcn_s_setprio(0); } while (0)
        const unsigned scr = lds0 + 143360 + wid * 256; const size_t gw = (size_t)(bid * 8 + wid);
        auto warm = [&](int slot) {
            if (slot == 0 && wid == 0) { token = rfl(*(const LAS unsigned*)(lds + 148480)) + 1u; const unsigned z = 0u, tk = token;
                asm volatile("s_nop 4\n\tglobal_store_dword %0, %1, %2 sc1\n\ts_nop 1" :: "v"(z), "v"(tk), "s"(ws + WS_HQ + bid * 4) : "memory"); }
            if (bid == 0 && wid == 1) {
                if (slot > 0) { const u32x4 rq = *(const LAS u32x4*)(lds + 147456 + lane * 16); ST_SC1(ws + WS_HR, (unsigned)lane * 16u, 0, rq); WAIT_L(0); }
                glds16c(ws + WS_HQ, (unsigned)lane * 16u, lds0 + 147456); }
            const unsigned vo = (unsigned)lane * 128u;
            if (slot == 0) { if (lane < 8) touch8k((const char*)x + gw * 1024, vo, scr); }
            else if (slot == 1) { if (lane < 16) touch8k((const char*)wq + gw * 2048, vo, scr); }
            else if (slot == 2) { if (lane < 6) touch8k((const char*)wqkv + gw * 768, vo, scr); }
            if (slot == 3) {
                const int row = lane >> 3, sl = lane & 7, nb = (sl >> 2) ^ ((row >> 1) & 1); const unsigned vw = (unsigned)(row * 1024 + nb * 64 + (sl & 3) * 16);
                const char* g = (const char*)wo_a + (size_t)(8 * hh_ + wid) * 16384 + jj * 128;
                glds16(g, vw, lds0 + 149504 + wid * 1024); glds16(g + 8192, vw, lds0 + 131072 + wid * 1024);
                if (wid < 2) glds16((const char*)wo_b + (size_t)hh_ * 16384 + wid * 8192 + jj * 128, vw, lds0 + 157696 + wid * 1024);
            } else if (slot == 4) {
                char* d = ws + WS_W3 + (size_t)(jj * 288 + 8 * hh_ + wid) * 2048; const unsigned vl = (unsigned)lane * 16u;
                { const u32x4 t0 = *(const LAS u32x4*)(lds + 149504 + wid * 1024 + lane * 16); ST_SC1(d, vl, 0, t0); }
                { const u32x4 t1 = *(const LAS u32x4*)(lds + 131072 + wid * 1024 + lane * 16); ST_SC1(d, vl, 1024, t1); }
                if (wid < 2) { const u32x4 t2 = *(const LAS u32x4*)(lds + 157696 + wid * 1024 + lane * 16); ST_SC1(ws + WS_W3 + (size_t)(jj * 288 + 256 + hh_) * 2048 + wid * 1024, vl, 0, t2); }
                WAIT_L(0);
            }
        };
        f32x4 acc[2][2][4][2] = {};
        bf16x8 At[4][2], B0[2][2], B1[2][2];
        constexpr int nt = 8;
        STAGE_B(0, 0, 0); STAGE_A(0, 0, 0); STAGE_B(0, 1, 0); STAGE_A(0, 1, 0);
        if (wid == 0) glds4c(ws + WS_HR + bid * 4, lds0 + 148480);
        if (wr == 1) BAR();
        if (wid == 0) { WAIT_V(5); } else { WAIT_V(4); } BAR();
        STAGE_B(1, 0, 1); STAGE_A(1, 0, 1); STAGE_B(1, 1, 1);
        if (wid == 0) { WAIT_V(7); } else { WAIT_V(6); } BAR();
        { const unsigned vo = (unsigned)lane * 128u;
          if (lane < 16) { touch8k((const char*)wuk + gw * 2048, vo, scr); touch8k((const char*)wuv + gw * 2048, vo, scr); }
          if (lane < 8) touch8k((const char*)ckv + gw * 1024, vo, scr); }
        STAMP();
        for (int t = 0; t < nt - 2; t += 2) {
            LDB(B0, 0, 0); SBAR(); LDA(At, 0, 0); STAGE_A(1, 1, t + 1);
            WAIT_L(8); BAR(); WAIT_L(0); MMA(0, 0, At, B0); BAR(); SBAR();
            LDB(B1, 0, 1); STAGE_B(0, 0, t + 2);
            BAR(); WAIT_L(0); MMA(0, 1, At, B1); BAR();
            LDA(At, 0, 1); STAGE_A(0, 0, t + 2);
            BAR(); WAIT_L(0); MMA(1, 0, At, B0); BAR(); SBAR();
            STAGE_B(0, 1, t + 2);
            WAIT_V(6); BAR(); MMA(1, 1, At, B1); BAR();
            warm(t);
            LDB(B0, 1, 0); SBAR(); LDA(At, 1, 0); STAGE_A(0, 1, t + 2);
            WAIT_L(8); BAR(); WAIT_L(0); MMA(0, 0, At, B0); BAR(); SBAR();
            LDB(B1, 1, 1); STAGE_B(1, 0, t + 3);
            BAR(); WAIT_L(0); MMA(0, 1, At, B1); BAR();
            LDA(At, 1, 1); STAGE_A(1, 0, t + 3);
            BAR(); WAIT_L(0); MMA(1, 0, At, B0); BAR(); SBAR();
            STAGE_B(1, 1, t + 3);
            WAIT_V(6); BAR(); MMA(1, 1, At, B1); BAR();
            warm(t + 1);
        }
        { LDB(B0, 0, 0); WAIT_V(0); LDA(At, 0, 0); STAGE_A(1, 1, nt - 1);
          BAR(); WAIT_L(0); MMA(0, 0, At, B0); BAR();
          LDB(B1, 0, 1); BAR(); WAIT_L(0); MMA(0, 1, At, B1); BAR();
          LDA(At, 0, 1); BAR(); WAIT_L(0); MMA(1, 0, At, B0); MMA(1, 1, At, B1); BAR(); }
        { LDB(B0, 1, 0); LDA(At, 1, 0); BAR(); WAIT_L(0); MMA(0, 0, At, B0); BAR();
          LDB(B1, 1, 1); WAIT_V(0);
          WISS(0); XISS(0);
          BAR(); WAIT_L(0); MMA(0, 1, At, B1); BAR();
          LDA(At, 1, 1); BAR(); WAIT_L(0); MMA(1, 0, At, B0); MMA(1, 1, At, B1); BAR(); }
        if (wr == 0) BAR();
        WISS(1); XISS(1);
        if (wid == 0) glds4c(ws + WS_HR + bid * 4, lds0 + 148480);
        SBAR();
#undef SA
#undef SB
#undef STAGE_A
#undef STAGE_B
#undef LDA
#undef LDB
#undef MMA
#pragma unroll
        for (int ai = 0; ai < 2; ++ai) {
#pragma unroll
            for (int m = 0; m < 4; ++m) { const f32x4 a = acc[ai][0][m][0], b = acc[ai][0][m][1];
                pkv[ai * 4 + m] = (u32x4){cvtpk(a[0], a[1]), cvtpk(a[2], a[3]), cvtpk(b[0], b[1]), cvtpk(b[2], b[3])}; }
#pragma unroll
            for (int u = 0; u < 2; ++u)
#pragma unroll
                for (int n = 0; n < 2; ++n) { const f32x4 a = acc[ai][1][2 * u][n], b = acc[ai][1][2 * u + 1][n];
                    pkv[8 + ai * 4 + u * 2 + n] = (u32x4){cvtpk(a[0], a[1]), cvtpk(a[2], a[3]), cvtpk(b[0], b[1]), cvtpk(b[2], b[3])}; }
        }
    }
    STAMP();
    const char* kvw = KVh + w4 * 4096;
    const unsigned ldw = lds0 + w4 * 4096;
    char* const qbp = ws + WS_QB + (size_t)(bid * 4 + w4) * 8192;
    char* const pqp = ws + WS_PQ + (size_t)(bid * 8 + wid) * 2048;
    bf16x8 qf[8]; float gate;
    {
        LANE_VARS();
        u16 wph[8];
#pragma unroll
        for (int j = 0; j < 8; ++j) wph[j] = wphi[hh_ * 256 + (8 * (j >> 2) + 4 * hi + (j & 3)) * 16 + (lane & 15)];
        const char* tb0 = KVh + (size_t)(4 * jj + G) * 32768;
        const unsigned vok = (unsigned)((4 * w4 + (lane >> 4)) * 1024 + (lane & 15) * 16), vov = (unsigned)(16384 + (lane >> 4) * 2048 + (w4 * 32 + (lane & 15)) * 16);
#define KVST(i) do { if ((i) < 8) ST_SC1(tb0 + ((i) >> 2) * 65536, vok, ((i) & 3) * 256, pkv[i]); \
                     else ST_SC1(tb0 + (((i) >> 2) & 1) * 65536 + (((i) >> 1) & 1) * 8192, vov, ((i) & 1) * 256, pkv[i]); } while (0)
        f32x16 qa[5] = {}, d1 = {};
        unsigned xo[2], wo_[4];
#pragma unroll
        for (int s = 0; s < 2; ++s) xo[s] = (unsigned)((32 * wid + r32) * 128 + 16 * ((2 * s + hi) ^ ((r32 >> 1) & 7)));
#pragma unroll
        for (int c = 0; c < 4; ++c) wo_[c] = (unsigned)(L_WE + (8 * hi + q4) * 256 + 64 * (c ^ q4) + 32 * (p4 >> 1) + 16 * blk + 8 * (p4 & 1));
        const unsigned eo = (unsigned)(L_WE + 16384 + (8 * hi + q4) * 128 + 64 * ((q4 >> 1) & 1) + 32 * blk + 8 * p4);
        const unsigned vo_ = (unsigned)(L_WE + 16384 + (8 * hi + q4) * 128 + 64 * (((q4 >> 1) & 1) ^ 1) + 8 * p4);
        bf16x8 fx[2], fw[2][6], fg[2];
#define QR_(buf, c, ks, u) do { \
        if ((u) == 0) { fx[buf] = ldsr128(lds + ((c) & 1) * 32768 + (xo[(ks) & 1] ^ (((ks) >> 1) << 6))); fg[buf] = ldsr128(lds + L_GATE + ((c) * 64 + (ks) * 16 + 8 * hi) * 2); } \
        else if ((u) < 5) { fw[buf][(u) - 1] = cat8(trrd(lds + wo_[(u) - 1] + ((c) % 3) * 24576 + (ks) * 4096), trrd(lds + wo_[(u) - 1] + ((c) % 3) * 24576 + (ks) * 4096 + 1024)); } \
        else { fw[buf][4] = cat8(trrd(lds + eo + ((c) % 3) * 24576 + (ks) * 2048), trrd(lds + eo + ((c) % 3) * 24576 + (ks) * 2048 + 512)); \
               fw[buf][5] = cat8(trrd(lds + vo_ + ((c) % 3) * 24576 + (ks) * 2048), trrd(lds + vo_ + ((c) % 3) * 24576 + (ks) * 2048 + 512)); } } while (0)
#define QM_(buf, u) do { if ((u) < 5) qa[u] = MFMA32(fw[buf][u], fx[buf], qa[u]); else d1 = MFMA32(fx[buf], blk ? fg[buf] : fw[buf][5], d1); } while (0)
#define QRD(buf, c, ks) do { _Pragma("unroll") for (int u_ = 0; u_ < 6; ++u_) QR_(buf, c, ks, u_); } while (0)
#define QSTEP(buf, c, ks, rd) do { _Pragma("unroll") for (int u_ = 0; u_ < 6; ++u_) { if (rd) QR_((buf) ^ 1, c, ks, u_); QM_(buf, u_); SBAR(); } } while (0)
#define WV_(n) asm volatile("s_waitcnt vmcnt(%0)" :: "i"(n) : "memory")
#define NST(c) ((c) < 0 ? 0 : 2)
#define ST0(c) (2 * (c))
#define QCHUNK(c) do { \
        SBAR(); QSTEP(0, c, 1, true); QSTEP(1, c, 2, true); \
        if ((c) < 7) { WV_(4 + NST((c) - 1)); } BAR(); asm volatile("" ::: "memory"); \
        if ((c) < 6) WISS((c) + 2); \
        _Pragma("unroll") for (int i_ = 0; i_ < NST(c); ++i_) KVST(ST0(c) + i_); \
        SBAR(); QSTEP(0, c, 3, true); \
        if ((c) < 7) { WV_(((c) < 6 ? 3 : 0) + NST(c)); } \
        SBAR(); QSTEP(1, (c) + 1, 0, (c) < 7); \
        if ((c) < 6) XISS((c) + 2); } while (0)
        if (wid == 0) { WAIT_V(8); } else { WAIT_V(7); } BAR(); asm volatile("" ::: "memory");
        bool hs_ok = true;
        bf16x8 wpf;
#pragma unroll
        for (int j = 0; j < 8; ++j) wpf[j] = (short)(blk ? (wph[j] ^ 0x8000) : wph[j]);
        PIN(wpf);
        STAMP();
        QRD(0, 0, 0);
        QCHUNK(0);
        {
            unsigned t0, t1, seen, em;
            asm volatile("v_mov_b32 %0, %5\n\tds_read_b32 %0, %0\n\tv_mov_b32 %1, 1\n\ts_waitcnt lgkmcnt(0)\n\tv_readfirstlane_b32 %2, %0\n\tv_mov_b32 %0, 0\n\ts_nop 3\n\t"
                         "s_min_u32 %3, %8, 1\n\ts_cmp_eq_u32 %7, 0\n\ts_cselect_b32 %3, %3, 0\n\t"
                         "s_cmp_eq_u32 %2, %6\n\ts_cselect_b32 %2, 1, 0\n\ts_and_b32 %3, %3, %2\n\ts_mov_b32 exec_lo, %3\n\ts_mov_b32 exec_hi, 0\n\t"
                         "global_atomic_add %0, %1, %4\n\ts_mov_b64 exec, -1"
                         : "=&v"(t0), "=&v"(t1), "=&s"(seen), "=&s"(em) : "s"(CNT + (80 + (bid & 15)) * 64), "s"(lds0 + 148480), "s"(token), "s"(wid), "s"(bid) : "memory", "scc");
            hs_ok = seen != 0u; }
        QCHUNK(1); QCHUNK(2); QCHUNK(3); QCHUNK(4); QCHUNK(5); QCHUNK(6); QCHUNK(7);
#undef QRD
#undef QR_
#undef QM_
#undef QSTEP
#undef QCHUNK
#undef XISS
#undef WISS
#undef KVST
#undef NST
#undef ST0
        STAMP();
#pragma unroll
        for (int cc = 0; cc < 4; ++cc) {
#pragma unroll
            for (int r = 0; r < 16; ++r) qa[cc][r] *= QSCALE;
            qf[2 * cc] = PACK8(qa[cc], 0); qf[2 * cc + 1] = PACK8(qa[cc], 8);
        }
        f32x16 pq = MFMA32(wpf, PACK8(qa[4], 0), f32x16{});
        f32x16 pk = MFMA32(wpf, PACK8(qa[4], 8), f32x16{});
        dual_softmax(pq); dual_softmax(pk);
        f32x16 d3;
        { bf16x8 id0, id1;
#pragma unroll
          for (int j = 0; j < 8; ++j) { const int f = 8 * (j >> 2) + 4 * hi + (j & 3); id0[j] = (short)(r32 == f ? 0x3F80 : 0); id1[j] = (short)(r32 == f + 16 ? 0x3F80 : 0); }
          d3 = MFMA32(PACK8(pk, 0), id0, f32x16{}); d3 = MFMA32(PACK8(pk, 8), id1, d3); }
        const bf16x8 ones = {0x3F80, 0x3F80, 0x3F80, 0x3F80, 0x3F80, 0x3F80, 0x3F80, 0x3F80};
        const bf16x8 v0 = PACK8(d1, 0), v1 = PACK8(d1, 8);
        f32x16 d4 = MFMA32(PACK8(d3, 0), blk ? ones : v0, f32x16{});
        d4 = MFMA32(PACK8(d3, 8), blk ? ones : v1, d4);
        bf16x8 pq0 = PACK8(pq, 0), pq1 = PACK8(pq, 8); PIN(pq0); PIN(pq1); PIN(d4);
        if (wid == 0) handshake_finish(ws, bid, token, hs_ok, lane);
        WAIT_BAR(0);
        if (tid == 0) __hip_atomic_fetch_add(CNT + hh_ * 64, 1u, RLX_AGENT);
        STAMP();
        LAS float* red = (LAS float*)(lds + L_V + G * 32768 + w4 * 4096);
        if (r32 == 16) {
#pragma unroll
            for (int r = 0; r < 16; ++r) red[(r & 3) + 8 * (r >> 2) + 4 * hi] = d1[r];
        }
        WAIT_L(0);
        { const float gz = bfround(red[r32]); gate = bfround(1.f / (1.f + __expf(-gz))); }
        PIN(gate);
#pragma unroll
        for (int r = 0; r < 16; ++r) red[((r & 3) + 8 * (r >> 2) + 4 * hi) * 32 + r32] = d4[r];
        ST_PL(pqp, vlin, 0, pq0); ST_PL(pqp, vlin, 1024, pq1);
        if (G == 0) {
#pragma unroll
            for (int s = 0; s < 8; ++s) ST_PL(qbp + (s >> 2) * 4096, vlin, (s & 3) * 1024, qf[s]);
        }
        WAIT_L(0); BAR(); asm volatile("" ::: "memory");
        if (tid < 256) {
            f32x4 s = {0.f, 0.f, 0.f, 0.f};
#pragma unroll
            for (int w = 0; w < 8; ++w) s += *(const LAS f32x4*)(lds + L_V + (w >> 2) * 32768 + (w & 3) * 4096 + tid * 16);
            ST_SC1(ws + WS_KVP + (size_t)(hh_ * 8 + jj) * 4096, (unsigned)tid * 16u, 0, s);
        }
        wait_cnt(CNT + hh_ * 64, 8u);
        asm volatile("" ::: "memory");
#pragma unroll
        for (int e = 0; e < 4; ++e) glds16c(kvw + (size_t)(e >> 1) * 32768 + (2 * G + (e & 1)) * 1024, vlin, ldw + (e >> 1) * 16384 + (2 * G + (e & 1)) * 1024);
        WAIT_BAR(0);
    }
    STAMP();
    {
    LANE_VARS();
    const int prow = (r32 & 7) | ((r32 & 8) << 1) | ((r32 & 16) >> 1);
    const unsigned kro = (unsigned)(prow * 16 + hi * 1024), vro = (unsigned)(L_V + r32 * 16 + hi * 2048);
    const int dq0 = 32 * w4;
    const int T = 2 * jj + 2;
    unsigned ks0 = 0u, ks1 = 16384u, ks2 = 32768u;
    int rho = 0; const int TG = G ? T : 99;
    f32x16 o[4], negm, sA0, sA1, sB0, sB1; bf16x8 kf[16], vf[16]; u32x4 pw[4]; float m, l;
#define KRD(i, kp) kf[i] = ldsr128((kp) + ((i) >> 1) * 2048 + ((i) & 1) * 512)
#define VRD(i, vp) vf[i] = ldsr128((vp) + ((i) >> 2) * 4096 + ((i) & 3) * 512)
#define PF(k) __builtin_bit_cast(bf16x8, pw[k])
#define DMA1(i) do { if ((i) < 2) GLDS_IMM(gk_, vlin, dk_, ((i) & 3) * 1024); else if ((i) < 4) GLDS_IMM_UNLESS(gk_, vlin, dk_, ((i) & 3) * 1024, rk_, T); \
                     else if ((i) < 6) GLDS_IMM(gv_, vlin, dv_, ((i) & 3) * 1024); else GLDS_IMM_UNLESS(gv_, vlin, dv_, ((i) & 3) * 1024, rho, T); } while (0)
    const char* gk_; const char* gv_; unsigned dk_, dv_, ko_, vo_; int rk_;
    unsigned go1, go2;
#define GOFF(r) ((r) < T ? (unsigned)(r) * 32768u + G * 2048u : (unsigned)(2 * (r) - T + G) * 32768u)
#define PREP_A(r, k0) do { ko_ = ((r) >= TG ? 49152u : 0u) + (k0); vo_ = ((r) - 1 >= TG ? 32768u : 0u) + (((r) - 1) & 1) * 16384u; } while (0)
#define PREP_B(r, gcur) do { gv_ = kvw + ((gcur) + 16384u); dv_ = ldw + L_V + ((r) & 1) * 16384u + ((r) < T ? G * 2048u : G * 32768u); } while (0)
#define PREP_C(r) do { rk_ = (r) + 2 <= 16 ? (r) + 2 : 16; go1 = go2; go2 = GOFF(rk_); } while (0)
#define PREP_D(k2) do { gk_ = kvw + go2; dk_ = ldw + (k2) + (rk_ < T ? G * 2048u : G * 49152u); } while (0)
#define ROT() do { const unsigned t_ = ks0; ks0 = ks1; ks1 = ks2; ks2 = t_; ++rho; } while (0)
#define GAPA(i, C0, C1, P0, P1) do { \
        if ((i) + PD < 16) { KRD((i) + PD, kp_); } else { VRD((i) + PD - 16, vp_); } \
        if (((i) & 1) == 0) DMA1((i) >> 1); \
        if ((i) & 1) C1 = MFMA32(kf[i], qf[(i) >> 1], (i) < 2 ? negm : C1); else C0 = MFMA32(kf[i], qf[(i) >> 1], (i) < 2 ? negm : C0); \
        if ((i) < 8) { P1[2 * (i)] = EX2(P1[2 * (i)]); P1[2 * (i) + 1] = EX2(P1[2 * (i) + 1]); pw[(i) >> 2][(i) & 3] = cvtpk(P0[2 * (i)], P0[2 * (i) + 1]); PIN(P1[2 * (i)]); PIN(P1[2 * (i) + 1]); PIN(pw[(i) >> 2][(i) & 3]); } \
        else { l += P1[2 * (i) - 16]; l2 += P1[2 * (i) - 15]; pw[(i) >> 2][(i) & 3] = cvtpk(P1[2 * (i) - 16], P1[2 * (i) - 15]); PIN(l); PIN(l2); PIN(pw[(i) >> 2][(i) & 3]); } \
        SBAR(); } while (0)
#define PVM(i) o[(i) & 3] = MFMA32(vf[i], PF((i) >> 2), o[(i) & 3])
#define RDB(i) do { if ((i) + PD < 16) { VRD((i) + PD, vp_); } else { KRD((i) + PD - 16, kn_); } } while (0)
#define ROUND(C0, C1, P0, P1) do { \
        const lds_cptr kp_ = lds + ko_ + kro; const lds_cptr vp_ = lds + vo_ + vro; \
        float l2 = 0.f; lds_cptr kn_ = kp_; SBAR(); \
        GAPA(0, C0, C1, P0, P1); GAPA(1, C0, C1, P0, P1); GAPA(2, C0, C1, P0, P1); GAPA(3, C0, C1, P0, P1); \
        GAPA(4, C0, C1, P0, P1); GAPA(5, C0, C1, P0, P1); GAPA(6, C0, C1, P0, P1); GAPA(7, C0, C1, P0, P1); \
        GAPA(8, C0, C1, P0, P1); GAPA(9, C0, C1, P0, P1); GAPA(10, C0, C1, P0, P1); GAPA(11, C0, C1, P0, P1); \
        GAPA(12, C0, C1, P0, P1); GAPA(13, C0, C1, P0, P1); GAPA(14, C0, C1, P0, P1); GAPA(15, C0, C1, P0, P1); \
        l += l2; \
        ldq_unless(qf, qbp, vlin, (unsigned)(rho + 1 - TG));        \
        RDB(0); PVM(0); SBAR(); RDB(1); PVM(1); SBAR(); \
        if (__builtin_expect(rho == 16 || (rho >= TG - 2 && rho < TG), 0)) mask_tile(C0, C1, dq0 - (rho == 16 ? 64 * G : 64 * (rho - TG + 2))); \
        float ma, mb; \
        RDB(2); PVM(2); ma = MX3(C0[0], C0[1], C1[0]); mb = MX3(C0[2], C0[3], C1[1]); ma = MX3(ma, C1[2], C1[3]); mb = MX3(mb, C0[4], C0[5]); PIN(ma); PIN(mb); SBAR(); \
        RDB(3); PVM(3); ma = MX3(ma, C0[6], C0[7]); mb = MX3(mb, C1[4], C1[5]); ma = MX3(ma, C1[6], C1[7]); mb = MX3(mb, C0[8], C0[9]); PIN(ma); PIN(mb); SBAR(); \
        RDB(4); PVM(4); ma = MX3(ma, C0[10], C0[11]); mb = MX3(mb, C1[8], C1[9]); ma = MX3(ma, C1[10], C1[11]); mb = MX3(mb, C0[12], C0[13]); PIN(ma); PIN(mb); SBAR(); \
        RDB(5); PVM(5); ma = MX3(ma, C0[14], C0[15]); mb = MX3(mb, C1[12], C1[13]); ma = MX3(ma, C1[14], C1[15]); \
        const float rm = swap32_max(fmaxf(ma, mb)); bool pend = false; float fsc = 1.f; \
        if (__builtin_expect(__any(rm > THR) || rho == TG, 0)) { float dl; pend = true; \
            if (rho == TG) { dl = fmaxf(rm, -1e30f); m = dl; fsc = gate / swap32_add(l); l = 0.f; }        \
            else { dl = fmaxf(rm, 0.f); m += dl; fsc = EX2(-dl); l *= fsc; } \
            _Pragma("unroll") for (int e = 0; e < 16; ++e) { C0[e] -= dl; C1[e] -= dl; negm[e] = -m; } } \
        SBAR(); \
        RDB(6); PVM(6); C0[0] = EX2(C0[0]); C0[1] = EX2(C0[1]); PIN(C0[0]); PIN(C0[1]); SBAR(); \
        RDB(7); PVM(7); C0[2] = EX2(C0[2]); C0[3] = EX2(C0[3]); l += C0[0]; l2 = C0[1]; PIN(C0[2]); PIN(C0[3]); PIN(l); SBAR(); \
        RDB(8); PVM(8); PREP_A(rho + 1, ks1); kn_ = lds + ko_ + kro; C0[4] = EX2(C0[4]); C0[5] = EX2(C0[5]); l += C0[2]; l2 += C0[3]; PIN(C0[4]); PIN(C0[5]); PIN(l); PIN(l2); SBAR(); \
        RDB(9); PVM(9); PREP_B(rho + 1, go1); C0[6] = EX2(C0[6]); C0[7] = EX2(C0[7]); l += C0[4]; l2 += C0[5]; PIN(C0[6]); PIN(C0[7]); PIN(l); PIN(l2); SBAR(); \
        RDB(10); PVM(10); PREP_C(rho + 1); C0[8] = EX2(C0[8]); C0[9] = EX2(C0[9]); l += C0[6]; l2 += C0[7]; PIN(C0[8]); PIN(C0[9]); PIN(l); PIN(l2); SBAR(); \
        RDB(11); PVM(11); PREP_D(ks0); C0[10] = EX2(C0[10]); C0[11] = EX2(C0[11]); l += C0[8]; l2 += C0[9]; PIN(C0[10]); PIN(C0[11]); PIN(l); PIN(l2); SBAR(); \
        RDB(12); PVM(12); C0[12] = EX2(C0[12]); C0[13] = EX2(C0[13]); l += C0[10]; l2 += C0[11]; PIN(C0[12]); PIN(C0[13]); PIN(l); PIN(l2); SBAR(); \
        RDB(13); PVM(13); C0[14] = EX2(C0[14]); C0[15] = EX2(C0[15]); l += C0[12]; l2 += C0[13]; PIN(C0[14]); PIN(C0[15]); PIN(l); PIN(l2); SBAR(); \
        RDB(14); PVM(14); l += C0[14]; l2 += C0[15]; PIN(l); PIN(l2); SBAR(); \
        RDB(15); PVM(15); l += l2; PIN(l); SBAR(); \
        if (__builtin_expect(pend || rho + 1 == TG, 0)) { \
            if (pend) { _Pragma("unroll") for (int c = 0; c < 4; ++c) _Pragma("unroll") for (int e = 0; e < 16; ++e) o[c][e] *= fsc; } \
            if (rho + 1 == TG) negm = f32x16{}; \
            if (rho == TG) { const int ln = fresh_lane(); const unsigned vod = (unsigned)((ln >> 5) * 2048 + (32 * w4 + (ln & 31)) * 16); const char* dst = A3 + (size_t)(bB * 576 + hh_ * 16) * 2048; \
                _Pragma("unroll") for (int c = 0; c < 4; ++c) { const bf16x8 t0 = PACK8(o[c], 0), t1 = PACK8(o[c], 8); ST_SC1(dst + c * 8192, vod, 0, t0); ST_SC1(dst + c * 8192 + 4096, vod, 0, t1); o[c] = f32x16{}; } } } \
        asm volatile("s_cmp_eq_u32 %0, %1\n\ts_cbranch_scc1 .Lw%=\n\ts_waitcnt vmcnt(0)\n.Lw%=:\n\ts_waitcnt vmcnt(8)\n\ts_barrier" :: "s"(rho), "s"(TG) : "memory", "scc");     \
        if (__builtin_expect(rho == TG + 1 && w4 == 0, 0)) { if (fresh_lane() == 0) __hip_atomic_fetch_or(CNT + (64 + bB) * 64, 1u << hh_, RLX_AGENT); }        \
        ROT(); } while (0)

#define ROUND0(C0, C1) do { \
        const lds_cptr kp_ = lds + ks0 + kro; const lds_cptr kn_ = lds + ks1 + kro; \
        go2 = GOFF(1); PREP_B(0, GOFF(0)); PREP_C(0); PREP_D(ks2); \
        _Pragma("unroll") for (int i = 0; i < 6; ++i) KRD(i, kp_); \
        _Pragma("unroll") for (int i = 0; i < 8; ++i) DMA1(i); \
        _Pragma("unroll") for (int c = 0; c < 4; ++c) o[c] = f32x16{}; \
        PREP_A(1, ks1); PREP_B(1, go1); PREP_C(1); PREP_D(ks0); SBAR(); \
        _Pragma("unroll") for (int i = 0; i < 16; ++i) { \
            if (i + 6 < 16) KRD(i + 6, kp_); \
            if (i & 1) C1 = MFMA32(kf[i], qf[i >> 1], i < 2 ? f32x16{} : C1); else C0 = MFMA32(kf[i], qf[i >> 1], i < 2 ? f32x16{} : C0); \
            SBAR(); } \
        if (TG == 2) mask_tile(C0, C1, dq0); \
        float ma = MX3(C0[0], C0[1], C1[0]), mb = MX3(C0[2], C0[3], C1[1]); ma = MX3(ma, C1[2], C1[3]); \
        _Pragma("unroll") for (int r = 4; r < 16; r += 4) { ma = MX3(ma, C0[r], C0[r + 1]); mb = MX3(mb, C0[r + 2], C0[r + 3]); ma = MX3(ma, C1[r], C1[r + 1]); mb = MX3(mb, C1[r + 2], C1[r + 3]); } \
        m = fmaxf(swap32_max(fmaxf(ma, mb)), -1e30f); l = 0.f; \
        _Pragma("unroll") for (int i = 0; i < PD; ++i) KRD(i, kn_); \
        _Pragma("unroll") for (int e = 0; e < 16; ++e) { C0[e] = EX2(C0[e] - m); l += C0[e]; C1[e] -= m; negm[e] = -m; } \
        asm volatile("s_waitcnt vmcnt(0)\n\ts_barrier" ::: "memory"); ROT(); } while (0)
#define DRAIN(P0, P1, MID) do { \
        const lds_cptr vp_ = lds + G * 32768 + vro; \
        _Pragma("unroll") for (int i = 0; i < 8; ++i) VRD(i, vp_); \
        _Pragma("unroll") for (int i = 0; i < 8; ++i) pw[i >> 2][i & 3] = cvtpk(P0[2 * i], P0[2 * i + 1]); \
        SBAR(); \
        _Pragma("unroll") for (int i = 0; i < 16; ++i) { \
            if (i + 8 < 16) VRD(i + 8, vp_); \
            if (i == 8) { MID } \
            PVM(i); \
            if (i < 8) { P1[2 * i] = EX2(P1[2 * i]); P1[2 * i + 1] = EX2(P1[2 * i + 1]); PIN(P1); } \
            if (i >= 1 && i < 9) { l += P1[2 * i - 2]; l += P1[2 * i - 1]; pw[2 + ((i - 1) >> 2)][(i - 1) & 3] = cvtpk(P1[2 * i - 2], P1[2 * i - 1]); PIN(l); PIN(pw[2 + ((i - 1) >> 2)]); } \
            SBAR(); } \
        l = swap32_add(l); } while (0)
    ROUND0(sA0, sA1);
    if (wid == 0 && fresh_lane() == 0) __hip_atomic_fetch_add(CNT + (32 + hh_) * 64, 1u, RLX_AGENT);
    STAMP();
#pragma clang loop unroll(disable)
    for (int pr = 0; pr < 8; ++pr) { ROUND(sB0, sB1, sA0, sA1); ROUND(sA0, sA1, sB0, sB1); }
    STAMP();
    {
        f32x2 ps[8]; bf16x8 phq[2];
        LANE_VARS();
        const unsigned f2 = __hip_atomic_load(CNT + (32 + hh_) * 64, RLX_AGENT);
        DRAIN(sA0, sA1,
            if (rfl(f2) < 8u) wait_cnt(CNT + (32 + hh_) * 64, 8u);
            _Pragma("unroll") for (int w = 0; w < 8; ++w) ps[w] = __builtin_bit_cast(f32x2, __hip_atomic_load((const unsigned long long*)(ws + WS_KVP + (size_t)(hh_ * 8) * 4096 + (unsigned)(w * 4096 + tid * 8)), RLX_AGENT));
            phq[0] = *(const bf16x8*)(pqp + vlin); phq[1] = *(const bf16x8*)(pqp + (vlin + 1024)); );
        const unsigned vod = (unsigned)(hi * 2048 + (32 * w4 + r32) * 16);
        BAR(); asm volatile("" ::: "memory");
        const unsigned st0 = ks2;
        { const lds_ptr xp = lds + L_V + wid * 8192 + lane * 16;
#pragma unroll
          for (int c = 0; c < 2; ++c)
#pragma unroll
              for (int g = 0; g < 4; ++g) { f32x4 t;
#pragma unroll
                  for (int e = 0; e < 4; ++e) t[e] = G ? o[c][4 * g + e] : o[2 + c][4 * g + e];
                  *(LAS f32x4*)(xp + (c * 4 + g) * 1024) = t; }
          *(LAS f32x4*)(lds + st0 + wid * 1024 + lane * 16) = (f32x4){m, l, gate, 0.f};
          f32x2 s = ps[0];
#pragma unroll
          for (int w = 1; w < 8; ++w) s += ps[w];
          *(LAS f32x2*)(lds + st0 + 8192 + tid * 8) = s; }
        WAIT_L(0); BAR(); asm volatile("" ::: "memory");
        { const int ow = wid ^ 4;
          const f32x4 st = *(const LAS f32x4*)(lds + st0 + ow * 1024 + lane * 16);
          const float mn = fmaxf(m, st[0]), f0 = EX2(m - mn), f1 = EX2(st[0] - mn);
          const float gg = G ? st[2] : gate;
          const float inv = gg / (l * f0 + st[1] * f1); const float c0 = f0 * inv, c1 = f1 * inv;
          const lds_cptr xp = lds + L_V + ow * 8192 + lane * 16;
          const char* dst = A3 + (size_t)(bA * 576 + hh_ * 16 + G * 8) * 2048;
#pragma unroll
          for (int c = 0; c < 2; ++c) {
              float v[16];
#pragma unroll
              for (int g = 0; g < 4; ++g) { const f32x4 t = *(const LAS f32x4*)(xp + (c * 4 + g) * 1024);
#pragma unroll
                  for (int e = 0; e < 4; ++e) v[4 * g + e] = (G ? o[2 + c][4 * g + e] : o[c][4 * g + e]) * c0 + t[e] * c1; }
              { const bf16x8 t0 = PACK8(v, 0), t1 = PACK8(v, 8); ST_SC1(dst + c * 8192, vod, 0, t0); ST_SC1(dst + c * 8192 + 4096, vod, 0, t1); }
          }
          float kvv[2][8];
          const int col = r32 < 16 ? r32 : 16;
#pragma unroll
          for (int s = 0; s < 2; ++s)
#pragma unroll
              for (int j = 0; j < 8; ++j) { const int f = 16 * s + 8 * (j >> 2) + 4 * hi + (j & 3); const float v = *(const LAS float*)(lds + st0 + 8192 + (f * 32 + col) * 4); kvv[s][j] = r32 <= 16 ? v : 0.f; }
          f32x16 nm = MFMA32(PACK8(kvv[0], 0), phq[0], f32x16{});
          nm = MFMA32(PACK8(kvv[1], 0), phq[1], nm);
          const float den = bfround(bfround(lo32_bcast(nm[8])) + 1e-6f); const float sc = (1.f - gate) / den;
          float ov[8];
#pragma unroll
          for (int r = 0; r < 8; ++r) ov[r] = bfround(nm[r]) * sc;
          const bf16x8 t0 = PACK8(ov, 0);
          ST_SC1(A3 + (size_t)((G ? bB : bA) * 576 + 512 + 2 * hh_) * 2048, vod, 0, t0);
        }
        WAIT_L(0); BAR(); asm volatile("" ::: "memory");
        STAMP();
    }
#undef KRD
#undef VRD
#undef PF
#undef DMA1
#undef ROT
#undef GOFF
#undef PREP_A
#undef PREP_B
#undef PREP_C
#undef PREP_D
#undef GAPA
#undef PVM
#undef RDB
#undef ROUND
#undef ROUND0
#undef DRAIN
    }
    {
        LANE_VARS();
        const int rt = 4 * (bid & 7) + (bid >> 6), ct = (bid >> 3) & 7;
        const char* gA = A3 + (size_t)((rt >> 1) * 576 + 2 * wid) * 2048 + (rt & 1) * 1024;
        const char* gW3 = ws + WS_W3 + (size_t)(ct * 288 + wid) * 2048;
        const unsigned wb = lds0 + wid * 20480;
        const unsigned* rdy = CNT + (64 + (rt >> 1)) * 64;
        auto issue = [&](int u, unsigned slot) {
            const char* ga = gA + (size_t)u * 32768; const char* gw = gW3 + (size_t)u * 16384;
            GLDS_IMM(ga, vlin, wb + slot, 0); GLDS_IMM(ga + 1024, vlin, wb + slot, 1024); GLDS_IMM(gw, vlin, wb + slot + 2048, 0); GLDS_IMM(gw, vlin, wb + slot + 2048, 1024); };
        unsigned seen, seenB; int hnext = 0;
        auto poll = [&]() { const unsigned long long w = __hip_atomic_load((const unsigned long long*)rdy, RLX_AGENT); seen = rfl((unsigned)w); seenB = rfl((unsigned)(w >> 32)); };
        auto next_head = [&]() -> int {
            unsigned spins = 0;
            while (hnext < 32 ? !((seen >> hnext) & 1u) : seenB != ~0u) { poll(); if (++spins > (1u << 20)) break; if (spins > 1) __builtin_amdgcn_s_sleep(2); }
            asm volatile("" ::: "memory");
            return hnext++; };
        poll();
        asm volatile("s_waitcnt vmcnt(0)\n\ts_barrier" ::: "memory");
        if (tid == 0) { __hip_atomic_fetch_or(CNT + (64 + bA) * 64, 1u << hh_, RLX_AGENT); __hip_atomic_fetch_or(CNT + (64 + bB) * 64, 1u << hh_, RLX_AGENT);
                        __hip_atomic_fetch_or(CNT + (64 + bA) * 64 + 1, 1u << hh_, RLX_AGENT); __hip_atomic_fetch_or(CNT + (64 + bB) * 64 + 1, 1u << hh_, RLX_AGENT); }
        STAMP();
#pragma unroll
        for (int i = 0; i < 5; ++i) issue(next_head(), i * 4096u);
        f32x16 acc[2][2] = {};
        const unsigned ao = (unsigned)(wid * 20480 + hi * 1024 + r32 * 16);
        unsigned bo[2];
#pragma unroll
        for (int nb = 0; nb < 2; ++nb) bo[nb] = (unsigned)(wid * 20480 + 2048 + (4 * hi + q4) * 128 + 64 * (nb ^ ((q4 >> 1) & 1)) + 32 * blk + 8 * p4);
        bf16x8 af[2][2], bf[2][2];
#define K3RD(buf, sb) do { af[buf][0] = ldsr128((sb) + ao); bf[buf][0] = cat8(trrd((sb) + bo[0]), trrd((sb) + bo[0] + 1024)); \
        af[buf][1] = ldsr128((sb) + ao + 512); bf[buf][1] = cat8(trrd((sb) + bo[1]), trrd((sb) + bo[1] + 1024)); } while (0)
#define K3BODY(buf, i) do { \
        const unsigned snx = sc == 16384u ? 0u : sc + 4096u; \
        if ((i) + 1 < 36) K3RD(buf ^ 1, lds + snx); \
        SBAR(); acc[0][0] = MFMA32(bf[buf][0], af[buf][0], acc[0][0]); acc[1][0] = MFMA32(bf[buf][0], af[buf][1], acc[1][0]); SBAR(); \
        if ((i) + 5 < 36) issue(next_head(), sc); \
        SBAR(); acc[0][1] = MFMA32(bf[buf][1], af[buf][0], acc[0][1]); acc[1][1] = MFMA32(bf[buf][1], af[buf][1], acc[1][1]); SBAR(); \
        sc = snx; } while (0)
#define K3W(n) asm volatile("s_waitcnt vmcnt(" #n ") lgkmcnt(0)" ::: "memory")
        unsigned sc = 0u;
        WAIT_V(16);
        K3RD(0, lds);
        for (int i = 0; i < 32; i += 2) {
            if (G) __builtin_amdgcn_s_setprio(1); K3W(12); K3BODY(0, i);
            if (G) __builtin_amdgcn_s_setprio(0); K3W(12); K3BODY(1, i + 1); }
        K3W(8); K3BODY(0, 32); K3W(4); K3BODY(1, 33); K3W(0); K3BODY(0, 34); K3W(0); K3BODY(1, 35);
#undef K3RD
#undef K3BODY
#undef K3W
        STAMP();
        BAR(); asm volatile("" ::: "memory");
#pragma unroll
        for (int mi = 0; mi < 2; ++mi)
#pragma unroll
            for (int nb = 0; nb < 2; ++nb)
#pragma unroll
                for (int g = 0; g < 4; ++g) { const int row = 32 * mi + r32, ch = 8 * nb + 2 * g + hi;
                    *(LAS f32x4*)(lds + wid * 16384 + row * 256 + 16 * (ch ^ (row & 15))) = (f32x4){acc[mi][nb][4 * g], acc[mi][nb][4 * g + 1], acc[mi][nb][4 * g + 2], acc[mi][nb][4 * g + 3]}; }
        WAIT_L(0); BAR(); asm volatile("" ::: "memory");
        { const int row = tid >> 3, c8 = tid & 7; f32x4 s0 = {}, s1 = {};
#pragma unroll
          for (int w = 0; w < 8; ++w) { s0 += *(const LAS f32x4*)(lds + w * 16384 + row * 256 + 16 * ((2 * c8) ^ (row & 15)));
                                        s1 += *(const LAS f32x4*)(lds + w * 16384 + row * 256 + 16 * ((2 * c8 + 1) ^ (row & 15))); }
          u32x4 w = {cvtpk(s0[0], s0[1]), cvtpk(s0[2], s0[3]), cvtpk(s1[0], s1[1]), cvtpk(s1[2], s1[3])};
          *(u32x4*)(out + (size_t)(rt * 64 + row) * 512 + ct * 64 + c8 * 8) = w; }
    }
    STAMP(); STAMP_RT();
    STAMP_OUT(ws + WS_END);
}

extern "C" void kernel_launch(void* const* d_in, const int* in_sizes, int n_in, void* d_out, int out_size, void* d_ws, size_t ws_size,
                              hipStream_t stream) {
    (void)in_sizes; (void)n_in; (void)out_size; (void)ws_size;
    char* ws = (char*)d_ws;
    (void)hipFuncSetAttribute((const void*)kf_all, hipFuncAttributeMaxDynamicSharedMemorySize, LDS_BYTES);
    kf_all<<<256, 512, LDS_BYTES, stream>>>((const u16*)d_in[0], (const u16*)d_in[1], (const u16*)d_in[2], (const u16*)d_in[3], (const u16*)d_in[4], (const u16*)d_in[5],
                                            (const u16*)d_in[7], (const u16*)d_in[8], (const u16*)d_in[9], (const u16*)d_in[10], ws, (u16*)d_out);
}
```

```cpp
#include <hip/hip_runtime.h>
#include <hip/hip_bf16.h>
#include <stdint.h>
#define PD 4
#define CODEWARM 1
#define PREFETCH 1

typedef unsigned short u16;
typedef short bf16x8 __attribute__((ext_vector_type(8)));
typedef short s16x4 __attribute__((ext_vector_type(4)));
typedef float f32x2 __attribute__((ext_vector_type(2)));
typedef float f32x4 __attribute__((ext_vector_type(4)));
typedef float f32x16 __attribute__((ext_vector_type(16)));
typedef unsigned u32x4 __attribute__((ext_vector_type(4)));
typedef unsigned u32x2 __attribute__((ext_vector_type(2)));
typedef __bf16 bf16x2_t __attribute__((ext_vector_type(2)));
#define LAS __attribute__((address_space(3)))
#define DI __device__ __forceinline__
typedef LAS const char* lds_cptr;
typedef LAS char* lds_ptr;

constexpr int SEQ = 2048, DM = 512, NH = 32, HD = 128;
constexpr size_t WS_CNT = 0;
constexpr size_t WS_HQ = 32768, WS_HR = 33792;
constexpr size_t WS_KV = 65536;
constexpr size_t WS_A3 = WS_KV + (size_t)NH * 32 * 32768;
constexpr size_t WS_KVP = WS_A3 + (size_t)576 * SEQ * 16;
constexpr size_t WS_QB = WS_KVP + (size_t)NH * 8 * 4096;
constexpr size_t WS_PQ = WS_QB + (size_t)256 * 32768;
constexpr size_t WS_W3 = WS_PQ + (size_t)256 * 16384;
constexpr size_t WS_END = WS_W3 + (size_t)8 * 288 * 2048;
constexpr int LDS_BYTES = 163840;

#define MFMA32(a, b, c) __builtin_amdgcn_mfma_f32_32x32x16_bf16((a), (b), (c), 0, 0, 0)
#define MFMA16(a, b, c) __builtin_amdgcn_mfma_f32_16x16x32_bf16((a), (b), (c), 0, 0, 0)
#define SBAR() __builtin_amdgcn_sched_barrier(0)
#define WAIT_V(n) asm volatile("s_waitcnt vmcnt(" #n ")" ::: "memory")
#define WAIT_L(n) asm volatile("s_waitcnt lgkmcnt(" #n ")" ::: "memory")
#define BAR() __builtin_amdgcn_s_barrier()
#define WAIT_BAR(N) asm volatile("s_waitcnt vmcnt(" #N ") lgkmcnt(0)\n\ts_barrier" ::: "memory")

#define STAMP_DECL()
#define STAMP()
#define STAMP_RT()
#define STAMP_OUT(base)
#define STAMP_PARAM
#define STAMP_ARG
DI unsigned rfl(unsigned x) { return __builtin_amdgcn_readfirstlane(x); }
DI unsigned cvtpk(float lo, float hi) { f32x2 v = {lo, hi}; bf16x2_t b = __builtin_convertvector(v, bf16x2_t); return __builtin_bit_cast(unsigned, b); }
DI float bfround(float x) { return __uint_as_float(cvtpk(x, x) << 16); }
DI void glds16(const void* sbase, unsigned voff, unsigned lds_dst) {
    unsigned sv;
    asm volatile("s_mov_b32 %0, m0\n\ts_mov_b32 m0, %3\n\ts_nop 2\n\tglobal_load_lds_dwordx4 %1, %2\n\ts_mov_b32 m0, %0"
                 : "=&s"(sv) : "v"(voff), "s"(sbase), "s"(lds_dst) : "memory");
}
DI void glds16c(const void* sbase, unsigned voff, unsigned lds_dst) {
    unsigned sv;
    asm volatile("s_mov_b32 %0, m0\n\ts_mov_b32 m0, %3\n\ts_nop 2\n\tglobal_load_lds_dwordx4 %1, %2 sc1\n\ts_mov_b32 m0, %0"
                 : "=&s"(sv) : "v"(voff), "s"(sbase), "s"(lds_dst) : "memory");
}
DI void touch8k(const void* sbase, unsigned voff, unsigned lds_dst) {
    unsigned sv;
    asm volatile("s_mov_b32 %0, m0\n\ts_mov_b32 m0, %3\n\ts_nop 2\n\tglobal_load_lds_dword %1, %2\n\ts_mov_b32 m0, %0"
                 : "=&s"(sv) : "v"(voff), "s"(sbase), "s"(lds_dst) : "memory");
}
DI void glds16c_unless(const void* sbase, unsigned voff, unsigned lds_dst, int a, int b) {
    unsigned sv;
    asm volatile("s_cmp_lt_i32 %4, %5\n\ts_cselect_b64 exec, 0, -1\n\ts_mov_b32 %0, m0\n\ts_mov_b32 m0, %3\n\ts_nop 2\n\tglobal_load_lds_dwordx4 %1, %2 sc1\n\ts_mov_b32 m0, %0\n\ts_mov_b64 exec, -1"
                 : "=&s"(sv) : "v"(voff), "s"(sbase), "s"(lds_dst), "s"(a), "s"(b) : "memory", "scc");
}
DI void ldq_unless(bf16x8* q, const void* sbase, unsigned voff, unsigned skip) {
    asm volatile("s_cmp_lg_u32 %10, 0\n\ts_cbranch_scc0 .Lrare%=\n.Lback%=:\n\t.subsection 1\n.Lrare%=:\n\ts_nop 4\n\t"
                 "global_load_dwordx4 %0, %8, %9 offset:-4096\n\tglobal_load_dwordx4 %1, %8, %9 offset:-3072\n\tglobal_load_dwordx4 %2, %8, %9 offset:-2048\n\tglobal_load_dwordx4 %3, %8, %9 offset:-1024\n\t"
                 "global_load_dwordx4 %4, %8, %9\n\tglobal_load_dwordx4 %5, %8, %9 offset:1024\n\tglobal_load_dwordx4 %6, %8, %9 offset:2048\n\tglobal_load_dwordx4 %7, %8, %9 offset:3072\n\ts_branch .Lback%=\n\t.subsection 0"
                 : "+v"(q[0]), "+v"(q[1]), "+v"(q[2]), "+v"(q[3]), "+v"(q[4]), "+v"(q[5]), "+v"(q[6]), "+v"(q[7]) : "v"(voff), "s"((const char*)sbase + 4096), "s"(skip) : "memory", "scc");
}
DI void glds4c(const void* sbase, unsigned lds_dst) {
    unsigned sv, z;
    asm volatile("v_mov_b32 %1, 0\n\ts_mov_b32 %0, m0\n\ts_mov_b32 m0, %3\n\ts_nop 2\n\tglobal_load_lds_dword %1, %2 sc1\n\ts_mov_b32 m0, %0"
                 : "=&s"(sv), "=&v"(z) : "s"(sbase), "s"(lds_dst) : "memory");
}
#define GLDS_IMM(sbase, voff, lds_dst, IMM) asm volatile("s_mov_b32 m0, %2\n\ts_nop 3\n\tglobal_load_lds_dwordx4 %0, %1 offset:%3 sc1" :: "v"(voff), "s"(sbase), "s"(lds_dst), "i"(IMM) : "memory")
#define GLDS_IMM_UNLESS(sbase, voff, lds_dst, IMM, a, b) asm volatile("s_cmp_lt_i32 %4, %5\n\ts_cselect_b64 exec, 0, -1\n\ts_mov_b32 m0, %2\n\ts_nop 1\n\tglobal_load_lds_dwordx4 %0, %1 offset:%3 sc1\n\ts_mov_b64 exec, -1" \
        :: "v"(voff), "s"(sbase), "s"(lds_dst), "i"(IMM), "s"(a), "s"(b) : "memory", "scc")
typedef short v4i16_t __attribute__((ext_vector_type(4)));
DI s16x4 trrd(lds_cptr p) { return __builtin_bit_cast(s16x4, __builtin_amdgcn_ds_read_tr16_b64_v4i16((LAS v4i16_t*)p)); }
DI bf16x8 cat8(s16x4 a, s16x4 b) { return __builtin_shufflevector(a, b, 0, 1, 2, 3, 4, 5, 6, 7); }
DI bf16x8 ldsr128(lds_cptr p) { return *(const LAS bf16x8*)p; }
DI bf16x8 pack8(float a0, float a1, float a2, float a3, float a4, float a5, float a6, float a7) {
    u32x4 w = {cvtpk(a0, a1), cvtpk(a2, a3), cvtpk(a4, a5), cvtpk(a6, a7)}; return __builtin_bit_cast(bf16x8, w); }
#define PACK8(X, B) pack8((X)[(B)], (X)[(B) + 1], (X)[(B) + 2], (X)[(B) + 3], (X)[(B) + 4], (X)[(B) + 5], (X)[(B) + 6], (X)[(B) + 7])
DI float swap32_max(float v) { auto rr = __builtin_amdgcn_permlane32_swap(__float_as_uint(v), __float_as_uint(v), false, false); return fmaxf(__uint_as_float(rr[0]), __uint_as_float(rr[1])); }
DI float swap32_add(float v) { auto rr = __builtin_amdgcn_permlane32_swap(__float_as_uint(v), __float_as_uint(v), false, false); return __uint_as_float(rr[0]) + __uint_as_float(rr[1]); }
DI float lo32_bcast(float v) { auto rr = __builtin_amdgcn_permlane32_swap(__float_as_uint(v), __float_as_uint(v), false, false); return __uint_as_float(rr[0]); }
template <int CTRL> DI float dpp(float v) { return __uint_as_float(__builtin_amdgcn_update_dpp(0u, __float_as_uint(v), CTRL, 0xF, 0xF, true)); }
DI float row16_max(float v) { v = fmaxf(v, dpp<0xB1>(v)); v = fmaxf(v, dpp<0x4E>(v)); v = fmaxf(v, dpp<0x141>(v)); v = fmaxf(v, dpp<0x140>(v)); return v; }
DI float row16_add(float v) { v += dpp<0xB1>(v); v += dpp<0x4E>(v); v += dpp<0x141>(v); v += dpp<0x140>(v); return v; }

constexpr float QSCALE = 0.08838834764831845f * 1.4426950408889634f;
constexpr float THR = 8.f;
constexpr int L_V = 98304;
constexpr int L_WE = 65536, L_GATE = 139264;
#define PIN(x) asm volatile("" : "+v"(x))
#define MX3(a, b, c) __builtin_fmaxf(__builtin_fmaxf((a), (b)), (c))
#define EX2(v) __builtin_amdgcn_exp2f(v)
#define RLX_AGENT __ATOMIC_RELAXED, __HIP_MEMORY_SCOPE_AGENT
#define ST_PL(base, voff, imm, v) asm volatile("s_nop 4\n\tglobal_store_dwordx4 %0, %1, %2 offset:%3\n\ts_nop 1" :: "v"(voff), "v"(v), "s"(base), "i"(imm) : "memory")
#define ST_SC1(base, voff, imm, v) asm volatile("s_nop 4\n\tglobal_store_dwordx4 %0, %1, %2 offset:%3 sc1\n\ts_nop 1" :: "v"(voff), "v"(v), "s"(base), "i"(imm) : "memory")
DI int fresh_lane() { int l; asm volatile("v_mbcnt_lo_u32_b32 %0, -1, 0\n\tv_mbcnt_hi_u32_b32 %0, -1, %0" : "=v"(l)); return l; }
DI void wait_cnt(const unsigned* p, unsigned want) {
    unsigned spins = 0;
    while (rfl(__hip_atomic_load(p, RLX_AGENT)) < want) { __builtin_amdgcn_s_sleep(2); if (++spins > (1u << 20)) break; }
}
DI void handshake_finish(char* ws, int bid, unsigned token, bool ok, int lane) {
    unsigned* CNT = (unsigned*)(ws + WS_CNT); unsigned spins = 0;
    if (bid == 0) {
        for (;;) {
            const unsigned a = __hip_atomic_load(CNT + (80 + (lane & 15)) * 64, RLX_AGENT);
            if (__all(a >= ((lane & 15) ? 16u : 15u))) break;
#pragma unroll
            for (int i = 0; i < 4; ++i) { const unsigned q = __hip_atomic_load((const unsigned*)(ws + WS_HQ) + i * 64 + lane, RLX_AGENT); __hip_atomic_store((unsigned*)(ws + WS_HR) + i * 64 + lane, q, RLX_AGENT); }
            __builtin_amdgcn_s_sleep(8); if (++spins > (1u << 18)) break; }
    } else if (!ok) {
        while (rfl(__hip_atomic_load((const unsigned*)(ws + WS_HR) + bid, RLX_AGENT)) != token) { __builtin_amdgcn_s_sleep(8); if (++spins > (1u << 20)) break; }
        if (lane == 0) __hip_atomic_fetch_add(CNT + (80 + (bid & 15)) * 64, 1u, RLX_AGENT);
    }
}
DI void dual_softmax(f32x16& p) {
#pragma unroll
    for (int hf = 0; hf < 2; ++hf) {
        float mx = -INFINITY;
#pragma unroll
        for (int r = 0; r < 8; ++r) { p[8 * hf + r] = bfround(p[8 * hf + r]); mx = fmaxf(mx, p[8 * hf + r]); }
        mx = swap32_max(mx); float s = 0.f;
#pragma unroll
        for (int r = 0; r < 8; ++r) { p[8 * hf + r] = EX2((p[8 * hf + r] - mx) * 1.4426950408889634f); s += p[8 * hf + r]; }
        s = __builtin_amdgcn_rcpf(swap32_add(s));
#pragma unroll
        for (int r = 0; r < 8; ++r) p[8 * hf + r] *= s;
    }
}
DI void mask_half(f32x16& sc, int dqw) {
    if (dqw < 0) {
#pragma unroll
        for (int r = 0; r < 16; ++r) sc[r] = -INFINITY;
    } else if (dqw == 0) {
        const int ln = fresh_lane(); const int lr = (ln & 31) - 4 * (ln >> 5); const float ninf = -INFINITY;
#pragma unroll
        for (int r = 0; r < 16; ++r)
            asm volatile("s_nop 0\n\tv_cmp_gt_i32 vcc, %c3, %2\n\tv_cndmask_b32 %0, %0, %1, vcc" : "+v"(sc[r]) : "v"(ninf), "v"(lr), "i"(16 * ((r >> 2) & 1) + 8 * (r >> 3) + (r & 3)) : "vcc");
    }
}
DI void mask_tile(f32x16& s0, f32x16& s1, int dqw) {
    asm volatile("s_nop 7\n\ts_nop 7" ::: "memory");
    mask_half(s0, dqw); mask_half(s1, dqw - 32); }

__global__ __launch_bounds__(512, 2) void kf_all(const u16* __restrict__ x, const u16* __restrict__ ckv, const u16* __restrict__ wq, const u16* __restrict__ wuk,
                                                 const u16* __restrict__ wuv, const u16* __restrict__ wo_a, const u16* __restrict__ wqkv,
                                                 const u16* __restrict__ wphi, const u16* __restrict__ wo_b, const u16* __restrict__ gatew,
                                                 char* ws, u16* __restrict__ out) {
    extern __shared__ __attribute__((aligned(16))) char shm[];
    const lds_ptr lds = (lds_ptr)shm; const unsigned lds0 = (unsigned)(uintptr_t)shm;
    const int wid = rfl(threadIdx.x >> 6), G = wid >> 2, w4 = wid & 3;
    const int bid = blockIdx.x, hh_ = 4 * (bid & 7) + (bid >> 6), jj = (bid >> 3) & 7;
#define LANE_VARS() int tid = threadIdx.x; asm volatile("" : "+v"(tid)); const int lane = tid & 63, r32 = lane & 31, hi = lane >> 5, blk = (lane >> 4) & 1, \
        q4 = (lane & 15) >> 2, p4 = lane & 3; const unsigned vlin = (unsigned)lane * 16u; (void)r32; (void)hi; (void)blk; (void)q4; (void)p4; (void)vlin
    unsigned* const CNT = (unsigned*)(ws + WS_CNT);
    char* const KVh = ws + WS_KV + (size_t)hh_ * 32 * 32768;
    char* const A3 = ws + WS_A3;
    STAMP_DECL(); STAMP_RT(); STAMP();
    u32x4 pkv[16];
    unsigned token = 0u;
    const int bA = 15 - jj, bB = jj;
    const char* gX = (const char*)x + (size_t)((G ? bB : bA) * 128 + 32 * w4) * 1024; const char* gW = (const char*)wq + hh_ * 256; const char* gE = (const char*)wqkv + hh_ * 32;
    const unsigned lx = lds0 + wid * 4096, lw = lds0 + L_WE + wid * 1024;
    unsigned voffX, voffW, voffE;
    { const int lane = threadIdx.x & 63;
        { const int row = lane >> 3, ch = (lane & 7) ^ ((row >> 1) & 3); voffX = (unsigned)(row * 1024 + ch * 16); }
        { const int row = 4 * wid + (lane >> 4), j = lane & 15, c = (j >> 2) ^ (row & 3); voffW = (unsigned)(row * 8192 + c * 64 + (j & 3) * 16); }
        { const int row = 8 * wid + (lane >> 3), sl = lane & 7, hf = (sl >> 2) ^ ((row >> 1) & 1), part = hf ? 2 : ((sl >> 1) & 1);
          voffE = (unsigned)(row * 3072 + part * 1024 + (sl & 1) * 16); }
        if (wid == 0) glds16(gatew, (unsigned)lane * 16u, lds0 + L_GATE);
        if (bid == 0 && wid == 1) {
            const unsigned z = 0u;
            asm volatile("global_store_dword %0, %1, %2 sc1\n\tglobal_store_dword %0, %1, %3 sc1\n\ts_nop 1" :: "v"((unsigned)lane * 256u), "v"(z), "s"(ws + WS_CNT), "s"(ws + WS_CNT + 16384) : "memory");
            asm volatile("global_store_dword %0, %1, %2 offset:4 sc1\n\ts_nop 1" :: "v"((unsigned)lane * 256u), "v"(z), "s"(ws + WS_CNT + 16384) : "memory"); }
        PIN(voffX); PIN(voffW); PIN(voffE);
    }
#define XISS(c) do { _Pragma("unroll") for (int e = 0; e < 4; ++e) glds16(gX + e * 8192 + (c) * 128, voffX ^ ((e & 1) << 6), lx + ((c) & 1) * 32768 + e * 1024); } while (0)
#define WISS(c) do { glds16(gW + (size_t)(c) * 524288, voffW, lw + ((c) % 3) * 24576); glds16(gW + (size_t)(c) * 524288 + 262144, voffW, lw + ((c) % 3) * 24576 + 8192); \
                     glds16(gE + (size_t)(c) * 196608, voffE, lw + ((c) % 3) * 24576 + 16384); } while (0)
    {
        LANE_VARS();
        const int wr = wid >> 2, wc = wid & 3, fr = lane & 15, fq = lane >> 4;
        constexpr int HTB = 16384;
#define SA(b, h) (((b) * 2 + (h)) * HTB)
#define SB(b, h) ((4 + (b) * 2 + (h)) * HTB)
        unsigned voffA, voffB;
        { const int row = 8 * wid + (lane >> 3), ch = (lane & 7) ^ ((row >> 1) & 7); voffA = (unsigned)(row * 1024 + ch * 16); }
        { const int row = 4 * wid + (lane >> 4), j = lane & 15, s = ((row >> 3) & 1) * 4 + (row & 3), c = (j >> 1) ^ s; voffB = (unsigned)(row * 8192 + c * 32 + (j & 1) * 16); }
        const char* gA = (const char*)ckv + (size_t)jj * 256 * 1024;
        const char* gB0 = (const char*)wuk + hh_ * 256; const char* gB1 = (const char*)wuv + hh_ * 256;
        const unsigned ldsw = rfl(lds0 + wid * 1024);
#define STAGE_A(b, h, kt) do { const char* g_ = gA + (h) * 131072 + (kt) * 128; glds16(g_, voffA, ldsw + SA(b, h)); glds16(g_ + 65536, voffA, ldsw + SA(b, h) + 8192); } while (0)
#define STAGE_B(b, h, kt) do { const char* g_ = ((h) ? gB1 : gB0) + (size_t)(kt) * 524288; glds16(g_, voffB, ldsw + SB(b, h)); glds16(g_ + 262144, voffB, ldsw + SB(b, h) + 8192); } while (0)
        lds_cptr pa[2], pb[2];
#pragma unroll
        for (int ks = 0; ks < 2; ++ks) pa[ks] = lds + (wr * 64 + fr) * 128 + 16 * ((4 * ks + fq) ^ ((fr >> 1) & 7));
        { const int q = fr >> 2, p = fr & 3, s = (fq & 1) * 4 + q;
#pragma unroll
          for (int n = 0; n < 2; ++n) pb[n] = lds + (8 * fq + q) * 256 + 32 * ((2 * wc + n) ^ s) + 8 * p; }
#define LDA(dst, b, h) do { _Pragma("unroll") for (int m = 0; m < 4; ++m) _Pragma("unroll") for (int k = 0; k < 2; ++k) dst[m][k] = ldsr128(pa[k] + SA(b, h) + m * 2048); } while (0)
#define LDB(dst, b, h) do { _Pragma("unroll") for (int n = 0; n < 2; ++n) _Pragma("unroll") for (int k = 0; k < 2; ++k) \
        dst[n][k] = cat8(trrd(pb[n] + SB(b, h) + k * 8192), trrd(pb[n] + SB(b, h) + k * 8192 + 1024)); } while (0)
#define MMA(ai, bj, At, Bt) do { __builtin_amdgcn_s_setprio(1); _Pragma("unroll") for (int m = 0; m < 4; ++m) _Pragma("unroll") for (int n = 0; n < 2; ++n) _Pragma("unroll") for (int k = 0; k < 2; ++k) \
        acc[ai][bj][m][n] = (bj) ? MFMA16(At[m][k], Bt[n][k], acc[ai][bj][m][n]) : MFMA16(Bt[n][k], At[m][k], acc[ai][bj][m][n]); __builtin_amdgcn_s_setprio(0); } while (0)
        const unsigned scr = lds0 + 143360 + wid * 256; const size_t gw = (size_t)(bid * 8 + wid);
        auto warm = [&](int slot) {
            if (slot == 0 && wid == 0) { token = rfl(*(const LAS unsigned*)(lds + 148480)) + 1u; const unsigned z = 0u, tk = token;
                asm volatile("s_nop 4\n\tglobal_store_dword %0, %1, %2 sc1\n\ts_nop 1" :: "v"(z), "v"(tk), "s"(ws + WS_HQ + bid * 4) : "memory"); }
            if (bid == 0 && wid == 1) {
                if (slot > 0) { const u32x4 rq = *(const LAS u32x4*)(lds + 147456 + lane * 16); ST_SC1(ws + WS_HR, (unsigned)lane * 16u, 0, rq); WAIT_L(0); }
                glds16c(ws + WS_HQ, (unsigned)lane * 16u, lds0 + 147456); }
            const unsigned vo = (unsigned)lane * 128u;
            if (slot == 0) { if (lane < 8) touch8k((const char*)x + gw * 1024, vo, scr); }
            else if (slot == 1) { if (lane < 16) touch8k((const char*)wq + gw * 2048, vo, scr); }
            else if (slot == 2) { if (lane < 6) touch8k((const char*)wqkv + gw * 768, vo, scr); }
            if (slot == 3) {
                const int row = lane >> 3, sl = lane & 7, nb = (sl >> 2) ^ ((row >> 1) & 1); const unsigned vw = (unsigned)(row * 1024 + nb * 64 + (sl & 3) * 16);
                const char* g = (const char*)wo_a + (size_t)(8 * hh_ + wid) * 16384 + jj * 128;
                glds16(g, vw, lds0 + 149504 + wid * 1024); glds16(g + 8192, vw, lds0 + 131072 + wid * 1024);
                if (wid < 2) glds16((const char*)wo_b + (size_t)hh_ * 16384 + wid * 8192 + jj * 128, vw, lds0 + 157696 + wid * 1024);
            } else if (slot == 4) {
                char* d = ws + WS_W3 + (size_t)(jj * 288 + 8 * hh_ + wid) * 2048; const unsigned vl = (unsigned)lane * 16u;
                { const u32x4 t0 = *(const LAS u32x4*)(lds + 149504 + wid * 1024 + lane * 16); ST_SC1(d, vl, 0, t0); }
                { const u32x4 t1 = *(const LAS u32x4*)(lds + 131072 + wid * 1024 + lane * 16); ST_SC1(d, vl, 1024, t1); }
                if (wid < 2) { const u32x4 t2 = *(const LAS u32x4*)(lds + 157696 + wid * 1024 + lane * 16); ST_SC1(ws + WS_W3 + (size_t)(jj * 288 + 256 + hh_) * 2048 + wid * 1024, vl, 0, t2); }
                WAIT_L(0);
            }
        };
        f32x4 acc[2][2][4][2] = {};
        bf16x8 At[4][2], B0[2][2], B1[2][2];
        constexpr int nt = 8;
        STAGE_B(0, 0, 0); STAGE_A(0, 0, 0); STAGE_B(0, 1, 0); STAGE_A(0, 1, 0);
        if (wid == 3) {
            unsigned long long pc; asm volatile("s_getpc_b64 %0" : "=s"(pc));
            if (lane < 11) touch8k((const char*)(pc & ~127ull) + (size_t)(bid >> 3) * 11 * 128, (unsigned)lane * 128u, scr); }
        if (wid == 0) glds4c(ws + WS_HR + bid * 4, lds0 + 148480);
        if (wr == 1) BAR();
        if (wid == 0 || (CODEWARM && wid == 3)) { WAIT_V(5); } else { WAIT_V(4); } BAR();
        STAGE_B(1, 0, 1); STAGE_A(1, 0, 1); STAGE_B(1, 1, 1);
        if (wid == 0 || (CODEWARM && wid == 3)) { WAIT_V(7); } else { WAIT_V(6); } BAR();
        { const unsigned vo = (unsigned)lane * 128u;
          if (lane < 16) { touch8k((const char*)wuk + gw * 2048, vo, scr); touch8k((const char*)wuv + gw * 2048, vo, scr); }
          if (lane < 8) touch8k((const char*)ckv + gw * 1024, vo, scr); }
        STAMP();
        for (int t = 0; t < nt - 2; t += 2) {
            LDB(B0, 0, 0); SBAR(); LDA(At, 0, 0); STAGE_A(1, 1, t + 1);
            WAIT_L(8); BAR(); WAIT_L(0); MMA(0, 0, At, B0); BAR(); SBAR();
            LDB(B1, 0, 1); STAGE_B(0, 0, t + 2);
            BAR(); WAIT_L(0); MMA(0, 1, At, B1); BAR();
            LDA(At, 0, 1); STAGE_A(0, 0, t + 2);
            BAR(); WAIT_L(0); MMA(1, 0, At, B0); BAR(); SBAR();
            STAGE_B(0, 1, t + 2);
            WAIT_V(6); warm(t); BAR(); MMA(1, 1, At, B1); BAR();
            LDB(B0, 1, 0); SBAR(); LDA(At, 1, 0); STAGE_A(0, 1, t + 2);
            WAIT_L(8); BAR(); WAIT_L(0); MMA(0, 0, At, B0); BAR(); SBAR();
            LDB(B1, 1, 1); STAGE_B(1, 0, t + 3);
            BAR(); WAIT_L(0); MMA(0, 1, At, B1); BAR();
            LDA(At, 1, 1); STAGE_A(1, 0, t + 3);
            BAR(); WAIT_L(0); MMA(1, 0, At, B0); BAR(); SBAR();
            STAGE_B(1, 1, t + 3);
            WAIT_V(6); warm(t + 1); BAR(); MMA(1, 1, At, B1); BAR();
        }
        { LDB(B0, 0, 0); WAIT_V(0); LDA(At, 0, 0); STAGE_A(1, 1, nt - 1);
          BAR(); WAIT_L(0); MMA(0, 0, At, B0); BAR();
          LDB(B1, 0, 1); BAR(); WAIT_L(0); MMA(0, 1, At, B1); BAR();
          LDA(At, 0, 1); BAR(); WAIT_L(0); MMA(1, 0, At, B0); MMA(1, 1, At, B1); BAR(); }
        { LDB(B0, 1, 0); LDA(At, 1, 0); BAR(); WAIT_L(0); MMA(0, 0, At, B0); BAR();
          LDB(B1, 1, 1); WAIT_V(0);
          WISS(0); XISS(0);
          BAR(); WAIT_L(0); MMA(0, 1, At, B1); BAR();
          LDA(At, 1, 1); BAR(); WAIT_L(0); MMA(1, 0, At, B0); MMA(1, 1, At, B1); BAR(); }
        if (wr == 0) BAR();
        WISS(1); XISS(1);
        if (wid == 0) glds4c(ws + WS_HR + bid * 4, lds0 + 148480);
        SBAR();
#undef SA
#undef SB
#undef STAGE_A
#undef STAGE_B
#undef LDA
#undef LDB
#undef MMA
#pragma unroll
        for (int ai = 0; ai < 2; ++ai) {
#pragma unroll
            for (int m = 0; m < 4; ++m) { const f32x4 a = acc[ai][0][m][0], b = acc[ai][0][m][1];
                pkv[ai * 4 + m] = (u32x4){cvtpk(a[0], a[1]), cvtpk(a[2], a[3]), cvtpk(b[0], b[1]), cvtpk(b[2], b[3])}; }
#pragma unroll
            for (int u = 0; u < 2; ++u)
#pragma unroll
                for (int n = 0; n < 2; ++n) { const f32x4 a = acc[ai][1][2 * u][n], b = acc[ai][1][2 * u + 1][n];
                    pkv[8 + ai * 4 + u * 2 + n] = (u32x4){cvtpk(a[0], a[1]), cvtpk(a[2], a[3]), cvtpk(b[0], b[1]), cvtpk(b[2], b[3])}; }
        }
    }
    STAMP();
    const char* kvw = KVh + w4 * 4096;
    const unsigned ldw = lds0 + w4 * 4096;
    char* const qbp = ws + WS_QB + (size_t)(bid * 4 + w4) * 8192;
    char* const pqp = ws + WS_PQ + (size_t)(bid * 8 + wid) * 2048;
    bf16x8 qf[8]; float gate;
    {
        LANE_VARS();
        u16 wph[8];
#pragma unroll
        for (int j = 0; j < 8; ++j) wph[j] = wphi[hh_ * 256 + (8 * (j >> 2) + 4 * hi + (j & 3)) * 16 + (lane & 15)];
        const char* tb0 = KVh + (size_t)(4 * jj + G) * 32768;
        const unsigned vok = (unsigned)((4 * w4 + (lane >> 4)) * 1024 + (lane & 15) * 16), vov = (unsigned)(16384 + (lane >> 4) * 2048 + (w4 * 32 + (lane & 15)) * 16);
#define KVST(i) do { if ((i) < 8) ST_SC1(tb0 + ((i) >> 2) * 65536, vok, ((i) & 3) * 256, pkv[i]); \
                     else ST_SC1(tb0 + (((i) >> 2) & 1) * 65536 + (((i) >> 1) & 1) * 8192, vov, ((i) & 1) * 256, pkv[i]); } while (0)
        f32x16 qa[5] = {}, d1 = {};
        unsigned xo[2], wo_[4];
#pragma unroll
        for (int s = 0; s < 2; ++s) xo[s] = (unsigned)((32 * wid + r32) * 128 + 16 * ((2 * s + hi) ^ ((r32 >> 1) & 7)));
#pragma unroll
        for (int c = 0; c < 4; ++c) wo_[c] = (unsigned)(L_WE + (8 * hi + q4) * 256 + 64 * (c ^ q4) + 32 * (p4 >> 1) + 16 * blk + 8 * (p4 & 1));
        const unsigned eo = (unsigned)(L_WE + 16384 + (8 * hi + q4) * 128 + 64 * ((q4 >> 1) & 1) + 32 * blk + 8 * p4);
        const unsigned vo_ = (unsigned)(L_WE + 16384 + (8 * hi + q4) * 128 + 64 * (((q4 >> 1) & 1) ^ 1) + 8 * p4);
        bf16x8 fx[2], fw[2][6], fg[2];
#define QR_(buf, c, ks, u) do { \
        if ((u) == 0) { fx[buf] = ldsr128(lds + ((c) & 1) * 32768 + (xo[(ks) & 1] ^ (((ks) >> 1) << 6))); fg[buf] = ldsr128(lds + L_GATE + ((c) * 64 + (ks) * 16 + 8 * hi) * 2); } \
        else if ((u) < 5) { fw[buf][(u) - 1] = cat8(trrd(lds + wo_[(u) - 1] + ((c) % 3) * 24576 + (ks) * 4096), trrd(lds + wo_[(u) - 1] + ((c) % 3) * 24576 + (ks) * 4096 + 1024)); } \
        else { fw[buf][4] = cat8(trrd(lds + eo + ((c) % 3) * 24576 + (ks) * 2048), trrd(lds + eo + ((c) % 3) * 24576 + (ks) * 2048 + 512)); \
               fw[buf][5] = cat8(trrd(lds + vo_ + ((c) % 3) * 24576 + (ks) * 2048), trrd(lds + vo_ + ((c) % 3) * 24576 + (ks) * 2048 + 512)); } } while (0)
#define QM_(buf, u) do { if ((u) < 5) qa[u] = MFMA32(fw[buf][u], fx[buf], qa[u]); else d1 = MFMA32(fx[buf], blk ? fg[buf] : fw[buf][5], d1); } while (0)
#define QRD(buf, c, ks) do { _Pragma("unroll") for (int u_ = 0; u_ < 6; ++u_) QR_(buf, c, ks, u_); } while (0)
#define QSTEP(buf, c, ks, rd) do { _Pragma("unroll") for (int u_ = 0; u_ < 6; ++u_) { if (rd) QR_((buf) ^ 1, c, ks, u_); QM_(buf, u_); SBAR(); } } while (0)
#define WV_(n) asm volatile("s_waitcnt vmcnt(%0)" :: "i"(n) : "memory")
#define NST(c) ((c) < 0 ? 0 : 2)
#define ST0(c) (2 * (c))
#define QCHUNK(c) do { \
        SBAR(); QSTEP(0, c, 1, true); QSTEP(1, c, 2, true); \
        if ((c) < 7) { WV_(4 + NST((c) - 1)); } BAR(); asm volatile("" ::: "memory"); \
        if ((c) < 6) WISS((c) + 2); \
        _Pragma("unroll") for (int i_ = 0; i_ < NST(c); ++i_) KVST(ST0(c) + i_); \
        SBAR(); QSTEP(0, c, 3, true); \
        if ((c) < 7) { WV_(((c) < 6 ? 3 : 0) + NST(c)); } \
        SBAR(); QSTEP(1, (c) + 1, 0, (c) < 7); \
        if ((c) < 6) XISS((c) + 2); } while (0)
        if (wid == 0) { WAIT_V(8); } else { WAIT_V(7); } BAR(); asm volatile("" ::: "memory");
        bool hs_ok = true;
        bf16x8 wpf;
#pragma unroll
        for (int j = 0; j < 8; ++j) wpf[j] = (short)(blk ? (wph[j] ^ 0x8000) : wph[j]);
        PIN(wpf);
        STAMP();
        QRD(0, 0, 0);
        QCHUNK(0);
        {
            unsigned t0, t1, seen, em;
            asm volatile("v_mov_b32 %0, %5\n\tds_read_b32 %0, %0\n\tv_mov_b32 %1, 1\n\ts_waitcnt lgkmcnt(0)\n\tv_readfirstlane_b32 %2, %0\n\tv_mov_b32 %0, 0\n\ts_nop 3\n\t"
                         "s_min_u32 %3, %8, 1\n\ts_cmp_eq_u32 %7, 0\n\ts_cselect_b32 %3, %3, 0\n\t"
                         "s_cmp_eq_u32 %2, %6\n\ts_cselect_b32 %2, 1, 0\n\ts_and_b32 %3, %3, %2\n\ts_mov_b32 exec_lo, %3\n\ts_mov_b32 exec_hi, 0\n\t"
                         "global_atomic_add %0, %1, %4\n\ts_mov_b64 exec, -1"
                         : "=&v"(t0), "=&v"(t1), "=&s"(seen), "=&s"(em) : "s"(CNT + (80 + (bid & 15)) * 64), "s"(lds0 + 148480), "s"(token), "s"(wid), "s"(bid) : "memory", "scc");
            hs_ok = seen != 0u; }
        QCHUNK(1); QCHUNK(2); QCHUNK(3); QCHUNK(4); QCHUNK(5); QCHUNK(6); QCHUNK(7);
#undef QRD
#undef QR_
#undef QM_
#undef QSTEP
#undef QCHUNK
#undef XISS
#undef WISS
#undef KVST
#undef NST
#undef ST0
        STAMP();
#pragma unroll
        for (int cc = 0; cc < 4; ++cc) {
#pragma unroll
            for (int r = 0; r < 16; ++r) qa[cc][r] *= QSCALE;
            qf[2 * cc] = PACK8(qa[cc], 0); qf[2 * cc + 1] = PACK8(qa[cc], 8);
        }
        f32x16 pq = MFMA32(wpf, PACK8(qa[4], 0), f32x16{});
        f32x16 pk = MFMA32(wpf, PACK8(qa[4], 8), f32x16{});
        dual_softmax(pq); dual_softmax(pk);
        f32x16 d3;
        { bf16x8 id0, id1;
#pragma unroll
          for (int j = 0; j < 8; ++j) { const int f = 8 * (j >> 2) + 4 * hi + (j & 3); id0[j] = (short)(r32 == f ? 0x3F80 : 0); id1[j] = (short)(r32 == f + 16 ? 0x3F80 : 0); }
          d3 = MFMA32(PACK8(pk, 0), id0, f32x16{}); d3 = MFMA32(PACK8(pk, 8), id1, d3); }
        const bf16x8 ones = {0x3F80, 0x3F80, 0x3F80, 0x3F80, 0x3F80, 0x3F80, 0x3F80, 0x3F80};
        const bf16x8 v0 = PACK8(d1, 0), v1 = PACK8(d1, 8);
        f32x16 d4 = MFMA32(PACK8(d3, 0), blk ? ones : v0, f32x16{});
        d4 = MFMA32(PACK8(d3, 8), blk ? ones : v1, d4);
        bf16x8 pq0 = PACK8(pq, 0), pq1 = PACK8(pq, 8); PIN(pq0); PIN(pq1); PIN(d4);
        if (wid == 0) handshake_finish(ws, bid, token, hs_ok, lane);
        WAIT_BAR(0);
        if (tid == 0) __hip_atomic_fetch_add(CNT + hh_ * 64, 1u, RLX_AGENT);
        STAMP();
        LAS float* red = (LAS float*)(lds + L_V + G * 32768 + w4 * 4096);
        if (r32 == 16) {
#pragma unroll
            for (int r = 0; r < 16; ++r) red[(r & 3) + 8 * (r >> 2) + 4 * hi] = d1[r];
        }
        WAIT_L(0);
        { const float gz = bfround(red[r32]); gate = bfround(1.f / (1.f + __expf(-gz))); }
        PIN(gate);
#pragma unroll
        for (int r = 0; r < 16; ++r) red[((r & 3) + 8 * (r >> 2) + 4 * hi) * 32 + r32] = d4[r];
        ST_PL(pqp, vlin, 0, pq0); ST_PL(pqp, vlin, 1024, pq1);
        if (G == 0) {
#pragma unroll
            for (int s = 0; s < 8; ++s) ST_PL(qbp + (s >> 2) * 4096, vlin, (s & 3) * 1024, qf[s]);
        }
        WAIT_L(0); BAR(); asm volatile("" ::: "memory");
        if (tid < 256) {
            f32x4 s = {0.f, 0.f, 0.f, 0.f};
#pragma unroll
            for (int w = 0; w < 8; ++w) s += *(const LAS f32x4*)(lds + L_V + (w >> 2) * 32768 + (w & 3) * 4096 + tid * 16);
            ST_SC1(ws + WS_KVP + (size_t)(hh_ * 8 + jj) * 4096, (unsigned)tid * 16u, 0, s);
        }
        wait_cnt(CNT + hh_ * 64, 8u);
        asm volatile("" ::: "memory");
#pragma unroll
        for (int e = 0; e < 4; ++e) glds16c(kvw + (size_t)(e >> 1) * 32768 + (2 * G + (e & 1)) * 1024, vlin, ldw + (e >> 1) * 16384 + (2 * G + (e & 1)) * 1024);
        WAIT_BAR(0);
    }
    STAMP();
    {
    LANE_VARS();
    const int prow = (r32 & 7) | ((r32 & 8) << 1) | ((r32 & 16) >> 1);
    const unsigned kro = (unsigned)(prow * 16 + hi * 1024), vro = (unsigned)(L_V + r32 * 16 + hi * 2048);
    const int dq0 = 32 * w4;
    const int T = 2 * jj + 2;
    unsigned ks0 = 0u, ks1 = 16384u, ks2 = 32768u;
    int rho = 0; const int TG = G ? T : 99;
    f32x16 o[4], negm, sA0, sA1, sB0, sB1; bf16x8 kf[16], vf[16]; u32x4 pw[4]; float m, l;
#define KRD(i, kp) kf[i] = ldsr128((kp) + ((i) >> 1) * 2048 + ((i) & 1) * 512)
#define VRD(i, vp) vf[i] = ldsr128((vp) + ((i) >> 2) * 4096 + ((i) & 3) * 512)
#define PF(k) __builtin_bit_cast(bf16x8, pw[k])
#define DMA1(i) do { if ((i) < 2) GLDS_IMM(gk_, vlin, dk_, ((i) & 3) * 1024); else if ((i) < 4) GLDS_IMM_UNLESS(gk_, vlin, dk_, ((i) & 3) * 1024, rk_, T); \
                     else if ((i) < 6) GLDS_IMM(gv_, vlin, dv_, ((i) & 3) * 1024); else GLDS_IMM_UNLESS(gv_, vlin, dv_, ((i) & 3) * 1024, rho, T); } while (0)
    const char* gk_; const char* gv_; unsigned dk_, dv_, ko_, vo_; int rk_;
    unsigned go1, go2;
#define GOFF(r) ((r) < T ? (unsigned)(r) * 32768u + G * 2048u : (unsigned)(2 * (r) - T + G) * 32768u)
#define PREP_A(r, k0) do { ko_ = ((r) >= TG ? 49152u : 0u) + (k0); vo_ = ((r) - 1 >= TG ? 32768u : 0u) + (((r) - 1) & 1) * 16384u; } while (0)
#define PREP_B(r, gcur) do { gv_ = kvw + ((gcur) + 16384u); dv_ = ldw + L_V + ((r) & 1) * 16384u + ((r) < T ? G * 2048u : G * 32768u); } while (0)
#define PREP_C(r) do { rk_ = (r) + 2 <= 16 ? (r) + 2 : 16; go1 = go2; go2 = GOFF(rk_); } while (0)
#define PREP_D(k2) do { gk_ = kvw + go2; dk_ = ldw + (k2) + (rk_ < T ? G * 2048u : G * 49152u); } while (0)
#define ROT() do { const unsigned t_ = ks0; ks0 = ks1; ks1 = ks2; ks2 = t_; ++rho; } while (0)
#define GAPA(i, C0, C1, P0, P1) do { \
        if ((i) + PD < 16) { KRD((i) + PD, kp_); } else { VRD((i) + PD - 16, vp_); } \
        if (((i) & 1) == 0) DMA1((i) >> 1); \
        if ((i) & 1) C1 = MFMA32(kf[i], qf[(i) >> 1], (i) < 2 ? negm : C1); else C0 = MFMA32(kf[i], qf[(i) >> 1], (i) < 2 ? negm : C0); \
        if ((i) < 8) { P1[2 * (i)] = EX2(P1[2 * (i)]); P1[2 * (i) + 1] = EX2(P1[2 * (i) + 1]); pw[(i) >> 2][(i) & 3] = cvtpk(P0[2 * (i)], P0[2 * (i) + 1]); PIN(P1[2 * (i)]); PIN(P1[2 * (i) + 1]); PIN(pw[(i) >> 2][(i) & 3]); } \
        else { l += P1[2 * (i) - 16]; l2 += P1[2 * (i) - 15]; pw[(i) >> 2][(i) & 3] = cvtpk(P1[2 * (i) - 16], P1[2 * (i) - 15]); PIN(l); PIN(l2); PIN(pw[(i) >> 2][(i) & 3]); } \
        SBAR(); } while (0)
#define PVM(i) o[(i) & 3] = MFMA32(vf[i], PF((i) >> 2), o[(i) & 3])
#define RDB(i) do { if ((i) + PD < 16) { VRD((i) + PD, vp_); } else { KRD((i) + PD - 16, kn_); } } while (0)
#define ROUND(C0, C1, P0, P1) do { \
        const lds_cptr kp_ = lds + ko_ + kro; const lds_cptr vp_ = lds + vo_ + vro; \
        float l2 = 0.f; lds_cptr kn_ = kp_; SBAR(); \
        GAPA(0, C0, C1, P0, P1); GAPA(1, C0, C1, P0, P1); GAPA(2, C0, C1, P0, P1); GAPA(3, C0, C1, P0, P1); \
        GAPA(4, C0, C1, P0, P1); GAPA(5, C0, C1, P0, P1); GAPA(6, C0, C1, P0, P1); GAPA(7, C0, C1, P0, P1); \
        GAPA(8, C0, C1, P0, P1); GAPA(9, C0, C1, P0, P1); GAPA(10, C0, C1, P0, P1); GAPA(11, C0, C1, P0, P1); \
        GAPA(12, C0, C1, P0, P1); GAPA(13, C0, C1, P0, P1); GAPA(14, C0, C1, P0, P1); GAPA(15, C0, C1, P0, P1); \
        l += l2; \
        ldq_unless(qf, qbp, vlin, (unsigned)(rho + 1 - TG));        \
        RDB(0); PVM(0); SBAR(); RDB(1); PVM(1); SBAR(); \
        if (__builtin_expect(rho == 16 || (rho >= TG - 2 && rho < TG), 0)) mask_tile(C0, C1, dq0 - (rho == 16 ? 64 * G : 64 * (rho - TG + 2))); \
        float ma, mb; \
        RDB(2); PVM(2); ma = MX3(C0[0], C0[1], C1[0]); mb = MX3(C0[2], C0[3], C1[1]); ma = MX3(ma, C1[2], C1[3]); mb = MX3(mb, C0[4], C0[5]); PIN(ma); PIN(mb); SBAR(); \
        RDB(3); PVM(3); ma = MX3(ma, C0[6], C0[7]); mb = MX3(mb, C1[4], C1[5]); ma = MX3(ma, C1[6], C1[7]); mb = MX3(mb, C0[8], C0[9]); PIN(ma); PIN(mb); SBAR(); \
        RDB(4); PVM(4); ma = MX3(ma, C0[10], C0[11]); mb = MX3(mb, C1[8], C1[9]); ma = MX3(ma, C1[10], C1[11]); mb = MX3(mb, C0[12], C0[13]); PIN(ma); PIN(mb); SBAR(); \
        RDB(5); PVM(5); ma = MX3(ma, C0[14], C0[15]); mb = MX3(mb, C1[12], C1[13]); ma = MX3(ma, C1[14], C1[15]); \
        const float rm = swap32_max(fmaxf(ma, mb)); bool pend = false; float fsc = 1.f; \
        if (__builtin_expect(__any(rm > THR) || rho == TG, 0)) { float dl; pend = true; \
            if (rho == TG) { dl = fmaxf(rm, -1e30f); m = dl; fsc = gate / swap32_add(l); l = 0.f; }        \
            else { dl = fmaxf(rm, 0.f); m += dl; fsc = EX2(-dl); l *= fsc; } \
            _Pragma("unroll") for (int e = 0; e < 16; ++e) { C0[e] -= dl; C1[e] -= dl; negm[e] = -m; } } \
        SBAR(); \
        RDB(6); PVM(6); C0[0] = EX2(C0[0]); C0[1] = EX2(C0[1]); PIN(C0[0]); PIN(C0[1]); SBAR(); \
        RDB(7); PVM(7); C0[2] = EX2(C0[2]); C0[3] = EX2(C0[3]); l += C0[0]; l2 = C0[1]; PIN(C0[2]); PIN(C0[3]); PIN(l); SBAR(); \
        RDB(8); PVM(8); PREP_A(rho + 1, ks1); kn_ = lds + ko_ + kro; C0[4] = EX2(C0[4]); C0[5] = EX2(C0[5]); l += C0[2]; l2 += C0[3]; PIN(C0[4]); PIN(C0[5]); PIN(l); PIN(l2); SBAR(); \
        RDB(9); PVM(9); PREP_B(rho + 1, go1); C0[6] = EX2(C0[6]); C0[7] = EX2(C0[7]); l += C0[4]; l2 += C0[5]; PIN(C0[6]); PIN(C0[7]); PIN(l); PIN(l2); SBAR(); \
        RDB(10); PVM(10); PREP_C(rho + 1); C0[8] = EX2(C0[8]); C0[9] = EX2(C0[9]); l += C0[6]; l2 += C0[7]; PIN(C0[8]); PIN(C0[9]); PIN(l); PIN(l2); SBAR(); \
        RDB(11); PVM(11); PREP_D(ks0); C0[10] = EX2(C0[10]); C0[11] = EX2(C0[11]); l += C0[8]; l2 += C0[9]; PIN(C0[10]); PIN(C0[11]); PIN(l); PIN(l2); SBAR(); \
        RDB(12); PVM(12); C0[12] = EX2(C0[12]); C0[13] = EX2(C0[13]); l += C0[10]; l2 += C0[11]; PIN(C0[12]); PIN(C0[13]); PIN(l); PIN(l2); SBAR(); \
        RDB(13); PVM(13); C0[14] = EX2(C0[14]); C0[15] = EX2(C0[15]); l += C0[12]; l2 += C0[13]; PIN(C0[14]); PIN(C0[15]); PIN(l); PIN(l2); SBAR(); \
        RDB(14); PVM(14); l += C0[14]; l2 += C0[15]; PIN(l); PIN(l2); SBAR(); \
        RDB(15); PVM(15); l += l2; PIN(l); SBAR(); \
        if (__builtin_expect(pend || rho + 1 == TG, 0)) { \
            if (pend) { _Pragma("unroll") for (int c = 0; c < 4; ++c) _Pragma("unroll") for (int e = 0; e < 16; ++e) o[c][e] *= fsc; } \
            if (rho + 1 == TG) negm = f32x16{}; \
            if (rho == TG) { const int ln = fresh_lane(); const unsigned vod = (unsigned)((ln >> 5) * 2048 + (32 * w4 + (ln & 31)) * 16); const char* dst = A3 + (size_t)(bB * 576 + hh_ * 16) * 2048; \
                _Pragma("unroll") for (int c = 0; c < 4; ++c) { const bf16x8 t0 = PACK8(o[c], 0), t1 = PACK8(o[c], 8); ST_SC1(dst + c * 8192, vod, 0, t0); ST_SC1(dst + c * 8192 + 4096, vod, 0, t1); o[c] = f32x16{}; } } } \
        asm volatile("s_cmp_eq_u32 %0, %1\n\ts_cbranch_scc1 .Lw%=\n\ts_waitcnt vmcnt(0)\n.Lw%=:\n\ts_waitcnt vmcnt(8)\n\ts_barrier" :: "s"(rho), "s"(TG) : "memory", "scc");     \
        if (__builtin_expect(rho == TG + 1 && w4 == 0, 0)) { if (fresh_lane() == 0) __hip_atomic_fetch_or(CNT + (64 + bB) * 64, 1u << hh_, RLX_AGENT); }        \
        ROT(); } while (0)

#define ROUND0(C0, C1) do { \
        const lds_cptr kp_ = lds + ks0 + kro; const lds_cptr kn_ = lds + ks1 + kro; \
        go2 = GOFF(1); PREP_B(0, GOFF(0)); PREP_C(0); PREP_D(ks2); \
        _Pragma("unroll") for (int i = 0; i < 6; ++i) KRD(i, kp_); \
        _Pragma("unroll") for (int i = 0; i < 8; ++i) DMA1(i); \
        _Pragma("unroll") for (int c = 0; c < 4; ++c) o[c] = f32x16{}; \
        PREP_A(1, ks1); PREP_B(1, go1); PREP_C(1); PREP_D(ks0); SBAR(); \
        _Pragma("unroll") for (int i = 0; i < 16; ++i) { \
            if (i + 6 < 16) KRD(i + 6, kp_); \
            if (i & 1) C1 = MFMA32(kf[i], qf[i >> 1], i < 2 ? f32x16{} : C1); else C0 = MFMA32(kf[i], qf[i >> 1], i < 2 ? f32x16{} : C0); \
            SBAR(); } \
        if (TG == 2) mask_tile(C0, C1, dq0); \
        float ma = MX3(C0[0], C0[1], C1[0]), mb = MX3(C0[2], C0[3], C1[1]); ma = MX3(ma, C1[2], C1[3]); \
        _Pragma("unroll") for (int r = 4; r < 16; r += 4) { ma = MX3(ma, C0[r], C0[r + 1]); mb = MX3(mb, C0[r + 2], C0[r + 3]); ma = MX3(ma, C1[r], C1[r + 1]); mb = MX3(mb, C1[r + 2], C1[r + 3]); } \
        m = fmaxf(swap32_max(fmaxf(ma, mb)), -1e30f); l = 0.f; \
        _Pragma("unroll") for (int i = 0; i < PD; ++i) KRD(i, kn_); \
        _Pragma("unroll") for (int e = 0; e < 16; ++e) { C0[e] = EX2(C0[e] - m); l += C0[e]; C1[e] -= m; negm[e] = -m; } \
        asm volatile("s_waitcnt vmcnt(0)\n\ts_barrier" ::: "memory"); ROT(); } while (0)
#define DRAIN(P0, P1, MID) do { \
        const lds_cptr vp_ = lds + G * 32768 + vro; \
        _Pragma("unroll") for (int i = 0; i < 8; ++i) VRD(i, vp_); \
        _Pragma("unroll") for (int i = 0; i < 8; ++i) pw[i >> 2][i & 3] = cvtpk(P0[2 * i], P0[2 * i + 1]); \
        SBAR(); \
        _Pragma("unroll") for (int i = 0; i < 16; ++i) { \
            if (i + 8 < 16) VRD(i + 8, vp_); \
            if (i == 8) { MID } \
            PVM(i); \
            if (i < 8) { P1[2 * i] = EX2(P1[2 * i]); P1[2 * i + 1] = EX2(P1[2 * i + 1]); PIN(P1); } \
            if (i >= 1 && i < 9) { l += P1[2 * i - 2]; l += P1[2 * i - 1]; pw[2 + ((i - 1) >> 2)][(i - 1) & 3] = cvtpk(P1[2 * i - 2], P1[2 * i - 1]); PIN(l); PIN(pw[2 + ((i - 1) >> 2)]); } \
            SBAR(); } \
        l = swap32_add(l); } while (0)
    ROUND0(sA0, sA1);
    if (wid == 0 && fresh_lane() == 0) __hip_atomic_fetch_add(CNT + (32 + hh_) * 64, 1u, RLX_AGENT);
    STAMP();
#pragma clang loop unroll(disable)
    for (int pr = 0; pr < 8; ++pr) { ROUND(sB0, sB1, sA0, sA1); ROUND(sA0, sA1, sB0, sB1); }
    STAMP();
    {
        f32x2 ps[8]; bf16x8 phq[2];
        LANE_VARS();
        const unsigned f2 = __hip_atomic_load(CNT + (32 + hh_) * 64, RLX_AGENT);
        DRAIN(sA0, sA1,
            if (rfl(f2) < 8u) wait_cnt(CNT + (32 + hh_) * 64, 8u);
            _Pragma("unroll") for (int w = 0; w < 8; ++w) ps[w] = __builtin_bit_cast(f32x2, __hip_atomic_load((const unsigned long long*)(ws + WS_KVP + (size_t)(hh_ * 8) * 4096 + (unsigned)(w * 4096 + tid * 8)), RLX_AGENT));
            phq[0] = *(const bf16x8*)(pqp + vlin); phq[1] = *(const bf16x8*)(pqp + (vlin + 1024)); );
        const unsigned vod = (unsigned)(hi * 2048 + (32 * w4 + r32) * 16);
        BAR(); asm volatile("" ::: "memory");
        const unsigned st0 = ks2;
        { const lds_ptr xp = lds + L_V + wid * 8192 + lane * 16;
#pragma unroll
          for (int c = 0; c < 2; ++c)
#pragma unroll
              for (int g = 0; g < 4; ++g) { f32x4 t;
#pragma unroll
                  for (int e = 0; e < 4; ++e) t[e] = G ? o[c][4 * g + e] : o[2 + c][4 * g + e];
                  *(LAS f32x4*)(xp + (c * 4 + g) * 1024) = t; }
          *(LAS f32x4*)(lds + st0 + wid * 1024 + lane * 16) = (f32x4){m, l, gate, 0.f};
          f32x2 s = ps[0];
#pragma unroll
          for (int w = 1; w < 8; ++w) s += ps[w];
          *(LAS f32x2*)(lds + st0 + 8192 + tid * 8) = s; }
        WAIT_L(0); BAR(); asm volatile("" ::: "memory");
        { const int ow = wid ^ 4;
          const f32x4 st = *(const LAS f32x4*)(lds + st0 + ow * 1024 + lane * 16);
          const float mn = fmaxf(m, st[0]), f0 = EX2(m - mn), f1 = EX2(st[0] - mn);
          const float gg = G ? st[2] : gate;
          const float inv = gg / (l * f0 + st[1] * f1); const float c0 = f0 * inv, c1 = f1 * inv;
          const lds_cptr xp = lds + L_V + ow * 8192 + lane * 16;
          const char* dst = A3 + (size_t)(bA * 576 + hh_ * 16 + G * 8) * 2048;
#pragma unroll
          for (int c = 0; c < 2; ++c) {
              float v[16];
#pragma unroll
              for (int g = 0; g < 4; ++g) { const f32x4 t = *(const LAS f32x4*)(xp + (c * 4 + g) * 1024);
#pragma unroll
                  for (int e = 0; e < 4; ++e) v[4 * g + e] = (G ? o[2 + c][4 * g + e] : o[c][4 * g + e]) * c0 + t[e] * c1; }
              { const bf16x8 t0 = PACK8(v, 0), t1 = PACK8(v, 8); ST_SC1(dst + c * 8192, vod, 0, t0); ST_SC1(dst + c * 8192 + 4096, vod, 0, t1); }
          }
          float kvv[2][8];
          const int col = r32 < 16 ? r32 : 16;
#pragma unroll
          for (int s = 0; s < 2; ++s)
#pragma unroll
              for (int j = 0; j < 8; ++j) { const int f = 16 * s + 8 * (j >> 2) + 4 * hi + (j & 3); const float v = *(const LAS float*)(lds + st0 + 8192 + (f * 32 + col) * 4); kvv[s][j] = r32 <= 16 ? v : 0.f; }
          f32x16 nm = MFMA32(PACK8(kvv[0], 0), phq[0], f32x16{});
          nm = MFMA32(PACK8(kvv[1], 0), phq[1], nm);
          const float den = bfround(bfround(lo32_bcast(nm[8])) + 1e-6f); const float sc = (1.f - gate) / den;
          float ov[8];
#pragma unroll
          for (int r = 0; r < 8; ++r) ov[r] = bfround(nm[r]) * sc;
          const bf16x8 t0 = PACK8(ov, 0);
          ST_SC1(A3 + (size_t)((G ? bB : bA) * 576 + 512 + 2 * hh_) * 2048, vod, 0, t0);
        }
        WAIT_L(0); BAR(); asm volatile("" ::: "memory");
        STAMP();
    }
#undef KRD
#undef VRD
#undef PF
#undef DMA1
#undef ROT
#undef GOFF
#undef PREP_A
#undef PREP_B
#undef PREP_C
#undef PREP_D
#undef GAPA
#undef PVM
#undef RDB
#undef ROUND
#undef ROUND0
#undef DRAIN
    }
    {
        LANE_VARS();
        const int rt = 4 * (bid & 7) + (bid >> 6), ct = (bid >> 3) & 7;
        const char* gA = A3 + (size_t)((rt >> 1) * 576 + 2 * wid) * 2048 + (rt & 1) * 1024;
        const char* gW3 = ws + WS_W3 + (size_t)(ct * 288 + wid) * 2048;
        const unsigned wb = lds0 + wid * 20480;
        const unsigned* rdy = CNT + (64 + (rt >> 1)) * 64;
        auto issue = [&](int u, unsigned slot) {
            const char* ga = gA + (size_t)u * 32768; const char* gw = gW3 + (size_t)u * 16384;
            GLDS_IMM(ga, vlin, wb + slot, 0); GLDS_IMM(ga + 1024, vlin, wb + slot, 1024); GLDS_IMM(gw, vlin, wb + slot + 2048, 0); GLDS_IMM(gw, vlin, wb + slot + 2048, 1024); };
        unsigned seen, seenB; int hnext = 0;
        auto poll = [&]() { const unsigned long long w = __hip_atomic_load((const unsigned long long*)rdy, RLX_AGENT); seen = rfl((unsigned)w); seenB = rfl((unsigned)(w >> 32)); };
        auto next_head = [&]() -> int {
            unsigned spins = 0;
            while (hnext < 32 ? !((seen >> hnext) & 1u) : seenB != ~0u) { poll(); if (++spins > (1u << 20)) break; if (spins > 1) __builtin_amdgcn_s_sleep(2); }
            asm volatile("" ::: "memory");
            return hnext++; };
        poll();
        asm volatile("s_waitcnt vmcnt(0)\n\ts_barrier" ::: "memory");
        if (tid == 0) { __hip_atomic_fetch_or(CNT + (64 + bA) * 64, 1u << hh_, RLX_AGENT); __hip_atomic_fetch_or(CNT + (64 + bB) * 64, 1u << hh_, RLX_AGENT);
                        __hip_atomic_fetch_or(CNT + (64 + bA) * 64 + 1, 1u << hh_, RLX_AGENT); __hip_atomic_fetch_or(CNT + (64 + bB) * 64 + 1, 1u << hh_, RLX_AGENT); }
        STAMP();
#pragma unroll
        for (int i = 0; i < 5; ++i) issue(next_head(), i * 4096u);
        f32x16 acc[2][2] = {};
        const unsigned ao = (unsigned)(wid * 20480 + hi * 1024 + r32 * 16);
        unsigned bo[2];
#pragma unroll
        for (int nb = 0; nb < 2; ++nb) bo[nb] = (unsigned)(wid * 20480 + 2048 + (4 * hi + q4) * 128 + 64 * (nb ^ ((q4 >> 1) & 1)) + 32 * blk + 8 * p4);
        bf16x8 af[2][2], bf[2][2];
#define K3RD(buf, sb) do { af[buf][0] = ldsr128((sb) + ao); bf[buf][0] = cat8(trrd((sb) + bo[0]), trrd((sb) + bo[0] + 1024)); \
        af[buf][1] = ldsr128((sb) + ao + 512); bf[buf][1] = cat8(trrd((sb) + bo[1]), trrd((sb) + bo[1] + 1024)); } while (0)
#define K3BODY(buf, i) do { \
        const unsigned snx = sc == 16384u ? 0u : sc + 4096u; \
        if ((i) + 1 < 36) K3RD(buf ^ 1, lds + snx); \
        SBAR(); acc[0][0] = MFMA32(bf[buf][0], af[buf][0], acc[0][0]); acc[1][0] = MFMA32(bf[buf][0], af[buf][1], acc[1][0]); SBAR(); \
        if ((i) + 5 < 36) issue(next_head(), sc); \
        SBAR(); acc[0][1] = MFMA32(bf[buf][1], af[buf][0], acc[0][1]); acc[1][1] = MFMA32(bf[buf][1], af[buf][1], acc[1][1]); SBAR(); \
        sc = snx; } while (0)
#define K3W(n) asm volatile("s_waitcnt vmcnt(" #n ") lgkmcnt(0)" ::: "memory")
        unsigned sc = 0u;
        WAIT_V(16);
        K3RD(0, lds);
        for (int i = 0; i < 32; i += 2) {
            if (G) __builtin_amdgcn_s_setprio(1); K3W(12); K3BODY(0, i);
            if (G) __builtin_amdgcn_s_setprio(0); K3W(12); K3BODY(1, i + 1); }
        K3W(8); K3BODY(0, 32); K3W(4); K3BODY(1, 33); K3W(0); K3BODY(0, 34); K3W(0); K3BODY(1, 35);
#undef K3RD
#undef K3BODY
#undef K3W
        STAMP();
        BAR(); asm volatile("" ::: "memory");
#pragma unroll
        for (int mi = 0; mi < 2; ++mi)
#pragma unroll
            for (int nb = 0; nb < 2; ++nb)
#pragma unroll
                for (int g = 0; g < 4; ++g) { const int row = 32 * mi + r32, ch = 8 * nb + 2 * g + hi;
                    *(LAS f32x4*)(lds + wid * 16384 + row * 256 + 16 * (ch ^ (row & 15))) = (f32x4){acc[mi][nb][4 * g], acc[mi][nb][4 * g + 1], acc[mi][nb][4 * g + 2], acc[mi][nb][4 * g + 3]}; }
        WAIT_L(0); BAR(); asm volatile("" ::: "memory");
        { const int row = tid >> 3, c8 = tid & 7; f32x4 s0 = {}, s1 = {};
#pragma unroll
          for (int w = 0; w < 8; ++w) { s0 += *(const LAS f32x4*)(lds + w * 16384 + row * 256 + 16 * ((2 * c8) ^ (row & 15)));
                                        s1 += *(const LAS f32x4*)(lds + w * 16384 + row * 256 + 16 * ((2 * c8 + 1) ^ (row & 15))); }
          u32x4 w = {cvtpk(s0[0], s0[1]), cvtpk(s0[2], s0[3]), cvtpk(s1[0], s1[1]), cvtpk(s1[2], s1[3])};
          *(u32x4*)(out + (size_t)(rt * 64 + row) * 512 + ct * 64 + c8 * 8) = w; }
    }
    STAMP(); STAMP_RT();
    STAMP_OUT(ws + WS_END);
}

extern "C" void kernel_launch(void* const* d_in, const int* in_sizes, int n_in, void* d_out, int out_size, void* d_ws, size_t ws_size,
                              hipStream_t stream) {
    (void)in_sizes; (void)n_in; (void)out_size; (void)ws_size;
    char* ws = (char*)d_ws;
    (void)hipFuncSetAttribute((const void*)kf_all, hipFuncAttributeMaxDynamicSharedMemorySize, LDS_BYTES);
    kf_all<<<256, 512, LDS_BYTES, stream>>>((const u16*)d_in[0], (const u16*)d_in[1], (const u16*)d_in[2], (const u16*)d_in[3], (const u16*)d_in[4], (const u16*)d_in[5],
                                            (const u16*)d_in[7], (const u16*)d_in[8], (const u16*)d_in[9], (const u16*)d_in[10], ws, (u16*)d_out);
}
```

```cpp
#include <hip/hip_runtime.h>
#include <hip/hip_bf16.h>
#include <stdint.h>
#define PD 4
#define CODEWARM 1
#define PREFETCH 1

typedef unsigned short u16;
typedef short bf16x8 __attribute__((ext_vector_type(8)));
typedef short s16x4 __attribute__((ext_vector_type(4)));
typedef float f32x2 __attribute__((ext_vector_type(2)));
typedef float f32x4 __attribute__((ext_vector_type(4)));
typedef float f32x16 __attribute__((ext_vector_type(16)));
typedef unsigned u32x4 __attribute__((ext_vector_type(4)));
typedef unsigned u32x2 __attribute__((ext_vector_type(2)));
typedef __bf16 bf16x2_t __attribute__((ext_vector_type(2)));
#define LAS __attribute__((address_space(3)))
#define DI __device__ __forceinline__
typedef LAS const char* lds_cptr;
typedef LAS char* lds_ptr;

constexpr int SEQ = 2048, DM = 512, NH = 32, HD = 128;
constexpr size_t WS_CNT = 0;
constexpr size_t WS_HQ = 32768, WS_HR = 33792;
constexpr size_t WS_KV = 65536;
constexpr size_t WS_A3 = WS_KV + (size_t)NH * 32 * 32768;
constexpr size_t WS_KVP = WS_A3 + (size_t)576 * SEQ * 16;
constexpr size_t WS_QB = WS_KVP + (size_t)NH * 8 * 4096;
constexpr size_t WS_PQ = WS_QB + (size_t)256 * 32768;
constexpr size_t WS_W3 = WS_PQ + (size_t)256 * 16384;
constexpr size_t WS_END = WS_W3 + (size_t)8 * 288 * 2048;
constexpr int LDS_BYTES = 163840;

#define MFMA32(a, b, c) __builtin_amdgcn_mfma_f32_32x32x16_bf16((a), (b), (c), 0, 0, 0)
#define MFMA16(a, b, c) __builtin_amdgcn_mfma_f32_16x16x32_bf16((a), (b), (c), 0, 0, 0)
#define SBAR() __builtin_amdgcn_sched_barrier(0)
#define WAIT_V(n) asm volatile("s_waitcnt vmcnt(" #n ")" ::: "memory")
#define WAIT_L(n) asm volatile("s_waitcnt lgkmcnt(" #n ")" ::: "memory")
#define BAR() __builtin_amdgcn_s_barrier()
#define WAIT_BAR(N) asm volatile("s_waitcnt vmcnt(" #N ") lgkmcnt(0)\n\ts_barrier" ::: "memory")

#define STAMP_DECL()
#define STAMP()
#define STAMP_RT()
#define STAMP_OUT(base)
#define STAMP_PARAM
#define STAMP_ARG
DI unsigned rfl(unsigned x) { return __builtin_amdgcn_readfirstlane(x); }
DI unsigned cvtpk(float lo, float hi) { f32x2 v = {lo, hi}; bf16x2_t b = __builtin_convertvector(v, bf16x2_t); return __builtin_bit_cast(unsigned, b); }
DI float bfround(float x) { return __uint_as_float(cvtpk(x, x) << 16); }
DI void glds16(const void* sbase, unsigned voff, unsigned lds_dst) {
    unsigned sv;
    asm volatile("s_mov_b32 %0, m0\n\ts_mov_b32 m0, %3\n\ts_nop 2\n\tglobal_load_lds_dwordx4 %1, %2\n\ts_mov_b32 m0, %0"
                 : "=&s"(sv) : "v"(voff), "s"(sbase), "s"(lds_dst) : "memory");
}
DI void glds16c(const void* sbase, unsigned voff, unsigned lds_dst) {
    unsigned sv;
    asm volatile("s_mov_b32 %0, m0\n\ts_mov_b32 m0, %3\n\ts_nop 2\n\tglobal_load_lds_dwordx4 %1, %2 sc1\n\ts_mov_b32 m0, %0"
                 : "=&s"(sv) : "v"(voff), "s"(sbase), "s"(lds_dst) : "memory");
}
DI void touch8k(const void* sbase, unsigned voff, unsigned lds_dst) {
    unsigned sv;
    asm volatile("s_mov_b32 %0, m0\n\ts_mov_b32 m0, %3\n\ts_nop 2\n\tglobal_load_lds_dword %1, %2\n\ts_mov_b32 m0, %0"
                 : "=&s"(sv) : "v"(voff), "s"(sbase), "s"(lds_dst) : "memory");
}
DI void glds16c_unless(const void* sbase, unsigned voff, unsigned lds_dst, int a, int b) {
    unsigned sv;
    asm volatile("s_cmp_lt_i32 %4, %5\n\ts_cselect_b64 exec, 0, -1\n\ts_mov_b32 %0, m0\n\ts_mov_b32 m0, %3\n\ts_nop 2\n\tglobal_load_lds_dwordx4 %1, %2 sc1\n\ts_mov_b32 m0, %0\n\ts_mov_b64 exec, -1"
                 : "=&s"(sv) : "v"(voff), "s"(sbase), "s"(lds_dst), "s"(a), "s"(b) : "memory", "scc");
}
DI void ldq_unless(bf16x8* q, const void* sbase, unsigned voff, unsigned skip) {
    asm volatile("s_cmp_lg_u32 %10, 0\n\ts_cbranch_scc0 .Lrare%=\n.Lback%=:\n\t.subsection 1\n.Lrare%=:\n\ts_nop 4\n\t"
                 "global_load_dwordx4 %0, %8, %9 offset:-4096\n\tglobal_load_dwordx4 %1, %8, %9 offset:-3072\n\tglobal_load_dwordx4 %2, %8, %9 offset:-2048\n\tglobal_load_dwordx4 %3, %8, %9 offset:-1024\n\t"
                 "global_load_dwordx4 %4, %8, %9\n\tglobal_load_dwordx4 %5, %8, %9 offset:1024\n\tglobal_load_dwordx4 %6, %8, %9 offset:2048\n\tglobal_load_dwordx4 %7, %8, %9 offset:3072\n\ts_branch .Lback%=\n\t.subsection 0"
                 : "+v"(q[0]), "+v"(q[1]), "+v"(q[2]), "+v"(q[3]), "+v"(q[4]), "+v"(q[5]), "+v"(q[6]), "+v"(q[7]) : "v"(voff), "s"((const char*)sbase + 4096), "s"(skip) : "memory", "scc");
}
DI void glds4c(const void* sbase, unsigned lds_dst) {
    unsigned sv, z;
    asm volatile("v_mov_b32 %1, 0\n\ts_mov_b32 %0, m0\n\ts_mov_b32 m0, %3\n\ts_nop 2\n\tglobal_load_lds_dword %1, %2 sc1\n\ts_mov_b32 m0, %0"
                 : "=&s"(sv), "=&v"(z) : "s"(sbase), "s"(lds_dst) : "memory");
}
#define GLDS_IMM(sbase, voff, lds_dst, IMM) asm volatile("s_mov_b32 m0, %2\n\ts_nop 3\n\tglobal_load_lds_dwordx4 %0, %1 offset:%3 sc1" :: "v"(voff), "s"(sbase), "s"(lds_dst), "i"(IMM) : "memory")
#define GLDS_IMM_UNLESS(sbase, voff, lds_dst, IMM, a, b) asm volatile("s_cmp_lt_i32 %4, %5\n\ts_cselect_b64 exec, 0, -1\n\ts_mov_b32 m0, %2\n\ts_nop 1\n\tglobal_load_lds_dwordx4 %0, %1 offset:%3 sc1\n\ts_mov_b64 exec, -1" \
        :: "v"(voff), "s"(sbase), "s"(lds_dst), "i"(IMM), "s"(a), "s"(b) : "memory", "scc")
typedef short v4i16_t __attribute__((ext_vector_type(4)));
DI s16x4 trrd(lds_cptr p) { return __builtin_bit_cast(s16x4, __builtin_amdgcn_ds_read_tr16_b64_v4i16((LAS v4i16_t*)p)); }
DI bf16x8 cat8(s16x4 a, s16x4 b) { return __builtin_shufflevector(a, b, 0, 1, 2, 3, 4, 5, 6, 7); }
DI bf16x8 ldsr128(lds_cptr p) { return *(const LAS bf16x8*)p; }
DI bf16x8 pack8(float a0, float a1, float a2, float a3, float a4, float a5, float a6, float a7) {
    u32x4 w = {cvtpk(a0, a1), cvtpk(a2, a3), cvtpk(a4, a5), cvtpk(a6, a7)}; return __builtin_bit_cast(bf16x8, w); }
#define PACK8(X, B) pack8((X)[(B)], (X)[(B) + 1], (X)[(B) + 2], (X)[(B) + 3], (X)[(B) + 4], (X)[(B) + 5], (X)[(B) + 6], (X)[(B) + 7])
DI float swap32_max(float v) { auto rr = __builtin_amdgcn_permlane32_swap(__float_as_uint(v), __float_as_uint(v), false, false); return fmaxf(__uint_as_float(rr[0]), __uint_as_float(rr[1])); }
DI float swap32_add(float v) { auto rr = __builtin_amdgcn_permlane32_swap(__float_as_uint(v), __float_as_uint(v), false, false); return __uint_as_float(rr[0]) + __uint_as_float(rr[1]); }
DI float lo32_bcast(float v) { auto rr = __builtin_amdgcn_permlane32_swap(__float_as_uint(v), __float_as_uint(v), false, false); return __uint_as_float(rr[0]); }
template <int CTRL> DI float dpp(float v) { return __uint_as_float(__builtin_amdgcn_update_dpp(0u, __float_as_uint(v), CTRL, 0xF, 0xF, true)); }
DI float row16_max(float v) { v = fmaxf(v, dpp<0xB1>(v)); v = fmaxf(v, dpp<0x4E>(v)); v = fmaxf(v, dpp<0x141>(v)); v = fmaxf(v, dpp<0x140>(v)); return v; }
DI float row16_add(float v) { v += dpp<0xB1>(v); v += dpp<0x4E>(v); v += dpp<0x141>(v); v += dpp<0x140>(v); return v; }

constexpr float QSCALE = 0.08838834764831845f * 1.4426950408889634f;
constexpr float THR = 8.f;
constexpr int L_V = 98304;
constexpr int L_WE = 65536, L_GATE = 139264;
#define PIN(x) asm volatile("" : "+v"(x))
#define MX3(a, b, c) __builtin_fmaxf(__builtin_fmaxf((a), (b)), (c))
#define EX2(v) __builtin_amdgcn_exp2f(v)
#define RLX_AGENT __ATOMIC_RELAXED, __HIP_MEMORY_SCOPE_AGENT
#define ST_PL(base, voff, imm, v) asm volatile("s_nop 4\n\tglobal_store_dwordx4 %0, %1, %2 offset:%3\n\ts_nop 1" :: "v"(voff), "v"(v), "s"(base), "i"(imm) : "memory")
#define ST_SC1(base, voff, imm, v) asm volatile("s_nop 4\n\tglobal_store_dwordx4 %0, %1, %2 offset:%3 sc1\n\ts_nop 1" :: "v"(voff), "v"(v), "s"(base), "i"(imm) : "memory")
DI int fresh_lane() { int l; asm volatile("v_mbcnt_lo_u32_b32 %0, -1, 0\n\tv_mbcnt_hi_u32_b32 %0, -1, %0" : "=v"(l)); return l; }
DI void wait_cnt(const unsigned* p, unsigned want) {
    unsigned spins = 0;
    while (rfl(__hip_atomic_load(p, RLX_AGENT)) < want) { __builtin_amdgcn_s_sleep(2); if (++spins > (1u << 20)) break; }
}
DI void handshake_finish(char* ws, int bid, unsigned token, bool ok, int lane) {
    unsigned* CNT = (unsigned*)(ws + WS_CNT); unsigned spins = 0;
    if (bid == 0) {
        for (;;) {
            const unsigned a = __hip_atomic_load(CNT + (80 + (lane & 15)) * 64, RLX_AGENT);
            if (__all(a >= ((lane & 15) ? 16u : 15u))) break;
#pragma unroll
            for (int i = 0; i < 4; ++i) { const unsigned q = __hip_atomic_load((const unsigned*)(ws + WS_HQ) + i * 64 + lane, RLX_AGENT); __hip_atomic_store((unsigned*)(ws + WS_HR) + i * 64 + lane, q, RLX_AGENT); }
            __builtin_amdgcn_s_sleep(8); if (++spins > (1u << 18)) break; }
    } else if (!ok) {
        while (rfl(__hip_atomic_load((const unsigned*)(ws + WS_HR) + bid, RLX_AGENT)) != token) { __builtin_amdgcn_s_sleep(8); if (++spins > (1u << 20)) break; }
        if (lane == 0) __hip_atomic_fetch_add(CNT + (80 + (bid & 15)) * 64, 1u, RLX_AGENT);
    }
}
DI void dual_softmax(f32x16& p) {
#pragma unroll
    for (int hf = 0; hf < 2; ++hf) {
        float mx = -INFINITY;
#pragma unroll
        for (int r = 0; r < 8; ++r) { p[8 * hf + r] = bfround(p[8 * hf + r]); mx = fmaxf(mx, p[8 * hf + r]); }
        mx = swap32_max(mx); float s = 0.f;
#pragma unroll
        for (int r = 0; r < 8; ++r) { p[8 * hf + r] = EX2((p[8 * hf + r] - mx) * 1.4426950408889634f); s += p[8 * hf + r]; }
        s = __builtin_amdgcn_rcpf(swap32_add(s));
#pragma unroll
        for (int r = 0; r < 8; ++r) p[8 * hf + r] *= s;
    }
}
DI void mask_half(f32x16& sc, int dqw) {
    if (dqw < 0) {
#pragma unroll
        for (int r = 0; r < 16; ++r) sc[r] = -INFINITY;
    } else if (dqw == 0) {
        const int ln = fresh_lane(); const int lr = (ln & 31) - 4 * (ln >> 5); const float ninf = -INFINITY;
#pragma unroll
        for (int r = 0; r < 16; ++r)
            asm volatile("s_nop 0\n\tv_cmp_gt_i32 vcc, %c3, %2\n\tv_cndmask_b32 %0, %0, %1, vcc" : "+v"(sc[r]) : "v"(ninf), "v"(lr), "i"(16 * ((r >> 2) & 1) + 8 * (r >> 3) + (r & 3)) : "vcc");
    }
}
DI void mask_tile(f32x16& s0, f32x16& s1, int dqw) {
    asm volatile("s_nop 7\n\ts_nop 7" ::: "memory");
    mask_half(s0, dqw); mask_half(s1, dqw - 32); }

__global__ __launch_bounds__(512, 2) void kf_all(const u16* __restrict__ x, const u16* __restrict__ ckv, const u16* __restrict__ wq, const u16* __restrict__ wuk,
                                                 const u16* __restrict__ wuv, const u16* __restrict__ wo_a, const u16* __restrict__ wqkv,
                                                 const u16* __restrict__ wphi, const u16* __restrict__ wo_b, const u16* __restrict__ gatew,
                                                 char* ws, u16* __restrict__ out) {
    extern __shared__ __attribute__((aligned(16))) char shm[];
    const lds_ptr lds = (lds_ptr)shm; const unsigned lds0 = (unsigned)(uintptr_t)shm;
    const int wid = rfl(threadIdx.x >> 6), G = wid >> 2, w4 = wid & 3;
    const int bid = blockIdx.x, hh_ = 4 * (bid & 7) + (bid >> 6), jj = (bid >> 3) & 7;
#define LANE_VARS() int tid = threadIdx.x; asm volatile("" : "+v"(tid)); const int lane = tid & 63, r32 = lane & 31, hi = lane >> 5, blk = (lane >> 4) & 1, \
        q4 = (lane & 15) >> 2, p4 = lane & 3; const unsigned vlin = (unsigned)lane * 16u; (void)r32; (void)hi; (void)blk; (void)q4; (void)p4; (void)vlin
    unsigned* const CNT = (unsigned*)(ws + WS_CNT);
    char* const KVh = ws + WS_KV + (size_t)hh_ * 32 * 32768;
    char* const A3 = ws + WS_A3;
    STAMP_DECL(); STAMP_RT(); STAMP();
    u32x4 pkv[16];
    unsigned token = 0u;
    const int bA = 15 - jj, bB = jj;
    const char* gX = (const char*)x + (size_t)((G ? bB : bA) * 128 + 32 * w4) * 1024; const char* gW = (const char*)wq + hh_ * 256; const char* gE = (const char*)wqkv + hh_ * 32;
    const unsigned lx = lds0 + wid * 4096, lw = lds0 + L_WE + wid * 1024;
    unsigned voffX, voffW, voffE;
    { const int lane = threadIdx.x & 63;
        { const int row = lane >> 3, ch = (lane & 7) ^ ((row >> 1) & 3); voffX = (unsigned)(row * 1024 + ch * 16); }
        { const int row = 4 * wid + (lane >> 4), j = lane & 15, c = (j >> 2) ^ (row & 3); voffW = (unsigned)(row * 8192 + c * 64 + (j & 3) * 16); }
        { const int row = 8 * wid + (lane >> 3), sl = lane & 7, hf = (sl >> 2) ^ ((row >> 1) & 1), part = hf ? 2 : ((sl >> 1) & 1);
          voffE = (unsigned)(row * 3072 + part * 1024 + (sl & 1) * 16); }
        if (wid == 0) glds16(gatew, (unsigned)lane * 16u, lds0 + L_GATE);
        if (bid == 0 && wid == 1) {
            const unsigned z = 0u;
            asm volatile("global_store_dword %0, %1, %2 sc1\n\tglobal_store_dword %0, %1, %3 sc1\n\ts_nop 1" :: "v"((unsigned)lane * 256u), "v"(z), "s"(ws + WS_CNT), "s"(ws + WS_CNT + 16384) : "memory");
            asm volatile("global_store_dword %0, %1, %2 offset:4 sc1\n\ts_nop 1" :: "v"((unsigned)lane * 256u), "v"(z), "s"(ws + WS_CNT + 16384) : "memory"); }
        PIN(voffX); PIN(voffW); PIN(voffE);
    }
#define XISS(c) do { _Pragma("unroll") for (int e = 0; e < 4; ++e) glds16(gX + e * 8192 + (c) * 128, voffX ^ ((e & 1) << 6), lx + ((c) & 1) * 32768 + e * 1024); } while (0)
#define WISS(c) do { glds16(gW + (size_t)(c) * 524288, voffW, lw + ((c) % 3) * 24576); glds16(gW + (size_t)(c) * 524288 + 262144, voffW, lw + ((c) % 3) * 24576 + 8192); \
                     glds16(gE + (size_t)(c) * 196608, voffE, lw + ((c) % 3) * 24576 + 16384); } while (0)
    {
        LANE_VARS();
        const int wr = wid >> 2, wc = wid & 3, fr = lane & 15, fq = lane >> 4;
        constexpr int HTB = 16384;
#define SA(b, h) (((b) * 2 + (h)) * HTB)
#define SB(b, h) ((4 + (b) * 2 + (h)) * HTB)
        unsigned voffA, voffB;
        { const int row = 8 * wid + (lane >> 3), ch = (lane & 7) ^ ((row >> 1) & 7); voffA = (unsigned)(row * 1024 + ch * 16); }
        { const int row = 4 * wid + (lane >> 4), j = lane & 15, s = ((row >> 3) & 1) * 4 + (row & 3), c = (j >> 1) ^ s; voffB = (unsigned)(row * 8192 + c * 32 + (j & 1) * 16); }
        const char* gA = (const char*)ckv + (size_t)jj * 256 * 1024;
        const char* gB0 = (const char*)wuk + hh_ * 256; const char* gB1 = (const char*)wuv + hh_ * 256;
        const unsigned ldsw = rfl(lds0 + wid * 1024);
#define STAGE_A(b, h, kt) do { const char* g_ = gA + (h) * 131072 + (kt) * 128; glds16(g_, voffA, ldsw + SA(b, h)); glds16(g_ + 65536, voffA, ldsw + SA(b, h) + 8192); } while (0)
#define STAGE_B(b, h, kt) do { const char* g_ = ((h) ? gB1 : gB0) + (size_t)(kt) * 524288; glds16(g_, voffB, ldsw + SB(b, h)); glds16(g_ + 262144, voffB, ldsw + SB(b, h) + 8192); } while (0)
        lds_cptr pa[2], pb[2];
#pragma unroll
        for (int ks = 0; ks < 2; ++ks) pa[ks] = lds + (wr * 64 + fr) * 128 + 16 * ((4 * ks + fq) ^ ((fr >> 1) & 7));
        { const int q = fr >> 2, p = fr & 3, s = (fq & 1) * 4 + q;
#pragma unroll
          for (int n = 0; n < 2; ++n) pb[n] = lds + (8 * fq + q) * 256 + 32 * ((2 * wc + n) ^ s) + 8 * p; }
#define LDA(dst, b, h) do { _Pragma("unroll") for (int m = 0; m < 4; ++m) _Pragma("unroll") for (int k = 0; k < 2; ++k) dst[m][k] = ldsr128(pa[k] + SA(b, h) + m * 2048); } while (0)
#define LDB(dst, b, h) do { _Pragma("unroll") for (int n = 0; n < 2; ++n) _Pragma("unroll") for (int k = 0; k < 2; ++k) \
        dst[n][k] = cat8(trrd(pb[n] + SB(b, h) + k * 8192), trrd(pb[n] + SB(b, h) + k * 8192 + 1024)); } while (0)
#define MMA(ai, bj, At, Bt) do { __builtin_amdgcn_s_setprio(1); _Pragma("unroll") for (int m = 0; m < 4; ++m) _Pragma("unroll") for (int n = 0; n < 2; ++n) _Pragma("unroll") for (int k = 0; k < 2; ++k) \
        acc[ai][bj][m][n] = (bj) ? MFMA16(At[m][k], Bt[n][k], acc[ai][bj][m][n]) : MFMA16(Bt[n][k], At[m][k], acc[ai][bj][m][n]); __builtin_amdgcn_s_setprio(0); } while (0)
        const unsigned scr = lds0 + 143360 + wid * 256; const size_t gw = (size_t)(bid * 8 + wid);
        auto warm = [&](int slot) {
            if (slot == 0 && wid == 0) { token = rfl(*(const LAS unsigned*)(lds + 148480)) + 1u; const unsigned z = 0u, tk = token;
                asm volatile("s_nop 4\n\tglobal_store_dword %0, %1, %2 sc1\n\ts_nop 1" :: "v"(z), "v"(tk), "s"(ws + WS_HQ + bid * 4) : "memory"); }
            if (bid == 0 && wid == 1) {
                if (slot > 0) { const u32x4 rq = *(const LAS u32x4*)(lds + 147456 + lane * 16); ST_SC1(ws + WS_HR, (unsigned)lane * 16u, 0, rq); WAIT_L(0); }
                glds16c(ws + WS_HQ, (unsigned)lane * 16u, lds0 + 147456); }
            const unsigned vo = (unsigned)lane * 128u;
            if (slot == 0) { if (lane < 8) touch8k((const char*)x + gw * 1024, vo, scr); }
            else if (slot == 1) { if (lane < 16) touch8k((const char*)wq + gw * 2048, vo, scr); }
            else if (slot == 2) { if (lane < 6) touch8k((const char*)wqkv + gw * 768, vo, scr); }
            if (slot == 3) {
                const int row = lane >> 3, sl = lane & 7, nb = (sl >> 2) ^ ((row >> 1) & 1); const unsigned vw = (unsigned)(row * 1024 + nb * 64 + (sl & 3) * 16);
                const char* g = (const char*)wo_a + (size_t)(8 * hh_ + wid) * 16384 + jj * 128;
                glds16(g, vw, lds0 + 149504 + wid * 1024); glds16(g + 8192, vw, lds0 + 131072 + wid * 1024);
                if (wid < 2) glds16((const char*)wo_b + (size_t)hh_ * 16384 + wid * 8192 + jj * 128, vw, lds0 + 157696 + wid * 1024);
            } else if (slot == 4) {
                char* d = ws + WS_W3 + (size_t)(jj * 288 + 8 * hh_ + wid) * 2048; const unsigned vl = (unsigned)lane * 16u;
                { const u32x4 t0 = *(const LAS u32x4*)(lds + 149504 + wid * 1024 + lane * 16); ST_SC1(d, vl, 0, t0); }
                { const u32x4 t1 = *(const LAS u32x4*)(lds + 131072 + wid * 1024 + lane * 16); ST_SC1(d, vl, 1024, t1); }
                if (wid < 2) { const u32x4 t2 = *(const LAS u32x4*)(lds + 157696 + wid * 1024 + lane * 16); ST_SC1(ws + WS_W3 + (size_t)(jj * 288 + 256 + hh_) * 2048 + wid * 1024, vl, 0, t2); }
                WAIT_L(0);
            }
        };
        f32x4 acc[2][2][4][2] = {};
        bf16x8 At[4][2], B0[2][2], B1[2][2];
        constexpr int nt = 8;
        STAGE_B(0, 0, 0); STAGE_A(0, 0, 0); STAGE_B(0, 1, 0); STAGE_A(0, 1, 0);
        if (wid == 3) {
            unsigned long long pc; unsigned len; asm volatile("s_getpc_b64 %0\n.Lcw_start:\n\ts_mov_b32 %1, .Lcw_end-.Lcw_start" : "=s"(pc), "=s"(len));
            const unsigned off = (unsigned)((bid >> 3) * 13 + lane) * 128u;
            if (lane < 13) touch8k((const char*)pc, off < len ? off : 0u, scr); }
        if (wid == 0) glds4c(ws + WS_HR + bid * 4, lds0 + 148480);
        if (wr == 1) BAR();
        if (wid == 0 || (CODEWARM && wid == 3)) { WAIT_V(5); } else { WAIT_V(4); } BAR();
        STAGE_B(1, 0, 1); STAGE_A(1, 0, 1); STAGE_B(1, 1, 1);
        if (wid == 0 || (CODEWARM && wid == 3)) { WAIT_V(7); } else { WAIT_V(6); } BAR();
        { const unsigned vo = (unsigned)lane * 128u;
          if (lane < 16) { touch8k((const char*)wuk + gw * 2048, vo, scr); touch8k((const char*)wuv + gw * 2048, vo, scr); }
          if (lane < 8) touch8k((const char*)ckv + gw * 1024, vo, scr); }
        STAMP();
        for (int t = 0; t < nt - 2; t += 2) {
            LDB(B0, 0, 0); SBAR(); LDA(At, 0, 0); STAGE_A(1, 1, t + 1);
            WAIT_L(8); BAR(); WAIT_L(0); MMA(0, 0, At, B0); BAR(); SBAR();
            LDB(B1, 0, 1); STAGE_B(0, 0, t + 2);
            BAR(); WAIT_L(0); MMA(0, 1, At, B1); BAR();
            LDA(At, 0, 1); STAGE_A(0, 0, t + 2);
            BAR(); WAIT_L(0); MMA(1, 0, At, B0); BAR(); SBAR();
            STAGE_B(0, 1, t + 2);
            WAIT_V(6); warm(t); BAR(); MMA(1, 1, At, B1); BAR();
            LDB(B0, 1, 0); SBAR(); LDA(At, 1, 0); STAGE_A(0, 1, t + 2);
            WAIT_L(8); BAR(); WAIT_L(0); MMA(0, 0, At, B0); BAR(); SBAR();
            LDB(B1, 1, 1); STAGE_B(1, 0, t + 3);
            BAR(); WAIT_L(0); MMA(0, 1, At, B1); BAR();
            LDA(At, 1, 1); STAGE_A(1, 0, t + 3);
            BAR(); WAIT_L(0); MMA(1, 0, At, B0); BAR(); SBAR();
            STAGE_B(1, 1, t + 3);
            WAIT_V(6); warm(t + 1); BAR(); MMA(1, 1, At, B1); BAR();
        }
        { LDB(B0, 0, 0); WAIT_V(0); LDA(At, 0, 0); STAGE_A(1, 1, nt - 1);
          BAR(); WAIT_L(0); MMA(0, 0, At, B0); BAR();
          LDB(B1, 0, 1); BAR(); WAIT_L(0); MMA(0, 1, At, B1); BAR();
          LDA(At, 0, 1); BAR(); WAIT_L(0); MMA(1, 0, At, B0); MMA(1, 1, At, B1); BAR(); }
        { LDB(B0, 1, 0); LDA(At, 1, 0); BAR(); WAIT_L(0); MMA(0, 0, At, B0); BAR();
          LDB(B1, 1, 1); WAIT_V(0);
          WISS(0); XISS(0);
          BAR(); WAIT_L(0); MMA(0, 1, At, B1); BAR();
          LDA(At, 1, 1); BAR(); WAIT_L(0); MMA(1, 0, At, B0); MMA(1, 1, At, B1); BAR(); }
        if (wr == 0) BAR();
        WISS(1); XISS(1);
        if (wid == 0) glds4c(ws + WS_HR + bid * 4, lds0 + 148480);
        SBAR();
#undef SA
#undef SB
#undef STAGE_A
#undef STAGE_B
#undef LDA
#undef LDB
#undef MMA
#pragma unroll
        for (int ai = 0; ai < 2; ++ai) {
#pragma unroll
            for (int m = 0; m < 4; ++m) { const f32x4 a = acc[ai][0][m][0], b = acc[ai][0][m][1];
                pkv[ai * 4 + m] = (u32x4){cvtpk(a[0], a[1]), cvtpk(a[2], a[3]), cvtpk(b[0], b[1]), cvtpk(b[2], b[3])}; }
#pragma unroll
            for (int u = 0; u < 2; ++u)
#pragma unroll
                for (int n = 0; n < 2; ++n) { const f32x4 a = acc[ai][1][2 * u][n], b = acc[ai][1][2 * u + 1][n];
                    pkv[8 + ai * 4 + u * 2 + n] = (u32x4){cvtpk(a[0], a[1]), cvtpk(a[2], a[3]), cvtpk(b[0], b[1]), cvtpk(b[2], b[3])}; }
        }
    }
    STAMP();
    const char* kvw = KVh + w4 * 4096;
    const unsigned ldw = lds0 + w4 * 4096;
    char* const qbp = ws + WS_QB + (size_t)(bid * 4 + w4) * 8192;
    char* const pqp = ws + WS_PQ + (size_t)(bid * 8 + wid) * 2048;
    bf16x8 qf[8]; float gate;
    {
        LANE_VARS();
        u16 wph[8];
#pragma unroll
        for (int j = 0; j < 8; ++j) wph[j] = wphi[hh_ * 256 + (8 * (j >> 2) + 4 * hi + (j & 3)) * 16 + (lane & 15)];
        const char* tb0 = KVh + (size_t)(4 * jj + G) * 32768;
        const unsigned vok = (unsigned)((4 * w4 + (lane >> 4)) * 1024 + (lane & 15) * 16), vov = (unsigned)(16384 + (lane >> 4) * 2048 + (w4 * 32 + (lane & 15)) * 16);
#define KVST(i) do { if ((i) < 8) ST_SC1(tb0 + ((i) >> 2) * 65536, vok, ((i) & 3) * 256, pkv[i]); \
                     else ST_SC1(tb0 + (((i) >> 2) & 1) * 65536 + (((i) >> 1) & 1) * 8192, vov, ((i) & 1) * 256, pkv[i]); } while (0)
        f32x16 qa[5] = {}, d1 = {};
        unsigned xo[2], wo_[4];
#pragma unroll
        for (int s = 0; s < 2; ++s) xo[s] = (unsigned)((32 * wid + r32) * 128 + 16 * ((2 * s + hi) ^ ((r32 >> 1) & 7)));
#pragma unroll
        for (int c = 0; c < 4; ++c) wo_[c] = (unsigned)(L_WE + (8 * hi + q4) * 256 + 64 * (c ^ q4) + 32 * (p4 >> 1) + 16 * blk + 8 * (p4 & 1));
        const unsigned eo = (unsigned)(L_WE + 16384 + (8 * hi + q4) * 128 + 64 * ((q4 >> 1) & 1) + 32 * blk + 8 * p4);
        const unsigned vo_ = (unsigned)(L_WE + 16384 + (8 * hi + q4) * 128 + 64 * (((q4 >> 1) & 1) ^ 1) + 8 * p4);
        bf16x8 fx[2], fw[2][6], fg[2];
#define QR_(buf, c, ks, u) do { \
        if ((u) == 0) { fx[buf] = ldsr128(lds + ((c) & 1) * 32768 + (xo[(ks) & 1] ^ (((ks) >> 1) << 6))); fg[buf] = ldsr128(lds + L_GATE + ((c) * 64 + (ks) * 16 + 8 * hi) * 2); } \
        else if ((u) < 5) { fw[buf][(u) - 1] = cat8(trrd(lds + wo_[(u) - 1] + ((c) % 3) * 24576 + (ks) * 4096), trrd(lds + wo_[(u) - 1] + ((c) % 3) * 24576 + (ks) * 4096 + 1024)); } \
        else { fw[buf][4] = cat8(trrd(lds + eo + ((c) % 3) * 24576 + (ks) * 2048), trrd(lds + eo + ((c) % 3) * 24576 + (ks) * 2048 + 512)); \
               fw[buf][5] = cat8(trrd(lds + vo_ + ((c) % 3) * 24576 + (ks) * 2048), trrd(lds + vo_ + ((c) % 3) * 24576 + (ks) * 2048 + 512)); } } while (0)
#define QM_(buf, u) do { if ((u) < 5) qa[u] = MFMA32(fw[buf][u], fx[buf], qa[u]); else d1 = MFMA32(fx[buf], blk ? fg[buf] : fw[buf][5], d1); } while (0)
#define QRD(buf, c, ks) do { _Pragma("unroll") for (int u_ = 0; u_ < 6; ++u_) QR_(buf, c, ks, u_); } while (0)
#define QSTEP(buf, c, ks, rd) do { _Pragma("unroll") for (int u_ = 0; u_ < 6; ++u_) { if (rd) QR_((buf) ^ 1, c, ks, u_); QM_(buf, u_); SBAR(); } } while (0)
#define WV_(n) asm volatile("s_waitcnt vmcnt(%0)" :: "i"(n) : "memory")
#define NST(c) ((c) < 0 ? 0 : 2)
#define ST0(c) (2 * (c))
#define QCHUNK(c) do { \
        SBAR(); QSTEP(0, c, 1, true); QSTEP(1, c, 2, true); \
        if ((c) < 7) { WV_(4 + NST((c) - 1)); } BAR(); asm volatile("" ::: "memory"); \
        if ((c) < 6) WISS((c) + 2); \
        _Pragma("unroll") for (int i_ = 0; i_ < NST(c); ++i_) KVST(ST0(c) + i_); \
        SBAR(); QSTEP(0, c, 3, true); \
        if ((c) < 7) { WV_(((c) < 6 ? 3 : 0) + NST(c)); } \
        SBAR(); QSTEP(1, (c) + 1, 0, (c) < 7); \
        if ((c) < 6) XISS((c) + 2); } while (0)
        if (wid == 0) { WAIT_V(8); } else { WAIT_V(7); } BAR(); asm volatile("" ::: "memory");
        bool hs_ok = true;
        bf16x8 wpf;
#pragma unroll
        for (int j = 0; j < 8; ++j) wpf[j] = (short)(blk ? (wph[j] ^ 0x8000) : wph[j]);
        PIN(wpf);
        STAMP();
        QRD(0, 0, 0);
        QCHUNK(0);
        {
            unsigned t0, t1, seen, em;
            asm volatile("v_mov_b32 %0, %5\n\tds_read_b32 %0, %0\n\tv_mov_b32 %1, 1\n\ts_waitcnt lgkmcnt(0)\n\tv_readfirstlane_b32 %2, %0\n\tv_mov_b32 %0, 0\n\ts_nop 3\n\t"
                         "s_min_u32 %3, %8, 1\n\ts_cmp_eq_u32 %7, 0\n\ts_cselect_b32 %3, %3, 0\n\t"
                         "s_cmp_eq_u32 %2, %6\n\ts_cselect_b32 %2, 1, 0\n\ts_and_b32 %3, %3, %2\n\ts_mov_b32 exec_lo, %3\n\ts_mov_b32 exec_hi, 0\n\t"
                         "global_atomic_add %0, %1, %4\n\ts_mov_b64 exec, -1"
                         : "=&v"(t0), "=&v"(t1), "=&s"(seen), "=&s"(em) : "s"(CNT + (80 + (bid & 15)) * 64), "s"(lds0 + 148480), "s"(token), "s"(wid), "s"(bid) : "memory", "scc");
            hs_ok = seen != 0u; }
        QCHUNK(1); QCHUNK(2); QCHUNK(3); QCHUNK(4); QCHUNK(5); QCHUNK(6); QCHUNK(7);
#undef QRD
#undef QR_
#undef QM_
#undef QSTEP
#undef QCHUNK
#undef XISS
#undef WISS
#undef KVST
#undef NST
#undef ST0
        STAMP();
#pragma unroll
        for (int cc = 0; cc < 4; ++cc) {
#pragma unroll
            for (int r = 0; r < 16; ++r) qa[cc][r] *= QSCALE;
            qf[2 * cc] = PACK8(qa[cc], 0); qf[2 * cc + 1] = PACK8(qa[cc], 8);
        }
        f32x16 pq = MFMA32(wpf, PACK8(qa[4], 0), f32x16{});
        f32x16 pk = MFMA32(wpf, PACK8(qa[4], 8), f32x16{});
        dual_softmax(pq); dual_softmax(pk);
        f32x16 d3;
        { bf16x8 id0, id1;
#pragma unroll
          for (int j = 0; j < 8; ++j) { const int f = 8 * (j >> 2) + 4 * hi + (j & 3); id0[j] = (short)(r32 == f ? 0x3F80 : 0); id1[j] = (short)(r32 == f + 16 ? 0x3F80 : 0); }
          d3 = MFMA32(PACK8(pk, 0), id0, f32x16{}); d3 = MFMA32(PACK8(pk, 8), id1, d3); }
        const bf16x8 ones = {0x3F80, 0x3F80, 0x3F80, 0x3F80, 0x3F80, 0x3F80, 0x3F80, 0x3F80};
        const bf16x8 v0 = PACK8(d1, 0), v1 = PACK8(d1, 8);
        f32x16 d4 = MFMA32(PACK8(d3, 0), blk ? ones : v0, f32x16{});
        d4 = MFMA32(PACK8(d3, 8), blk ? ones : v1, d4);
        bf16x8 pq0 = PACK8(pq, 0), pq1 = PACK8(pq, 8); PIN(pq0); PIN(pq1); PIN(d4);
        if (wid == 0) handshake_finish(ws, bid, token, hs_ok, lane);
        WAIT_BAR(0);
        if (tid == 0) __hip_atomic_fetch_add(CNT + hh_ * 64, 1u, RLX_AGENT);
        STAMP();
        LAS float* red = (LAS float*)(lds + L_V + G * 32768 + w4 * 4096);
        if (r32 == 16) {
#pragma unroll
            for (int r = 0; r < 16; ++r) red[(r & 3) + 8 * (r >> 2) + 4 * hi] = d1[r];
        }
        WAIT_L(0);
        { const float gz = bfround(red[r32]); gate = bfround(1.f / (1.f + __expf(-gz))); }
        PIN(gate);
#pragma unroll
        for (int r = 0; r < 16; ++r) red[((r & 3) + 8 * (r >> 2) + 4 * hi) * 32 + r32] = d4[r];
        ST_PL(pqp, vlin, 0, pq0); ST_PL(pqp, vlin, 1024, pq1);
        if (G == 0) {
#pragma unroll
            for (int s = 0; s < 8; ++s) ST_PL(qbp + (s >> 2) * 4096, vlin, (s & 3) * 1024, qf[s]);
        }
        WAIT_L(0); BAR(); asm volatile("" ::: "memory");
        if (tid < 256) {
            f32x4 s = {0.f, 0.f, 0.f, 0.f};
#pragma unroll
            for (int w = 0; w < 8; ++w) s += *(const LAS f32x4*)(lds + L_V + (w >> 2) * 32768 + (w & 3) * 4096 + tid * 16);
            ST_SC1(ws + WS_KVP + (size_t)(hh_ * 8 + jj) * 4096, (unsigned)tid * 16u, 0, s);
        }
        wait_cnt(CNT + hh_ * 64, 8u);
        asm volatile("" ::: "memory");
#pragma unroll
        for (int e = 0; e < 4; ++e) glds16c(kvw + (size_t)(e >> 1) * 32768 + (2 * G + (e & 1)) * 1024, vlin, ldw + (e >> 1) * 16384 + (2 * G + (e & 1)) * 1024);
        WAIT_BAR(0);
    }
    STAMP();
    {
    LANE_VARS();
    const int prow = (r32 & 7) | ((r32 & 8) << 1) | ((r32 & 16) >> 1);
    const unsigned kro = (unsigned)(prow * 16 + hi * 1024), vro = (unsigned)(L_V + r32 * 16 + hi * 2048);
    const int dq0 = 32 * w4;
    const int T = 2 * jj + 2;
    unsigned ks0 = 0u, ks1 = 16384u, ks2 = 32768u;
    int rho = 0; const int TG = G ? T : 99;
    f32x16 o[4], negm, sA0, sA1, sB0, sB1; bf16x8 kf[16], vf[16]; u32x4 pw[4]; float m, l;
#define KRD(i, kp) kf[i] = ldsr128((kp) + ((i) >> 1) * 2048 + ((i) & 1) * 512)
#define VRD(i, vp) vf[i] = ldsr128((vp) + ((i) >> 2) * 4096 + ((i) & 3) * 512)
#define PF(k) __builtin_bit_cast(bf16x8, pw[k])
#define DMA1(i) do { if ((i) < 2) GLDS_IMM(gk_, vlin, dk_, ((i) & 3) * 1024); else if ((i) < 4) GLDS_IMM_UNLESS(gk_, vlin, dk_, ((i) & 3) * 1024, rk_, T); \
                     else if ((i) < 6) GLDS_IMM(gv_, vlin, dv_, ((i) & 3) * 1024); else GLDS_IMM_UNLESS(gv_, vlin, dv_, ((i) & 3) * 1024, rho, T); } while (0)
    const char* gk_; const char* gv_; unsigned dk_, dv_, ko_, vo_; int rk_;
    unsigned go1, go2;
#define GOFF(r) ((r) < T ? (unsigned)(r) * 32768u + G * 2048u : (unsigned)(2 * (r) - T + G) * 32768u)
#define PREP_A(r, k0) do { ko_ = ((r) >= TG ? 49152u : 0u) + (k0); vo_ = ((r) - 1 >= TG ? 32768u : 0u) + (((r) - 1) & 1) * 16384u; } while (0)
#define PREP_B(r, gcur) do { gv_ = kvw + ((gcur) + 16384u); dv_ = ldw + L_V + ((r) & 1) * 16384u + ((r) < T ? G * 2048u : G * 32768u); } while (0)
#define PREP_C(r) do { rk_ = (r) + 2 <= 16 ? (r) + 2 : 16; go1 = go2; go2 = GOFF(rk_); } while (0)
#define PREP_D(k2) do { gk_ = kvw + go2; dk_ = ldw + (k2) + (rk_ < T ? G * 2048u : G * 49152u); } while (0)
#define ROT() do { const unsigned t_ = ks0; ks0 = ks1; ks1 = ks2; ks2 = t_; ++rho; } while (0)
#define GAPA(i, C0, C1, P0, P1) do { \
        if ((i) + PD < 16) { KRD((i) + PD, kp_); } else { VRD((i) + PD - 16, vp_); } \
        if (((i) & 1) == 0) DMA1((i) >> 1); \
        if ((i) & 1) C1 = MFMA32(kf[i], qf[(i) >> 1], (i) < 2 ? negm : C1); else C0 = MFMA32(kf[i], qf[(i) >> 1], (i) < 2 ? negm : C0); \
        if ((i) < 8) { P1[2 * (i)] = EX2(P1[2 * (i)]); P1[2 * (i) + 1] = EX2(P1[2 * (i) + 1]); pw[(i) >> 2][(i) & 3] = cvtpk(P0[2 * (i)], P0[2 * (i) + 1]); PIN(P1[2 * (i)]); PIN(P1[2 * (i) + 1]); PIN(pw[(i) >> 2][(i) & 3]); } \
        else { l += P1[2 * (i) - 16]; l2 += P1[2 * (i) - 15]; pw[(i) >> 2][(i) & 3] = cvtpk(P1[2 * (i) - 16], P1[2 * (i) - 15]); PIN(l); PIN(l2); PIN(pw[(i) >> 2][(i) & 3]); } \
        SBAR(); } while (0)
#define PVM(i) o[(i) & 3] = MFMA32(vf[i], PF((i) >> 2), o[(i) & 3])
#define RDB(i) do { if ((i) + PD < 16) { VRD((i) + PD, vp_); } else { KRD((i) + PD - 16, kn_); } } while (0)
#define ROUND(C0, C1, P0, P1) do { \
        const lds_cptr kp_ = lds + ko_ + kro; const lds_cptr vp_ = lds + vo_ + vro; \
        float l2 = 0.f; lds_cptr kn_ = kp_; SBAR(); \
        GAPA(0, C0, C1, P0, P1); GAPA(1, C0, C1, P0, P1); GAPA(2, C0, C1, P0, P1); GAPA(3, C0, C1, P0, P1); \
        GAPA(4, C0, C1, P0, P1); GAPA(5, C0, C1, P0, P1); GAPA(6, C0, C1, P0, P1); GAPA(7, C0, C1, P0, P1); \
        GAPA(8, C0, C1, P0, P1); GAPA(9, C0, C1, P0, P1); GAPA(10, C0, C1, P0, P1); GAPA(11, C0, C1, P0, P1); \
        GAPA(12, C0, C1, P0, P1); GAPA(13, C0, C1, P0, P1); GAPA(14, C0, C1, P0, P1); GAPA(15, C0, C1, P0, P1); \
        l += l2; \
        ldq_unless(qf, qbp, vlin, (unsigned)(rho + 1 - TG));        \
        RDB(0); PVM(0); SBAR(); RDB(1); PVM(1); SBAR(); \
        if (__builtin_expect(rho == 16 || (rho >= TG - 2 && rho < TG), 0)) mask_tile(C0, C1, dq0 - (rho == 16 ? 64 * G : 64 * (rho - TG + 2))); \
        float ma, mb; \
        RDB(2); PVM(2); ma = MX3(C0[0], C0[1], C1[0]); mb = MX3(C0[2], C0[3], C1[1]); ma = MX3(ma, C1[2], C1[3]); mb = MX3(mb, C0[4], C0[5]); PIN(ma); PIN(mb); SBAR(); \
        RDB(3); PVM(3); ma = MX3(ma, C0[6], C0[7]); mb = MX3(mb, C1[4], C1[5]); ma = MX3(ma, C1[6], C1[7]); mb = MX3(mb, C0[8], C0[9]); PIN(ma); PIN(mb); SBAR(); \
        RDB(4); PVM(4); ma = MX3(ma, C0[10], C0[11]); mb = MX3(mb, C1[8], C1[9]); ma = MX3(ma, C1[10], C1[11]); mb = MX3(mb, C0[12], C0[13]); PIN(ma); PIN(mb); SBAR(); \
        RDB(5); PVM(5); ma = MX3(ma, C0[14], C0[15]); mb = MX3(mb, C1[12], C1[13]); ma = MX3(ma, C1[14], C1[15]); \
        const float rm = swap32_max(fmaxf(ma, mb)); bool pend = false; float fsc = 1.f; \
        if (__builtin_expect(__any(rm > THR) || rho == TG, 0)) { float dl; pend = true; \
            if (rho == TG) { dl = fmaxf(rm, -1e30f); m = dl; fsc = gate / swap32_add(l); l = 0.f; }        \
            else { dl = fmaxf(rm, 0.f); m += dl; fsc = EX2(-dl); l *= fsc; } \
            _Pragma("unroll") for (int e = 0; e < 16; ++e) { C0[e] -= dl; C1[e] -= dl; negm[e] = -m; } } \
        SBAR(); \
        RDB(6); PVM(6); C0[0] = EX2(C0[0]); C0[1] = EX2(C0[1]); PIN(C0[0]); PIN(C0[1]); SBAR(); \
        RDB(7); PVM(7); C0[2] = EX2(C0[2]); C0[3] = EX2(C0[3]); l += C0[0]; l2 = C0[1]; PIN(C0[2]); PIN(C0[3]); PIN(l); SBAR(); \
        RDB(8); PVM(8); PREP_A(rho + 1, ks1); kn_ = lds + ko_ + kro; C0[4] = EX2(C0[4]); C0[5] = EX2(C0[5]); l += C0[2]; l2 += C0[3]; PIN(C0[4]); PIN(C0[5]); PIN(l); PIN(l2); SBAR(); \
        RDB(9); PVM(9); PREP_B(rho + 1, go1); C0[6] = EX2(C0[6]); C0[7] = EX2(C0[7]); l += C0[4]; l2 += C0[5]; PIN(C0[6]); PIN(C0[7]); PIN(l); PIN(l2); SBAR(); \
        RDB(10); PVM(10); PREP_C(rho + 1); C0[8] = EX2(C0[8]); C0[9] = EX2(C0[9]); l += C0[6]; l2 += C0[7]; PIN(C0[8]); PIN(C0[9]); PIN(l); PIN(l2); SBAR(); \
        RDB(11); PVM(11); PREP_D(ks0); C0[10] = EX2(C0[10]); C0[11] = EX2(C0[11]); l += C0[8]; l2 += C0[9]; PIN(C0[10]); PIN(C0[11]); PIN(l); PIN(l2); SBAR(); \
        RDB(12); PVM(12); C0[12] = EX2(C0[12]); C0[13] = EX2(C0[13]); l += C0[10]; l2 += C0[11]; PIN(C0[12]); PIN(C0[13]); PIN(l); PIN(l2); SBAR(); \
        RDB(13); PVM(13); C0[14] = EX2(C0[14]); C0[15] = EX2(C0[15]); l += C0[12]; l2 += C0[13]; PIN(C0[14]); PIN(C0[15]); PIN(l); PIN(l2); SBAR(); \
        RDB(14); PVM(14); l += C0[14]; l2 += C0[15]; PIN(l); PIN(l2); SBAR(); \
        RDB(15); PVM(15); l += l2; PIN(l); SBAR(); \
        if (__builtin_expect(pend || rho + 1 == TG, 0)) { \
            if (pend) { _Pragma("unroll") for (int c = 0; c < 4; ++c) _Pragma("unroll") for (int e = 0; e < 16; ++e) o[c][e] *= fsc; } \
            if (rho + 1 == TG) negm = f32x16{}; \
            if (rho == TG) { const int ln = fresh_lane(); const unsigned vod = (unsigned)((ln >> 5) * 2048 + (32 * w4 + (ln & 31)) * 16); const char* dst = A3 + (size_t)(bB * 576 + hh_ * 16) * 2048; \
                _Pragma("unroll") for (int c = 0; c < 4; ++c) { const bf16x8 t0 = PACK8(o[c], 0), t1 = PACK8(o[c], 8); ST_SC1(dst + c * 8192, vod, 0, t0); ST_SC1(dst + c * 8192 + 4096, vod, 0, t1); o[c] = f32x16{}; } } } \
        asm volatile("s_cmp_eq_u32 %0, %1\n\ts_cbranch_scc1 .Lw%=\n\ts_waitcnt vmcnt(0)\n.Lw%=:\n\ts_waitcnt vmcnt(8)\n\ts_barrier" :: "s"(rho), "s"(TG) : "memory", "scc");     \
        if (__builtin_expect(rho == TG + 1 && w4 == 0, 0)) { if (fresh_lane() == 0) __hip_atomic_fetch_or(CNT + (64 + bB) * 64, 1u << hh_, RLX_AGENT); }        \
        ROT(); } while (0)

#define ROUND0(C0, C1) do { \
        const lds_cptr kp_ = lds + ks0 + kro; const lds_cptr kn_ = lds + ks1 + kro; \
        go2 = GOFF(1); PREP_B(0, GOFF(0)); PREP_C(0); PREP_D(ks2); \
        _Pragma("unroll") for (int i = 0; i < 6; ++i) KRD(i, kp_); \
        _Pragma("unroll") for (int i = 0; i < 8; ++i) DMA1(i); \
        _Pragma("unroll") for (int c = 0; c < 4; ++c) o[c] = f32x16{}; \
        PREP_A(1, ks1); PREP_B(1, go1); PREP_C(1); PREP_D(ks0); SBAR(); \
        _Pragma("unroll") for (int i = 0; i < 16; ++i) { \
            if (i + 6 < 16) KRD(i + 6, kp_); \
            if (i & 1) C1 = MFMA32(kf[i], qf[i >> 1], i < 2 ? f32x16{} : C1); else C0 = MFMA32(kf[i], qf[i >> 1], i < 2 ? f32x16{} : C0); \
            SBAR(); } \
        if (TG == 2) mask_tile(C0, C1, dq0); \
        float ma = MX3(C0[0], C0[1], C1[0]), mb = MX3(C0[2], C0[3], C1[1]); ma = MX3(ma, C1[2], C1[3]); \
        _Pragma("unroll") for (int r = 4; r < 16; r += 4) { ma = MX3(ma, C0[r], C0[r + 1]); mb = MX3(mb, C0[r + 2], C0[r + 3]); ma = MX3(ma, C1[r], C1[r + 1]); mb = MX3(mb, C1[r + 2], C1[r + 3]); } \
        m = fmaxf(swap32_max(fmaxf(ma, mb)), -1e30f); l = 0.f; \
        _Pragma("unroll") for (int i = 0; i < PD; ++i) KRD(i, kn_); \
        _Pragma("unroll") for (int e = 0; e < 16; ++e) { C0[e] = EX2(C0[e] - m); l += C0[e]; C1[e] -= m; negm[e] = -m; } \
        asm volatile("s_waitcnt vmcnt(0)\n\ts_barrier" ::: "memory"); ROT(); } while (0)
#define DRAIN(P0, P1, MID) do { \
        const lds_cptr vp_ = lds + G * 32768 + vro; \
        _Pragma("unroll") for (int i = 0; i < 8; ++i) VRD(i, vp_); \
        _Pragma("unroll") for (int i = 0; i < 8; ++i) pw[i >> 2][i & 3] = cvtpk(P0[2 * i], P0[2 * i + 1]); \
        SBAR(); \
        _Pragma("unroll") for (int i = 0; i < 16; ++i) { \
            if (i + 8 < 16) VRD(i + 8, vp_); \
            if (i == 8) { MID } \
            PVM(i); \
            if (i < 8) { P1[2 * i] = EX2(P1[2 * i]); P1[2 * i + 1] = EX2(P1[2 * i + 1]); PIN(P1); } \
            if (i >= 1 && i < 9) { l += P1[2 * i - 2]; l += P1[2 * i - 1]; pw[2 + ((i - 1) >> 2)][(i - 1) & 3] = cvtpk(P1[2 * i - 2], P1[2 * i - 1]); PIN(l); PIN(pw[2 + ((i - 1) >> 2)]); } \
            SBAR(); } \
        l = swap32_add(l); } while (0)
    ROUND0(sA0, sA1);
    if (wid == 0 && fresh_lane() == 0) __hip_atomic_fetch_add(CNT + (32 + hh_) * 64, 1u, RLX_AGENT);
    STAMP();
#pragma clang loop unroll(disable)
    for (int pr = 0; pr < 8; ++pr) { ROUND(sB0, sB1, sA0, sA1); ROUND(sA0, sA1, sB0, sB1); }
    STAMP();
    {
        f32x2 ps[8]; bf16x8 phq[2];
        LANE_VARS();
        const unsigned f2 = __hip_atomic_load(CNT + (32 + hh_) * 64, RLX_AGENT);
        DRAIN(sA0, sA1,
            if (rfl(f2) < 8u) wait_cnt(CNT + (32 + hh_) * 64, 8u);
            _Pragma("unroll") for (int w = 0; w < 8; ++w) ps[w] = __builtin_bit_cast(f32x2, __hip_atomic_load((const unsigned long long*)(ws + WS_KVP + (size_t)(hh_ * 8) * 4096 + (unsigned)(w * 4096 + tid * 8)), RLX_AGENT));
            phq[0] = *(const bf16x8*)(pqp + vlin); phq[1] = *(const bf16x8*)(pqp + (vlin + 1024)); );
        const unsigned vod = (unsigned)(hi * 2048 + (32 * w4 + r32) * 16);
        BAR(); asm volatile("" ::: "memory");
        const unsigned st0 = ks2;
        { const lds_ptr xp = lds + L_V + wid * 8192 + lane * 16;
#pragma unroll
          for (int c = 0; c < 2; ++c)
#pragma unroll
              for (int g = 0; g < 4; ++g) { f32x4 t;
#pragma unroll
                  for (int e = 0; e < 4; ++e) t[e] = G ? o[c][4 * g + e] : o[2 + c][4 * g + e];
                  *(LAS f32x4*)(xp + (c * 4 + g) * 1024) = t; }
          *(LAS f32x4*)(lds + st0 + wid * 1024 + lane * 16) = (f32x4){m, l, gate, 0.f};
          f32x2 s = ps[0];
#pragma unroll
          for (int w = 1; w < 8; ++w) s += ps[w];
          *(LAS f32x2*)(lds + st0 + 8192 + tid * 8) = s; }
        WAIT_L(0); BAR(); asm volatile("" ::: "memory");
        { const int ow = wid ^ 4;
          const f32x4 st = *(const LAS f32x4*)(lds + st0 + ow * 1024 + lane * 16);
          const float mn = fmaxf(m, st[0]), f0 = EX2(m - mn), f1 = EX2(st[0] - mn);
          const float gg = G ? st[2] : gate;
          const float inv = gg / (l * f0 + st[1] * f1); const float c0 = f0 * inv, c1 = f1 * inv;
          const lds_cptr xp = lds + L_V + ow * 8192 + lane * 16;
          const char* dst = A3 + (size_t)(bA * 576 + hh_ * 16 + G * 8) * 2048;
#pragma unroll
          for (int c = 0; c < 2; ++c) {
              float v[16];
#pragma unroll
              for (int g = 0; g < 4; ++g) { const f32x4 t = *(const LAS f32x4*)(xp + (c * 4 + g) * 1024);
#pragma unroll
                  for (int e = 0; e < 4; ++e) v[4 * g + e] = (G ? o[2 + c][4 * g + e] : o[c][4 * g + e]) * c0 + t[e] * c1; }
              { const bf16x8 t0 = PACK8(v, 0), t1 = PACK8(v, 8); ST_SC1(dst + c * 8192, vod, 0, t0); ST_SC1(dst + c * 8192 + 4096, vod, 0, t1); }
          }
          float kvv[2][8];
          const int col = r32 < 16 ? r32 : 16;
#pragma unroll
          for (int s = 0; s < 2; ++s)
#pragma unroll
              for (int j = 0; j < 8; ++j) { const int f = 16 * s + 8 * (j >> 2) + 4 * hi + (j & 3); const float v = *(const LAS float*)(lds + st0 + 8192 + (f * 32 + col) * 4); kvv[s][j] = r32 <= 16 ? v : 0.f; }
          f32x16 nm = MFMA32(PACK8(kvv[0], 0), phq[0], f32x16{});
          nm = MFMA32(PACK8(kvv[1], 0), phq[1], nm);
          const float den = bfround(bfround(lo32_bcast(nm[8])) + 1e-6f); const float sc = (1.f - gate) / den;
          float ov[8];
#pragma unroll
          for (int r = 0; r < 8; ++r) ov[r] = bfround(nm[r]) * sc;
          const bf16x8 t0 = PACK8(ov, 0);
          ST_SC1(A3 + (size_t)((G ? bB : bA) * 576 + 512 + 2 * hh_) * 2048, vod, 0, t0);
        }
        WAIT_L(0); BAR(); asm volatile("" ::: "memory");
        STAMP();
    }
#undef KRD
#undef VRD
#undef PF
#undef DMA1
#undef ROT
#undef GOFF
#undef PREP_A
#undef PREP_B
#undef PREP_C
#undef PREP_D
#undef GAPA
#undef PVM
#undef RDB
#undef ROUND
#undef ROUND0
#undef DRAIN
    }
    {
        LANE_VARS();
        const int rt = 4 * (bid & 7) + (bid >> 6), ct = (bid >> 3) & 7;
        const char* gA = A3 + (size_t)((rt >> 1) * 576 + 2 * wid) * 2048 + (rt & 1) * 1024;
        const char* gW3 = ws + WS_W3 + (size_t)(ct * 288 + wid) * 2048;
        const unsigned wb = lds0 + wid * 20480;
        const unsigned* rdy = CNT + (64 + (rt >> 1)) * 64;
        auto issue = [&](int u, unsigned slot) {
            const char* ga = gA + (size_t)u * 32768; const char* gw = gW3 + (size_t)u * 16384;
            GLDS_IMM(ga, vlin, wb + slot, 0); GLDS_IMM(ga + 1024, vlin, wb + slot, 1024); GLDS_IMM(gw, vlin, wb + slot + 2048, 0); GLDS_IMM(gw, vlin, wb + slot + 2048, 1024); };
        unsigned seen, seenB; int hnext = 0;
        auto poll = [&]() { const unsigned long long w = __hip_atomic_load((const unsigned long long*)rdy, RLX_AGENT); seen = rfl((unsigned)w); seenB = rfl((unsigned)(w >> 32)); };
        auto next_head = [&]() -> int {
            unsigned spins = 0;
            while (hnext < 32 ? !((seen >> hnext) & 1u) : seenB != ~0u) { poll(); if (++spins > (1u << 20)) break; if (spins > 1) __builtin_amdgcn_s_sleep(2); }
            asm volatile("" ::: "memory");
            return hnext++; };
        poll();
        asm volatile("s_waitcnt vmcnt(0)\n\ts_barrier" ::: "memory");
        if (tid == 0) { __hip_atomic_fetch_or(CNT + (64 + bA) * 64, 1u << hh_, RLX_AGENT); __hip_atomic_fetch_or(CNT + (64 + bB) * 64, 1u << hh_, RLX_AGENT);
                        __hip_atomic_fetch_or(CNT + (64 + bA) * 64 + 1, 1u << hh_, RLX_AGENT); __hip_atomic_fetch_or(CNT + (64 + bB) * 64 + 1, 1u << hh_, RLX_AGENT); }
        STAMP();
#pragma unroll
        for (int i = 0; i < 5; ++i) issue(next_head(), i * 4096u);
        f32x16 acc[2][2] = {};
        const unsigned ao = (unsigned)(wid * 20480 + hi * 1024 + r32 * 16);
        unsigned bo[2];
#pragma unroll
        for (int nb = 0; nb < 2; ++nb) bo[nb] = (unsigned)(wid * 20480 + 2048 + (4 * hi + q4) * 128 + 64 * (nb ^ ((q4 >> 1) & 1)) + 32 * blk + 8 * p4);
        bf16x8 af[2][2], bf[2][2];
#define K3RD(buf, sb) do { af[buf][0] = ldsr128((sb) + ao); bf[buf][0] = cat8(trrd((sb) + bo[0]), trrd((sb) + bo[0] + 1024)); \
        af[buf][1] = ldsr128((sb) + ao + 512); bf[buf][1] = cat8(trrd((sb) + bo[1]), trrd((sb) + bo[1] + 1024)); } while (0)
#define K3BODY(buf, i) do { \
        const unsigned snx = sc == 16384u ? 0u : sc + 4096u; \
        if ((i) + 1 < 36) K3RD(buf ^ 1, lds + snx); \
        SBAR(); acc[0][0] = MFMA32(bf[buf][0], af[buf][0], acc[0][0]); acc[1][0] = MFMA32(bf[buf][0], af[buf][1], acc[1][0]); SBAR(); \
        if ((i) + 5 < 36) issue(next_head(), sc); \
        SBAR(); acc[0][1] = MFMA32(bf[buf][1], af[buf][0], acc[0][1]); acc[1][1] = MFMA32(bf[buf][1], af[buf][1], acc[1][1]); SBAR(); \
        sc = snx; } while (0)
#define K3W(n) asm volatile("s_waitcnt vmcnt(" #n ") lgkmcnt(0)" ::: "memory")
        unsigned sc = 0u;
        WAIT_V(16);
        K3RD(0, lds);
        for (int i = 0; i < 32; i += 2) {
            if (G) __builtin_amdgcn_s_setprio(1); K3W(12); K3BODY(0, i);
            if (G) __builtin_amdgcn_s_setprio(0); K3W(12); K3BODY(1, i + 1); }
        K3W(8); K3BODY(0, 32); K3W(4); K3BODY(1, 33); K3W(0); K3BODY(0, 34); K3W(0); K3BODY(1, 35);
#undef K3RD
#undef K3BODY
#undef K3W
        STAMP();
        BAR(); asm volatile("" ::: "memory");
#pragma unroll
        for (int mi = 0; mi < 2; ++mi)
#pragma unroll
            for (int nb = 0; nb < 2; ++nb)
#pragma unroll
                for (int g = 0; g < 4; ++g) { const int row = 32 * mi + r32, ch = 8 * nb + 2 * g + hi;
                    *(LAS f32x4*)(lds + wid * 16384 + row * 256 + 16 * (ch ^ (row & 15))) = (f32x4){acc[mi][nb][4 * g], acc[mi][nb][4 * g + 1], acc[mi][nb][4 * g + 2], acc[mi][nb][4 * g + 3]}; }
        WAIT_L(0); BAR(); asm volatile("" ::: "memory");
        { const int row = tid >> 3, c8 = tid & 7; f32x4 s0 = {}, s1 = {};
#pragma unroll
          for (int w = 0; w < 8; ++w) { s0 += *(const LAS f32x4*)(lds + w * 16384 + row * 256 + 16 * ((2 * c8) ^ (row & 15)));
                                        s1 += *(const LAS f32x4*)(lds + w * 16384 + row * 256 + 16 * ((2 * c8 + 1) ^ (row & 15))); }
          u32x4 w = {cvtpk(s0[0], s0[1]), cvtpk(s0[2], s0[3]), cvtpk(s1[0], s1[1]), cvtpk(s1[2], s1[3])};
          *(u32x4*)(out + (size_t)(rt * 64 + row) * 512 + ct * 64 + c8 * 8) = w; }
    }
    asm volatile(".Lcw_end:");
    STAMP(); STAMP_RT();
    STAMP_OUT(ws + WS_END);
}

extern "C" void kernel_launch(void* const* d_in, const int* in_sizes, int n_in, void* d_out, int out_size, void* d_ws, size_t ws_size,
                              hipStream_t stream) {
    (void)in_sizes; (void)n_in; (void)out_size; (void)ws_size;
    char* ws = (char*)d_ws;
    (void)hipFuncSetAttribute((const void*)kf_all, hipFuncAttributeMaxDynamicSharedMemorySize, LDS_BYTES);
    kf_all<<<256, 512, LDS_BYTES, stream>>>((const u16*)d_in[0], (const u16*)d_in[1], (const u16*)d_in[2], (const u16*)d_in[3], (const u16*)d_in[4], (const u16*)d_in[5],
                                            (const u16*)d_in[7], (const u16*)d_in[8], (const u16*)d_in[9], (const u16*)d_in[10], ws, (u16*)d_out);
}
```

```cpp
#include <hip/hip_runtime.h>
#include <hip/hip_bf16.h>
#include <stdint.h>
#define PD 4
#define CODEWARM 1
#define PREFETCH 1

typedef unsigned short u16;
typedef short bf16x8 __attribute__((ext_vector_type(8)));
typedef short s16x4 __attribute__((ext_vector_type(4)));
typedef float f32x2 __attribute__((ext_vector_type(2)));
typedef float f32x4 __attribute__((ext_vector_type(4)));
typedef float f32x16 __attribute__((ext_vector_type(16)));
typedef unsigned u32x4 __attribute__((ext_vector_type(4)));
typedef unsigned u32x2 __attribute__((ext_vector_type(2)));
typedef __bf16 bf16x2_t __attribute__((ext_vector_type(2)));
#define LAS __attribute__((address_space(3)))
#define DI __device__ __forceinline__
typedef LAS const char* lds_cptr;
typedef LAS char* lds_ptr;

constexpr int SEQ = 2048, DM = 512, NH = 32, HD = 128;
constexpr size_t WS_CNT = 0;
constexpr size_t WS_HQ = 32768, WS_HR = 33792;
constexpr size_t WS_KV = 65536;
constexpr size_t WS_A3 = WS_KV + (size_t)NH * 32 * 32768;
constexpr size_t WS_KVP = WS_A3 + (size_t)576 * SEQ * 16;
constexpr size_t WS_QB = WS_KVP + (size_t)NH * 8 * 4096;
constexpr size_t WS_PQ = WS_QB + (size_t)256 * 32768;
constexpr size_t WS_W3 = WS_PQ + (size_t)256 * 16384;
constexpr size_t WS_END = WS_W3 + (size_t)8 * 288 * 2048;
constexpr int LDS_BYTES = 163840;

#define MFMA32(a, b, c) __builtin_amdgcn_mfma_f32_32x32x16_bf16((a), (b), (c), 0, 0, 0)
#define MFMA16(a, b, c) __builtin_amdgcn_mfma_f32_16x16x32_bf16((a), (b), (c), 0, 0, 0)
#define SBAR() __builtin_amdgcn_sched_barrier(0)
#define WAIT_V(n) asm volatile("s_waitcnt vmcnt(" #n ")" ::: "memory")
#define WAIT_L(n) asm volatile("s_waitcnt lgkmcnt(" #n ")" ::: "memory")
#define BAR() __builtin_amdgcn_s_barrier()
#define WAIT_BAR(N) asm volatile("s_waitcnt vmcnt(" #N ") lgkmcnt(0)\n\ts_barrier" ::: "memory")

DI unsigned rfl(unsigned x) { return __builtin_amdgcn_readfirstlane(x); }
DI unsigned cvtpk(float lo, float hi) { f32x2 v = {lo, hi}; bf16x2_t b = __builtin_convertvector(v, bf16x2_t); return __builtin_bit_cast(unsigned, b); }
DI float bfround(float x) { return __uint_as_float(cvtpk(x, x) << 16); }
DI void glds16(const void* sbase, unsigned voff, unsigned lds_dst) {
    unsigned sv;
    asm volatile("s_mov_b32 %0, m0\n\ts_mov_b32 m0, %3\n\ts_nop 2\n\tglobal_load_lds_dwordx4 %1, %2\n\ts_mov_b32 m0, %0"
                 : "=&s"(sv) : "v"(voff), "s"(sbase), "s"(lds_dst) : "memory");
}
DI void glds16c(const void* sbase, unsigned voff, unsigned lds_dst) {
    unsigned sv;
    asm volatile("s_mov_b32 %0, m0\n\ts_mov_b32 m0, %3\n\ts_nop 2\n\tglobal_load_lds_dwordx4 %1, %2 sc1\n\ts_mov_b32 m0, %0"
                 : "=&s"(sv) : "v"(voff), "s"(sbase), "s"(lds_dst) : "memory");
}
DI void touch8k(const void* sbase, unsigned voff, unsigned lds_dst) {
    unsigned sv;
    asm volatile("s_mov_b32 %0, m0\n\ts_mov_b32 m0, %3\n\ts_nop 2\n\tglobal_load_lds_dword %1, %2\n\ts_mov_b32 m0, %0"
                 : "=&s"(sv) : "v"(voff), "s"(sbase), "s"(lds_dst) : "memory");
}
DI void ldq_unless(bf16x8* q, const void* sbase, unsigned voff, unsigned skip) {
    asm volatile("s_cmp_lg_u32 %10, 0\n\ts_cbranch_scc0 .Lrare%=\n.Lback%=:\n\t.subsection 1\n.Lrare%=:\n\ts_nop 4\n\t"
                 "global_load_dwordx4 %0, %8, %9 offset:-4096\n\tglobal_load_dwordx4 %1, %8, %9 offset:-3072\n\tglobal_load_dwordx4 %2, %8, %9 offset:-2048\n\tglobal_load_dwordx4 %3, %8, %9 offset:-1024\n\t"
                 "global_load_dwordx4 %4, %8, %9\n\tglobal_load_dwordx4 %5, %8, %9 offset:1024\n\tglobal_load_dwordx4 %6, %8, %9 offset:2048\n\tglobal_load_dwordx4 %7, %8, %9 offset:3072\n\ts_branch .Lback%=\n\t.subsection 0"
                 : "+v"(q[0]), "+v"(q[1]), "+v"(q[2]), "+v"(q[3]), "+v"(q[4]), "+v"(q[5]), "+v"(q[6]), "+v"(q[7]) : "v"(voff), "s"((const char*)sbase + 4096), "s"(skip) : "memory", "scc");
}
DI void glds4c(const void* sbase, unsigned lds_dst) {
    unsigned sv, z;
    asm volatile("v_mov_b32 %1, 0\n\ts_mov_b32 %0, m0\n\ts_mov_b32 m0, %3\n\ts_nop 2\n\tglobal_load_lds_dword %1, %2 sc1\n\ts_mov_b32 m0, %0"
                 : "=&s"(sv), "=&v"(z) : "s"(sbase), "s"(lds_dst) : "memory");
}
#define GLDS_IMM(sbase, voff, lds_dst, IMM) asm volatile("s_mov_b32 m0, %2\n\ts_nop 3\n\tglobal_load_lds_dwordx4 %0, %1 offset:%3 sc1" :: "v"(voff), "s"(sbase), "s"(lds_dst), "i"(IMM) : "memory")
#define GLDS_IMM_UNLESS(sbase, voff, lds_dst, IMM, a, b) asm volatile("s_cmp_lt_i32 %4, %5\n\ts_cselect_b64 exec, 0, -1\n\ts_mov_b32 m0, %2\n\ts_nop 1\n\tglobal_load_lds_dwordx4 %0, %1 offset:%3 sc1\n\ts_mov_b64 exec, -1" \
        :: "v"(voff), "s"(sbase), "s"(lds_dst), "i"(IMM), "s"(a), "s"(b) : "memory", "scc")
typedef short v4i16_t __attribute__((ext_vector_type(4)));
DI s16x4 trrd(lds_cptr p) { return __builtin_bit_cast(s16x4, __builtin_amdgcn_ds_read_tr16_b64_v4i16((LAS v4i16_t*)p)); }
DI bf16x8 cat8(s16x4 a, s16x4 b) { return __builtin_shufflevector(a, b, 0, 1, 2, 3, 4, 5, 6, 7); }
DI bf16x8 ldsr128(lds_cptr p) { return *(const LAS bf16x8*)p; }
DI bf16x8 pack8(float a0, float a1, float a2, float a3, float a4, float a5, float a6, float a7) {
    u32x4 w = {cvtpk(a0, a1), cvtpk(a2, a3), cvtpk(a4, a5), cvtpk(a6, a7)}; return __builtin_bit_cast(bf16x8, w); }
#define PACK8(X, B) pack8((X)[(B)], (X)[(B) + 1], (X)[(B) + 2], (X)[(B) + 3], (X)[(B) + 4], (X)[(B) + 5], (X)[(B) + 6], (X)[(B) + 7])
DI float swap32_max(float v) { auto rr = __builtin_amdgcn_permlane32_swap(__float_as_uint(v), __float_as_uint(v), false, false); return fmaxf(__uint_as_float(rr[0]), __uint_as_float(rr[1])); }
DI float swap32_add(float v) { auto rr = __builtin_amdgcn_permlane32_swap(__float_as_uint(v), __float_as_uint(v), false, false); return __uint_as_float(rr[0]) + __uint_as_float(rr[1]); }
DI float lo32_bcast(float v) { auto rr = __builtin_amdgcn_permlane32_swap(__float_as_uint(v), __float_as_uint(v), false, false); return __uint_as_float(rr[0]); }

constexpr float QSCALE = 0.08838834764831845f * 1.4426950408889634f;
constexpr float THR = 8.f;
constexpr int L_V = 98304;
constexpr int L_WE = 65536, L_GATE = 139264;
#define PIN(x) asm volatile("" : "+v"(x))
#define MX3(a, b, c) __builtin_fmaxf(__builtin_fmaxf((a), (b)), (c))
#define EX2(v) __builtin_amdgcn_exp2f(v)
#define RLX_AGENT __ATOMIC_RELAXED, __HIP_MEMORY_SCOPE_AGENT
#define ST_PL(base, voff, imm, v) asm volatile("s_nop 4\n\tglobal_store_dwordx4 %0, %1, %2 offset:%3\n\ts_nop 1" :: "v"(voff), "v"(v), "s"(base), "i"(imm) : "memory")
#define ST_SC1(base, voff, imm, v) asm volatile("s_nop 4\n\tglobal_store_dwordx4 %0, %1, %2 offset:%3 sc1\n\ts_nop 1" :: "v"(voff), "v"(v), "s"(base), "i"(imm) : "memory")
DI int fresh_lane() { int l; asm volatile("v_mbcnt_lo_u32_b32 %0, -1, 0\n\tv_mbcnt_hi_u32_b32 %0, -1, %0" : "=v"(l)); return l; }
DI void wait_cnt(const unsigned* p, unsigned want) {
    unsigned spins = 0;
    while (rfl(__hip_atomic_load(p, RLX_AGENT)) < want) { __builtin_amdgcn_s_sleep(2); if (++spins > (1u << 20)) break; }
}
DI void handshake_finish(char* ws, int bid, unsigned token, bool ok, int lane) {
    unsigned* CNT = (unsigned*)(ws + WS_CNT); unsigned spins = 0;
    if (bid == 0) {
        for (;;) {
            const unsigned a = __hip_atomic_load(CNT + (80 + (lane & 15)) * 64, RLX_AGENT);
            if (__all(a >= ((lane & 15) ? 16u : 15u))) break;
#pragma unroll
            for (int i = 0; i < 4; ++i) { const unsigned q = __hip_atomic_load((const unsigned*)(ws + WS_HQ) + i * 64 + lane, RLX_AGENT); __hip_atomic_store((unsigned*)(ws + WS_HR) + i * 64 + lane, q, RLX_AGENT); }
            __builtin_amdgcn_s_sleep(8); if (++spins > (1u << 18)) break; }
    } else if (!ok) {
        while (rfl(__hip_atomic_load((const unsigned*)(ws + WS_HR) + bid, RLX_AGENT)) != token) { __builtin_amdgcn_s_sleep(8); if (++spins > (1u << 20)) break; }
        if (lane == 0) __hip_atomic_fetch_add(CNT + (80 + (bid & 15)) * 64, 1u, RLX_AGENT);
    }
}
DI void dual_softmax(f32x16& p) {
#pragma unroll
    for (int hf = 0; hf < 2; ++hf) {
        float mx = -INFINITY;
#pragma unroll
        for (int r = 0; r < 8; ++r) { p[8 * hf + r] = bfround(p[8 * hf + r]); mx = fmaxf(mx, p[8 * hf + r]); }
        mx = swap32_max(mx); float s = 0.f;
#pragma unroll
        for (int r = 0; r < 8; ++r) { p[8 * hf + r] = EX2((p[8 * hf + r] - mx) * 1.4426950408889634f); s += p[8 * hf + r]; }
        s = __builtin_amdgcn_rcpf(swap32_add(s));
#pragma unroll
        for (int r = 0; r < 8; ++r) p[8 * hf + r] *= s;
    }
}
DI void mask_half(f32x16& sc, int dqw) {
    if (dqw < 0) {
#pragma unroll
        for (int r = 0; r < 16; ++r) sc[r] = -INFINITY;
    } else if (dqw == 0) {
        const int ln = fresh_lane(); const int lr = (ln & 31) - 4 * (ln >> 5); const float ninf = -INFINITY;
#pragma unroll
        for (int r = 0; r < 16; ++r)
            asm volatile("s_nop 0\n\tv_cmp_gt_i32 vcc, %c3, %2\n\tv_cndmask_b32 %0, %0, %1, vcc" : "+v"(sc[r]) : "v"(ninf), "v"(lr), "i"(16 * ((r >> 2) & 1) + 8 * (r >> 3) + (r & 3)) : "vcc");
    }
}
DI void mask_tile(f32x16& s0, f32x16& s1, int dqw) {
    asm volatile("s_nop 7\n\ts_nop 7" ::: "memory");
    mask_half(s0, dqw); mask_half(s1, dqw - 32); }

__global__ __launch_bounds__(512, 2) void kf_all(const u16* __restrict__ x, const u16* __restrict__ ckv, const u16* __restrict__ wq, const u16* __restrict__ wuk,
                                                 const u16* __restrict__ wuv, const u16* __restrict__ wo_a, const u16* __restrict__ wqkv,
                                                 const u16* __restrict__ wphi, const u16* __restrict__ wo_b, const u16* __restrict__ gatew,
                                                 char* ws, u16* __restrict__ out) {
    extern __shared__ __attribute__((aligned(16))) char shm[];
    const lds_ptr lds = (lds_ptr)shm; const unsigned lds0 = (unsigned)(uintptr_t)shm;
    const int wid = rfl(threadIdx.x >> 6), G = wid >> 2, w4 = wid & 3;
    const int bid = blockIdx.x, hh_ = 4 * (bid & 7) + (bid >> 6), jj = (bid >> 3) & 7;
#define LANE_VARS() int tid = threadIdx.x; asm volatile("" : "+v"(tid)); const int lane = tid & 63, r32 = lane & 31, hi = lane >> 5, blk = (lane >> 4) & 1, \
        q4 = (lane & 15) >> 2, p4 = lane & 3; const unsigned vlin = (unsigned)lane * 16u; (void)r32; (void)hi; (void)blk; (void)q4; (void)p4; (void)vlin
    unsigned* const CNT = (unsigned*)(ws + WS_CNT);
    char* const KVh = ws + WS_KV + (size_t)hh_ * 32 * 32768;
    char* const A3 = ws + WS_A3;
    u32x4 pkv[16];
    unsigned token = 0u;
    const int bA = 15 - jj, bB = jj;
    const char* gX = (const char*)x + (size_t)((G ? bB : bA) * 128 + 32 * w4) * 1024; const char* gW = (const char*)wq + hh_ * 256; const char* gE = (const char*)wqkv + hh_ * 32;
    const unsigned lx = lds0 + wid * 4096, lw = lds0 + L_WE + wid * 1024;
    unsigned voffX, voffW, voffE;
    { const int lane = threadIdx.x & 63;
        { const int row = lane >> 3, ch = (lane & 7) ^ ((row >> 1) & 3); voffX = (unsigned)(row * 1024 + ch * 16); }
        { const int row = 4 * wid + (lane >> 4), j = lane & 15, c = (j >> 2) ^ (row & 3); voffW = (unsigned)(row * 8192 + c * 64 + (j & 3) * 16); }
        { const int row = 8 * wid + (lane >> 3), sl = lane & 7, hf = (sl >> 2) ^ ((row >> 1) & 1), part = hf ? 2 : ((sl >> 1) & 1);
          voffE = (unsigned)(row * 3072 + part * 1024 + (sl & 1) * 16); }
        if (wid == 0) glds16(gatew, (unsigned)lane * 16u, lds0 + L_GATE);
        if (bid == 0 && wid == 1) {
            const unsigned z = 0u;
            asm volatile("global_store_dword %0, %1, %2 sc1\n\tglobal_store_dword %0, %1, %3 sc1\n\ts_nop 1" :: "v"((unsigned)lane * 256u), "v"(z), "s"(ws + WS_CNT), "s"(ws + WS_CNT + 16384) : "memory");
            asm volatile("global_store_dword %0, %1, %2 offset:4 sc1\n\ts_nop 1" :: "v"((unsigned)lane * 256u), "v"(z), "s"(ws + WS_CNT + 16384) : "memory"); }
        PIN(voffX); PIN(voffW); PIN(voffE);
    }
#define XISS(c) do { _Pragma("unroll") for (int e = 0; e < 4; ++e) glds16(gX + e * 8192 + (c) * 128, voffX ^ ((e & 1) << 6), lx + ((c) & 1) * 32768 + e * 1024); } while (0)
#define WISS(c) do { glds16(gW + (size_t)(c) * 524288, voffW, lw + ((c) % 3) * 24576); glds16(gW + (size_t)(c) * 524288 + 262144, voffW, lw + ((c) % 3) * 24576 + 8192); \
                     glds16(gE + (size_t)(c) * 196608, voffE, lw + ((c) % 3) * 24576 + 16384); } while (0)
    {
        LANE_VARS();
        const int wr = wid >> 2, wc = wid & 3, fr = lane & 15, fq = lane >> 4;
        constexpr int HTB = 16384;
#define SA(b, h) (((b) * 2 + (h)) * HTB)
#define SB(b, h) ((4 + (b) * 2 + (h)) * HTB)
        unsigned voffA, voffB;
        { const int row = 8 * wid + (lane >> 3), ch = (lane & 7) ^ ((row >> 1) & 7); voffA = (unsigned)(row * 1024 + ch * 16); }
        { const int row = 4 * wid + (lane >> 4), j = lane & 15, s = ((row >> 3) & 1) * 4 + (row & 3), c = (j >> 1) ^ s; voffB = (unsigned)(row * 8192 + c * 32 + (j & 1) * 16); }
        const char* gA = (const char*)ckv + (size_t)jj * 256 * 1024;
        const char* gB0 = (const char*)wuk + hh_ * 256; const char* gB1 = (const char*)wuv + hh_ * 256;
        const unsigned ldsw = rfl(lds0 + wid * 1024);
#define STAGE_A(b, h, kt) do { const char* g_ = gA + (h) * 131072 + (kt) * 128; glds16(g_, voffA, ldsw + SA(b, h)); glds16(g_ + 65536, voffA, ldsw + SA(b, h) + 8192); } while (0)
#define STAGE_B(b, h, kt) do { const char* g_ = ((h) ? gB1 : gB0) + (size_t)(kt) * 524288; glds16(g_, voffB, ldsw + SB(b, h)); glds16(g_ + 262144, voffB, ldsw + SB(b, h) + 8192); } while (0)
        lds_cptr pa[2], pb[2];
#pragma unroll
        for (int ks = 0; ks < 2; ++ks) pa[ks] = lds + (wr * 64 + fr) * 128 + 16 * ((4 * ks + fq) ^ ((fr >> 1) & 7));
        { const int q = fr >> 2, p = fr & 3, s = (fq & 1) * 4 + q;
#pragma unroll
          for (int n = 0; n < 2; ++n) pb[n] = lds + (8 * fq + q) * 256 + 32 * ((2 * wc + n) ^ s) + 8 * p; }
#define LDA(dst, b, h) do { _Pragma("unroll") for (int m = 0; m < 4; ++m) _Pragma("unroll") for (int k = 0; k < 2; ++k) dst[m][k] = ldsr128(pa[k] + SA(b, h) + m * 2048); } while (0)
#define LDB(dst, b, h) do { _Pragma("unroll") for (int n = 0; n < 2; ++n) _Pragma("unroll") for (int k = 0; k < 2; ++k) \
        dst[n][k] = cat8(trrd(pb[n] + SB(b, h) + k * 8192), trrd(pb[n] + SB(b, h) + k * 8192 + 1024)); } while (0)
#define MMA(ai, bj, At, Bt) do { __builtin_amdgcn_s_setprio(1); _Pragma("unroll") for (int m = 0; m < 4; ++m) _Pragma("unroll") for (int n = 0; n < 2; ++n) _Pragma("unroll") for (int k = 0; k < 2; ++k) \
        acc[ai][bj][m][n] = (bj) ? MFMA16(At[m][k], Bt[n][k], acc[ai][bj][m][n]) : MFMA16(Bt[n][k], At[m][k], acc[ai][bj][m][n]); __builtin_amdgcn_s_setprio(0); } while (0)
        const unsigned scr = lds0 + 143360 + wid * 256; const size_t gw = (size_t)(bid * 8 + wid);
        auto warm = [&](int slot) {
            if (slot == 0 && wid == 0) { token = rfl(*(const LAS unsigned*)(lds + 148480)) + 1u; const unsigned z = 0u, tk = token;
                asm volatile("s_nop 4\n\tglobal_store_dword %0, %1, %2 sc1\n\ts_nop 1" :: "v"(z), "v"(tk), "s"(ws + WS_HQ + bid * 4) : "memory"); }
            if (bid == 0 && wid == 1) {
                if (slot > 0) { const u32x4 rq = *(const LAS u32x4*)(lds + 147456 + lane * 16); ST_SC1(ws + WS_HR, (unsigned)lane * 16u, 0, rq); WAIT_L(0); }
                glds16c(ws + WS_HQ, (unsigned)lane * 16u, lds0 + 147456); }
            const unsigned vo = (unsigned)lane * 128u;
            if (slot == 0) { if (lane < 8) touch8k((const char*)x + gw * 1024, vo, scr); }
            else if (slot == 1) { if (lane < 16) touch8k((const char*)wq + gw * 2048, vo, scr); }
            else if (slot == 2) { if (lane < 6) touch8k((const char*)wqkv + gw * 768, vo, scr); }
            if (slot == 3) {
                const int row = lane >> 3, sl = lane & 7, nb = (sl >> 2) ^ ((row >> 1) & 1); const unsigned vw = (unsigned)(row * 1024 + nb * 64 + (sl & 3) * 16);
                const char* g = (const char*)wo_a + (size_t)(8 * hh_ + wid) * 16384 + jj * 128;
                glds16(g, vw, lds0 + 149504 + wid * 1024); glds16(g + 8192, vw, lds0 + 131072 + wid * 1024);
                if (wid < 2) glds16((const char*)wo_b + (size_t)hh_ * 16384 + wid * 8192 + jj * 128, vw, lds0 + 157696 + wid * 1024);
            } else if (slot == 4) {
                char* d = ws + WS_W3 + (size_t)(jj * 288 + 8 * hh_ + wid) * 2048; const unsigned vl = (unsigned)lane * 16u;
                { const u32x4 t0 = *(const LAS u32x4*)(lds + 149504 + wid * 1024 + lane * 16); ST_SC1(d, vl, 0, t0); }
                { const u32x4 t1 = *(const LAS u32x4*)(lds + 131072 + wid * 1024 + lane * 16); ST_SC1(d, vl, 1024, t1); }
                if (wid < 2) { const u32x4 t2 = *(const LAS u32x4*)(lds + 157696 + wid * 1024 + lane * 16); ST_SC1(ws + WS_W3 + (size_t)(jj * 288 + 256 + hh_) * 2048 + wid * 1024, vl, 0, t2); }
                WAIT_L(0);
            }
        };
        f32x4 acc[2][2][4][2] = {};
        bf16x8 At[4][2], B0[2][2], B1[2][2];
        constexpr int nt = 8;
        STAGE_B(0, 0, 0); STAGE_A(0, 0, 0); STAGE_B(0, 1, 0); STAGE_A(0, 1, 0);
        if (wid == 3) {
            unsigned long long pc; unsigned len; asm volatile("s_getpc_b64 %0\n.Lcw_start:\n\ts_mov_b32 %1, .Lcw_end-.Lcw_start" : "=s"(pc), "=s"(len));
            const unsigned off = (unsigned)((bid >> 3) * 13 + lane) * 128u;
            if (lane < 13) touch8k((const char*)pc, off < len ? off : 0u, scr); }
        if (wid == 0) glds4c(ws + WS_HR + bid * 4, lds0 + 148480);
        if (wr == 1) BAR();
        if (wid == 0 || (CODEWARM && wid == 3)) { WAIT_V(5); } else { WAIT_V(4); } BAR();
        STAGE_B(1, 0, 1); STAGE_A(1, 0, 1); STAGE_B(1, 1, 1);
        if (wid == 0 || (CODEWARM && wid == 3)) { WAIT_V(7); } else { WAIT_V(6); } BAR();
        { const unsigned vo = (unsigned)lane * 128u;
          if (lane < 16) { touch8k((const char*)wuk + gw * 2048, vo, scr); touch8k((const char*)wuv + gw * 2048, vo, scr); }
          if (lane < 8) touch8k((const char*)ckv + gw * 1024, vo, scr); }
        for (int t = 0; t < nt - 2; t += 2) {
            LDB(B0, 0, 0); SBAR(); LDA(At, 0, 0); STAGE_A(1, 1, t + 1);
            WAIT_L(8); BAR(); WAIT_L(0); MMA(0, 0, At, B0); BAR(); SBAR();
            LDB(B1, 0, 1); STAGE_B(0, 0, t + 2);
            BAR(); WAIT_L(0); MMA(0, 1, At, B1); BAR();
            LDA(At, 0, 1); STAGE_A(0, 0, t + 2);
            BAR(); WAIT_L(0); MMA(1, 0, At, B0); BAR(); SBAR();
            STAGE_B(0, 1, t + 2);
            WAIT_V(6); warm(t); BAR(); MMA(1, 1, At, B1); BAR();
            LDB(B0, 1, 0); SBAR(); LDA(At, 1, 0); STAGE_A(0, 1, t + 2);
            WAIT_L(8); BAR(); WAIT_L(0); MMA(0, 0, At, B0); BAR(); SBAR();
            LDB(B1, 1, 1); STAGE_B(1, 0, t + 3);
            BAR(); WAIT_L(0); MMA(0, 1, At, B1); BAR();
            LDA(At, 1, 1); STAGE_A(1, 0, t + 3);
            BAR(); WAIT_L(0); MMA(1, 0, At, B0); BAR(); SBAR();
            STAGE_B(1, 1, t + 3);
            WAIT_V(6); warm(t + 1); BAR(); MMA(1, 1, At, B1); BAR();
        }
        { LDB(B0, 0, 0); WAIT_V(0); LDA(At, 0, 0); STAGE_A(1, 1, nt - 1);
          BAR(); WAIT_L(0); MMA(0, 0, At, B0); BAR();
          LDB(B1, 0, 1); BAR(); WAIT_L(0); MMA(0, 1, At, B1); BAR();
          LDA(At, 0, 1); BAR(); WAIT_L(0); MMA(1, 0, At, B0); MMA(1, 1, At, B1); BAR(); }
        { LDB(B0, 1, 0); LDA(At, 1, 0); BAR(); WAIT_L(0); MMA(0, 0, At, B0); BAR();
          LDB(B1, 1, 1); WAIT_V(0);
          WISS(0); XISS(0);
          BAR(); WAIT_L(0); MMA(0, 1, At, B1); BAR();
          LDA(At, 1, 1); BAR(); WAIT_L(0); MMA(1, 0, At, B0); MMA(1, 1, At, B1); BAR(); }
        if (wr == 0) BAR();
        WISS(1); XISS(1);
        if (wid == 0) glds4c(ws + WS_HR + bid * 4, lds0 + 148480);
        SBAR();
#undef SA
#undef SB
#undef STAGE_A
#undef STAGE_B
#undef LDA
#undef LDB
#undef MMA
#pragma unroll
        for (int ai = 0; ai < 2; ++ai) {
#pragma unroll
            for (int m = 0; m < 4; ++m) { const f32x4 a = acc[ai][0][m][0], b = acc[ai][0][m][1];
                pkv[ai * 4 + m] = (u32x4){cvtpk(a[0], a[1]), cvtpk(a[2], a[3]), cvtpk(b[0], b[1]), cvtpk(b[2], b[3])}; }
#pragma unroll
            for (int u = 0; u < 2; ++u)
#pragma unroll
                for (int n = 0; n < 2; ++n) { const f32x4 a = acc[ai][1][2 * u][n], b = acc[ai][1][2 * u + 1][n];
                    pkv[8 + ai * 4 + u * 2 + n] = (u32x4){cvtpk(a[0], a[1]), cvtpk(a[2], a[3]), cvtpk(b[0], b[1]), cvtpk(b[2], b[3])}; }
        }
    }
    const char* kvw = KVh + w4 * 4096;
    const unsigned ldw = lds0 + w4 * 4096;
    char* const qbp = ws + WS_QB + (size_t)(bid * 4 + w4) * 8192;
    char* const pqp = ws + WS_PQ + (size_t)(bid * 8 + wid) * 2048;
    bf16x8 qf[8]; float gate;
    {
        LANE_VARS();
        u16 wph[8];
#pragma unroll
        for (int j = 0; j < 8; ++j) wph[j] = wphi[hh_ * 256 + (8 * (j >> 2) + 4 * hi + (j & 3)) * 16 + (lane & 15)];
        const char* tb0 = KVh + (size_t)(4 * jj + G) * 32768;
        const unsigned vok = (unsigned)((4 * w4 + (lane >> 4)) * 1024 + (lane & 15) * 16), vov = (unsigned)(16384 + (lane >> 4) * 2048 + (w4 * 32 + (lane & 15)) * 16);
#define KVST(i) do { if ((i) < 8) ST_SC1(tb0 + ((i) >> 2) * 65536, vok, ((i) & 3) * 256, pkv[i]); \
                     else ST_SC1(tb0 + (((i) >> 2) & 1) * 65536 + (((i) >> 1) & 1) * 8192, vov, ((i) & 1) * 256, pkv[i]); } while (0)
        f32x16 qa[5] = {}, d1 = {};
        unsigned xo[2], wo_[4];
#pragma unroll
        for (int s = 0; s < 2; ++s) xo[s] = (unsigned)((32 * wid + r32) * 128 + 16 * ((2 * s + hi) ^ ((r32 >> 1) & 7)));
#pragma unroll
        for (int c = 0; c < 4; ++c) wo_[c] = (unsigned)(L_WE + (8 * hi + q4) * 256 + 64 * (c ^ q4) + 32 * (p4 >> 1) + 16 * blk + 8 * (p4 & 1));
        const unsigned eo = (unsigned)(L_WE + 16384 + (8 * hi + q4) * 128 + 64 * ((q4 >> 1) & 1) + 32 * blk + 8 * p4);
        const unsigned vo_ = (unsigned)(L_WE + 16384 + (8 * hi + q4) * 128 + 64 * (((q4 >> 1) & 1) ^ 1) + 8 * p4);
        bf16x8 fx[2], fw[2][6], fg[2];
#define QR_(buf, c, ks, u) do { \
        if ((u) == 0) { fx[buf] = ldsr128(lds + ((c) & 1) * 32768 + (xo[(ks) & 1] ^ (((ks) >> 1) << 6))); fg[buf] = ldsr128(lds + L_GATE + ((c) * 64 + (ks) * 16 + 8 * hi) * 2); } \
        else if ((u) < 5) { fw[buf][(u) - 1] = cat8(trrd(lds + wo_[(u) - 1] + ((c) % 3) * 24576 + (ks) * 4096), trrd(lds + wo_[(u) - 1] + ((c) % 3) * 24576 + (ks) * 4096 + 1024)); } \
        else { fw[buf][4] = cat8(trrd(lds + eo + ((c) % 3) * 24576 + (ks) * 2048), trrd(lds + eo + ((c) % 3) * 24576 + (ks) * 2048 + 512)); \
               fw[buf][5] = cat8(trrd(lds + vo_ + ((c) % 3) * 24576 + (ks) * 2048), trrd(lds + vo_ + ((c) % 3) * 24576 + (ks) * 2048 + 512)); } } while (0)
#define QM_(buf, u) do { if ((u) < 5) qa[u] = MFMA32(fw[buf][u], fx[buf], qa[u]); else d1 = MFMA32(fx[buf], blk ? fg[buf] : fw[buf][5], d1); } while (0)
#define QRD(buf, c, ks) do { _Pragma("unroll") for (int u_ = 0; u_ < 6; ++u_) QR_(buf, c, ks, u_); } while (0)
#define QSTEP(buf, c, ks, rd) do { _Pragma("unroll") for (int u_ = 0; u_ < 6; ++u_) { if (rd) QR_((buf) ^ 1, c, ks, u_); QM_(buf, u_); SBAR(); } } while (0)
#define WV_(n) asm volatile("s_waitcnt vmcnt(%0)" :: "i"(n) : "memory")
#define NST(c) ((c) < 0 ? 0 : 2)
#define ST0(c) (2 * (c))
#define QCHUNK(c) do { \
        SBAR(); QSTEP(0, c, 1, true); QSTEP(1, c, 2, true); \
        if ((c) < 7) { WV_(4 + NST((c) - 1)); } BAR(); asm volatile("" ::: "memory"); \
        if ((c) < 6) WISS((c) + 2); \
        _Pragma("unroll") for (int i_ = 0; i_ < NST(c); ++i_) KVST(ST0(c) + i_); \
        SBAR(); QSTEP(0, c, 3, true); \
        if ((c) < 7) { WV_(((c) < 6 ? 3 : 0) + NST(c)); } \
        SBAR(); QSTEP(1, (c) + 1, 0, (c) < 7); \
        if ((c) < 6) XISS((c) + 2); } while (0)
        if (wid == 0) { WAIT_V(8); } else { WAIT_V(7); } BAR(); asm volatile("" ::: "memory");
        bool hs_ok = true;
        bf16x8 wpf;
#pragma unroll
        for (int j = 0; j < 8; ++j) wpf[j] = (short)(blk ? (wph[j] ^ 0x8000) : wph[j]);
        PIN(wpf);
        QRD(0, 0, 0);
        QCHUNK(0);
        {
            unsigned t0, t1, seen, em;
            asm volatile("v_mov_b32 %0, %5\n\tds_read_b32 %0, %0\n\tv_mov_b32 %1, 1\n\ts_waitcnt lgkmcnt(0)\n\tv_readfirstlane_b32 %2, %0\n\tv_mov_b32 %0, 0\n\ts_nop 3\n\t"
                         "s_min_u32 %3, %8, 1\n\ts_cmp_eq_u32 %7, 0\n\ts_cselect_b32 %3, %3, 0\n\t"
                         "s_cmp_eq_u32 %2, %6\n\ts_cselect_b32 %2, 1, 0\n\ts_and_b32 %3, %3, %2\n\ts_mov_b32 exec_lo, %3\n\ts_mov_b32 exec_hi, 0\n\t"
                         "global_atomic_add %0, %1, %4\n\ts_mov_b64 exec, -1"
                         : "=&v"(t0), "=&v"(t1), "=&s"(seen), "=&s"(em) : "s"(CNT + (80 + (bid & 15)) * 64), "s"(lds0 + 148480), "s"(token), "s"(wid), "s"(bid) : "memory", "scc");
            hs_ok = seen != 0u; }
        QCHUNK(1); QCHUNK(2); QCHUNK(3); QCHUNK(4); QCHUNK(5); QCHUNK(6); QCHUNK(7);
#undef QRD
#undef QR_
#undef QM_
#undef QSTEP
#undef QCHUNK
#undef XISS
#undef WISS
#undef KVST
#undef NST
#undef ST0
#pragma unroll
        for (int cc = 0; cc < 4; ++cc) {
#pragma unroll
            for (int r = 0; r < 16; ++r) qa[cc][r] *= QSCALE;
            qf[2 * cc] = PACK8(qa[cc], 0); qf[2 * cc + 1] = PACK8(qa[cc], 8);
        }
        f32x16 pq = MFMA32(wpf, PACK8(qa[4], 0), f32x16{});
        f32x16 pk = MFMA32(wpf, PACK8(qa[4], 8), f32x16{});
        dual_softmax(pq); dual_softmax(pk);
        f32x16 d3;
        { bf16x8 id0, id1;
#pragma unroll
          for (int j = 0; j < 8; ++j) { const int f = 8 * (j >> 2) + 4 * hi + (j & 3); id0[j] = (short)(r32 == f ? 0x3F80 : 0); id1[j] = (short)(r32 == f + 16 ? 0x3F80 : 0); }
          d3 = MFMA32(PACK8(pk, 0), id0, f32x16{}); d3 = MFMA32(PACK8(pk, 8), id1, d3); }
        const bf16x8 ones = {0x3F80, 0x3F80, 0x3F80, 0x3F80, 0x3F80, 0x3F80, 0x3F80, 0x3F80};
        const bf16x8 v0 = PACK8(d1, 0), v1 = PACK8(d1, 8);
        f32x16 d4 = MFMA32(PACK8(d3, 0), blk ? ones : v0, f32x16{});
        d4 = MFMA32(PACK8(d3, 8), blk ? ones : v1, d4);
        bf16x8 pq0 = PACK8(pq, 0), pq1 = PACK8(pq, 8); PIN(pq0); PIN(pq1); PIN(d4);
        if (wid == 0) handshake_finish(ws, bid, token, hs_ok, lane);
        WAIT_BAR(0);
        if (tid == 0) __hip_atomic_fetch_add(CNT + hh_ * 64, 1u, RLX_AGENT);
        LAS float* red = (LAS float*)(lds + L_V + G * 32768 + w4 * 4096);
        if (r32 == 16) {
#pragma unroll
            for (int r = 0; r < 16; ++r) red[(r & 3) + 8 * (r >> 2) + 4 * hi] = d1[r];
        }
        WAIT_L(0);
        { const float gz = bfround(red[r32]); gate = bfround(1.f / (1.f + __expf(-gz))); }
        PIN(gate);
#pragma unroll
        for (int r = 0; r < 16; ++r) red[((r & 3) + 8 * (r >> 2) + 4 * hi) * 32 + r32] = d4[r];
        ST_PL(pqp, vlin, 0, pq0); ST_PL(pqp, vlin, 1024, pq1);
        if (G == 0) {
#pragma unroll
            for (int s = 0; s < 8; ++s) ST_PL(qbp + (s >> 2) * 4096, vlin, (s & 3) * 1024, qf[s]);
        }
        WAIT_L(0); BAR(); asm volatile("" ::: "memory");
        if (tid < 256) {
            f32x4 s = {0.f, 0.f, 0.f, 0.f};
#pragma unroll
            for (int w = 0; w < 8; ++w) s += *(const LAS f32x4*)(lds + L_V + (w >> 2) * 32768 + (w & 3) * 4096 + tid * 16);
            ST_SC1(ws + WS_KVP + (size_t)(hh_ * 8 + jj) * 4096, (unsigned)tid * 16u, 0, s);
        }
        wait_cnt(CNT + hh_ * 64, 8u);
        asm volatile("" ::: "memory");
#pragma unroll
        for (int e = 0; e < 4; ++e) glds16c(kvw + (size_t)(e >> 1) * 32768 + (2 * G + (e & 1)) * 1024, vlin, ldw + (e >> 1) * 16384 + (2 * G + (e & 1)) * 1024);
        WAIT_BAR(0);
    }
    {
    LANE_VARS();
    const int prow = (r32 & 7) | ((r32 & 8) << 1) | ((r32 & 16) >> 1);
    const unsigned kro = (unsigned)(prow * 16 + hi * 1024), vro = (unsigned)(L_V + r32 * 16 + hi * 2048);
    const int dq0 = 32 * w4;
    const int T = 2 * jj + 2;
    unsigned ks0 = 0u, ks1 = 16384u, ks2 = 32768u;
    int rho = 0; const int TG = G ? T : 99;
    f32x16 o[4], negm, sA0, sA1, sB0, sB1; bf16x8 kf[16], vf[16]; u32x4 pw[4]; float m, l;
#define KRD(i, kp) kf[i] = ldsr128((kp) + ((i) >> 1) * 2048 + ((i) & 1) * 512)
#define VRD(i, vp) vf[i] = ldsr128((vp) + ((i) >> 2) * 4096 + ((i) & 3) * 512)
#define PF(k) __builtin_bit_cast(bf16x8, pw[k])
#define DMA1(i) do { if ((i) < 2) GLDS_IMM(gk_, vlin, dk_, ((i) & 3) * 1024); else if ((i) < 4) GLDS_IMM_UNLESS(gk_, vlin, dk_, ((i) & 3) * 1024, rk_, T); \
                     else if ((i) < 6) GLDS_IMM(gv_, vlin, dv_, ((i) & 3) * 1024); else GLDS_IMM_UNLESS(gv_, vlin, dv_, ((i) & 3) * 1024, rho, T); } while (0)
    const char* gk_; const char* gv_; unsigned dk_, dv_, ko_, vo_; int rk_;
    unsigned go1, go2;
#define GOFF(r) ((r) < T ? (unsigned)(r) * 32768u + G * 2048u : (unsigned)(2 * (r) - T + G) * 32768u)
#define PREP_A(r, k0) do { ko_ = ((r) >= TG ? 49152u : 0u) + (k0); vo_ = ((r) - 1 >= TG ? 32768u : 0u) + (((r) - 1) & 1) * 16384u; } while (0)
#define PREP_B(r, gcur) do { gv_ = kvw + ((gcur) + 16384u); dv_ = ldw + L_V + ((r) & 1) * 16384u + ((r) < T ? G * 2048u : G * 32768u); } while (0)
#define PREP_C(r) do { rk_ = (r) + 2 <= 16 ? (r) + 2 : 16; go1 = go2; go2 = GOFF(rk_); } while (0)
#define PREP_D(k2) do { gk_ = kvw + go2; dk_ = ldw + (k2) + (rk_ < T ? G * 2048u : G * 49152u); } while (0)
#define ROT() do { const unsigned t_ = ks0; ks0 = ks1; ks1 = ks2; ks2 = t_; ++rho; } while (0)
#define GAPA(i, C0, C1, P0, P1) do { \
        if ((i) + PD < 16) { KRD((i) + PD, kp_); } else { VRD((i) + PD - 16, vp_); } \
        if (((i) & 1) == 0) DMA1((i) >> 1); \
        if ((i) & 1) C1 = MFMA32(kf[i], qf[(i) >> 1], (i) < 2 ? negm : C1); else C0 = MFMA32(kf[i], qf[(i) >> 1], (i) < 2 ? negm : C0); \
        if ((i) < 8) { P1[2 * (i)] = EX2(P1[2 * (i)]); P1[2 * (i) + 1] = EX2(P1[2 * (i) + 1]); pw[(i) >> 2][(i) & 3] = cvtpk(P0[2 * (i)], P0[2 * (i) + 1]); PIN(P1[2 * (i)]); PIN(P1[2 * (i) + 1]); PIN(pw[(i) >> 2][(i) & 3]); } \
        else { l += P1[2 * (i) - 16]; l2 += P1[2 * (i) - 15]; pw[(i) >> 2][(i) & 3] = cvtpk(P1[2 * (i) - 16], P1[2 * (i) - 15]); PIN(l); PIN(l2); PIN(pw[(i) >> 2][(i) & 3]); } \
        SBAR(); } while (0)
#define PVM(i) o[(i) & 3] = MFMA32(vf[i], PF((i) >> 2), o[(i) & 3])
#define RDB(i) do { if ((i) + PD < 16) { VRD((i) + PD, vp_); } else { KRD((i) + PD - 16, kn_); } } while (0)
#define ROUND(C0, C1, P0, P1) do { \
        const lds_cptr kp_ = lds + ko_ + kro; const lds_cptr vp_ = lds + vo_ + vro; \
        float l2 = 0.f; lds_cptr kn_ = kp_; SBAR(); \
        GAPA(0, C0, C1, P0, P1); GAPA(1, C0, C1, P0, P1); GAPA(2, C0, C1, P0, P1); GAPA(3, C0, C1, P0, P1); \
        GAPA(4, C0, C1, P0, P1); GAPA(5, C0, C1, P0, P1); GAPA(6, C0, C1, P0, P1); GAPA(7, C0, C1, P0, P1); \
        GAPA(8, C0, C1, P0, P1); GAPA(9, C0, C1, P0, P1); GAPA(10, C0, C1, P0, P1); GAPA(11, C0, C1, P0, P1); \
        GAPA(12, C0, C1, P0, P1); GAPA(13, C0, C1, P0, P1); GAPA(14, C0, C1, P0, P1); GAPA(15, C0, C1, P0, P1); \
        l += l2; \
        ldq_unless(qf, qbp, vlin, (unsigned)(rho + 1 - TG));        \
        RDB(0); PVM(0); SBAR(); RDB(1); PVM(1); SBAR(); \
        if (__builtin_expect(rho == 16 || (rho >= TG - 2 && rho < TG), 0)) mask_tile(C0, C1, dq0 - (rho == 16 ? 64 * G : 64 * (rho - TG + 2))); \
        float ma, mb; \
        RDB(2); PVM(2); ma = MX3(C0[0], C0[1], C1[0]); mb = MX3(C0[2], C0[3], C1[1]); ma = MX3(ma, C1[2], C1[3]); mb = MX3(mb, C0[4], C0[5]); PIN(ma); PIN(mb); SBAR(); \
        RDB(3); PVM(3); ma = MX3(ma, C0[6], C0[7]); mb = MX3(mb, C1[4], C1[5]); ma = MX3(ma, C1[6], C1[7]); mb = MX3(mb, C0[8], C0[9]); PIN(ma); PIN(mb); SBAR(); \
        RDB(4); PVM(4); ma = MX3(ma, C0[10], C0[11]); mb = MX3(mb, C1[8], C1[9]); ma = MX3(ma, C1[10], C1[11]); mb = MX3(mb, C0[12], C0[13]); PIN(ma); PIN(mb); SBAR(); \
        RDB(5); PVM(5); ma = MX3(ma, C0[14], C0[15]); mb = MX3(mb, C1[12], C1[13]); ma = MX3(ma, C1[14], C1[15]); \
        const float rm = swap32_max(fmaxf(ma, mb)); bool pend = false; float fsc = 1.f; \
        if (__builtin_expect(__any(rm > THR) || rho == TG, 0)) { float dl; pend = true; \
            if (rho == TG) { dl = fmaxf(rm, -1e30f); m = dl; fsc = gate / swap32_add(l); l = 0.f; }        \
            else { dl = fmaxf(rm, 0.f); m += dl; fsc = EX2(-dl); l *= fsc; } \
            _Pragma("unroll") for (int e = 0; e < 16; ++e) { C0[e] -= dl; C1[e] -= dl; negm[e] = -m; } } \
        SBAR(); \
        RDB(6); PVM(6); C0[0] = EX2(C0[0]); C0[1] = EX2(C0[1]); PIN(C0[0]); PIN(C0[1]); SBAR(); \
        RDB(7); PVM(7); C0[2] = EX2(C0[2]); C0[3] = EX2(C0[3]); l += C0[0]; l2 = C0[1]; PIN(C0[2]); PIN(C0[3]); PIN(l); SBAR(); \
        RDB(8); PVM(8); PREP_A(rho + 1, ks1); kn_ = lds + ko_ + kro; C0[4] = EX2(C0[4]); C0[5] = EX2(C0[5]); l += C0[2]; l2 += C0[3]; PIN(C0[4]); PIN(C0[5]); PIN(l); PIN(l2); SBAR(); \
        RDB(9); PVM(9); PREP_B(rho + 1, go1); C0[6] = EX2(C0[6]); C0[7] = EX2(C0[7]); l += C0[4]; l2 += C0[5]; PIN(C0[6]); PIN(C0[7]); PIN(l); PIN(l2); SBAR(); \
        RDB(10); PVM(10); PREP_C(rho + 1); C0[8] = EX2(C0[8]); C0[9] = EX2(C0[9]); l += C0[6]; l2 += C0[7]; PIN(C0[8]); PIN(C0[9]); PIN(l); PIN(l2); SBAR(); \
        RDB(11); PVM(11); PREP_D(ks0); C0[10] = EX2(C0[10]); C0[11] = EX2(C0[11]); l += C0[8]; l2 += C0[9]; PIN(C0[10]); PIN(C0[11]); PIN(l); PIN(l2); SBAR(); \
        RDB(12); PVM(12); C0[12] = EX2(C0[12]); C0[13] = EX2(C0[13]); l += C0[10]; l2 += C0[11]; PIN(C0[12]); PIN(C0[13]); PIN(l); PIN(l2); SBAR(); \
        RDB(13); PVM(13); C0[14] = EX2(C0[14]); C0[15] = EX2(C0[15]); l += C0[12]; l2 += C0[13]; PIN(C0[14]); PIN(C0[15]); PIN(l); PIN(l2); SBAR(); \
        RDB(14); PVM(14); l += C0[14]; l2 += C0[15]; PIN(l); PIN(l2); SBAR(); \
        RDB(15); PVM(15); l += l2; PIN(l); SBAR(); \
        if (__builtin_expect(pend || rho + 1 == TG, 0)) { \
            if (pend) { _Pragma("unroll") for (int c = 0; c < 4; ++c) _Pragma("unroll") for (int e = 0; e < 16; ++e) o[c][e] *= fsc; } \
            if (rho + 1 == TG) negm = f32x16{}; \
            if (rho == TG) { const int ln = fresh_lane(); const unsigned vod = (unsigned)((ln >> 5) * 2048 + (32 * w4 + (ln & 31)) * 16); const char* dst = A3 + (size_t)(bB * 576 + hh_ * 16) * 2048; \
                _Pragma("unroll") for (int c = 0; c < 4; ++c) { const bf16x8 t0 = PACK8(o[c], 0), t1 = PACK8(o[c], 8); ST_SC1(dst + c * 8192, vod, 0, t0); ST_SC1(dst + c * 8192 + 4096, vod, 0, t1); o[c] = f32x16{}; } } } \
        asm volatile("s_cmp_eq_u32 %0, %1\n\ts_cbranch_scc1 .Lw%=\n\ts_waitcnt vmcnt(0)\n.Lw%=:\n\ts_waitcnt vmcnt(8)\n\ts_barrier" :: "s"(rho), "s"(TG) : "memory", "scc");     \
        if (__builtin_expect(rho == TG + 1 && w4 == 0, 0)) { if (fresh_lane() == 0) __hip_atomic_fetch_or(CNT + (64 + bB) * 64, 1u << hh_, RLX_AGENT); }        \
        ROT(); } while (0)

#define ROUND0(C0, C1) do { \
        const lds_cptr kp_ = lds + ks0 + kro; const lds_cptr kn_ = lds + ks1 + kro; \
        go2 = GOFF(1); PREP_B(0, GOFF(0)); PREP_C(0); PREP_D(ks2); \
        _Pragma("unroll") for (int i = 0; i < 6; ++i) KRD(i, kp_); \
        _Pragma("unroll") for (int i = 0; i < 8; ++i) DMA1(i); \
        _Pragma("unroll") for (int c = 0; c < 4; ++c) o[c] = f32x16{}; \
        PREP_A(1, ks1); PREP_B(1, go1); PREP_C(1); PREP_D(ks0); SBAR(); \
        _Pragma("unroll") for (int i = 0; i < 16; ++i) { \
            if (i + 6 < 16) KRD(i + 6, kp_); \
            if (i & 1) C1 = MFMA32(kf[i], qf[i >> 1], i < 2 ? f32x16{} : C1); else C0 = MFMA32(kf[i], qf[i >> 1], i < 2 ? f32x16{} : C0); \
            SBAR(); } \
        if (TG == 2) mask_tile(C0, C1, dq0); \
        float ma = MX3(C0[0], C0[1], C1[0]), mb = MX3(C0[2], C0[3], C1[1]); ma = MX3(ma, C1[2], C1[3]); \
        _Pragma("unroll") for (int r = 4; r < 16; r += 4) { ma = MX3(ma, C0[r], C0[r + 1]); mb = MX3(mb, C0[r + 2], C0[r + 3]); ma = MX3(ma, C1[r], C1[r + 1]); mb = MX3(mb, C1[r + 2], C1[r + 3]); } \
        m = fmaxf(swap32_max(fmaxf(ma, mb)), -1e30f); l = 0.f; \
        _Pragma("unroll") for (int i = 0; i < PD; ++i) KRD(i, kn_); \
        _Pragma("unroll") for (int e = 0; e < 16; ++e) { C0[e] = EX2(C0[e] - m); l += C0[e]; C1[e] -= m; negm[e] = -m; } \
        asm volatile("s_waitcnt vmcnt(0)\n\ts_barrier" ::: "memory"); ROT(); } while (0)
#define DRAIN(P0, P1, MID) do { \
        const lds_cptr vp_ = lds + G * 32768 + vro; \
        _Pragma("unroll") for (int i = 0; i < 8; ++i) VRD(i, vp_); \
        _Pragma("unroll") for (int i = 0; i < 8; ++i) pw[i >> 2][i & 3] = cvtpk(P0[2 * i], P0[2 * i + 1]); \
        SBAR(); \
        _Pragma("unroll") for (int i = 0; i < 16; ++i) { \
            if (i + 8 < 16) VRD(i + 8, vp_); \
            if (i == 8) { MID } \
            PVM(i); \
            if (i < 8) { P1[2 * i] = EX2(P1[2 * i]); P1[2 * i + 1] = EX2(P1[2 * i + 1]); PIN(P1); } \
            if (i >= 1 && i < 9) { l += P1[2 * i - 2]; l += P1[2 * i - 1]; pw[2 + ((i - 1) >> 2)][(i - 1) & 3] = cvtpk(P1[2 * i - 2], P1[2 * i - 1]); PIN(l); PIN(pw[2 + ((i - 1) >> 2)]); } \
            SBAR(); } \
        l = swap32_add(l); } while (0)
    ROUND0(sA0, sA1);
    if (wid == 0 && fresh_lane() == 0) __hip_atomic_fetch_add(CNT + (32 + hh_) * 64, 1u, RLX_AGENT);
#pragma clang loop unroll(disable)
    for (int pr = 0; pr < 8; ++pr) { ROUND(sB0, sB1, sA0, sA1); ROUND(sA0, sA1, sB0, sB1); }
    {
        f32x2 ps[8]; bf16x8 phq[2];
        LANE_VARS();
        const unsigned f2 = __hip_atomic_load(CNT + (32 + hh_) * 64, RLX_AGENT);
        DRAIN(sA0, sA1,
            if (rfl(f2) < 8u) wait_cnt(CNT + (32 + hh_) * 64, 8u);
            _Pragma("unroll") for (int w = 0; w < 8; ++w) ps[w] = __builtin_bit_cast(f32x2, __hip_atomic_load((const unsigned long long*)(ws + WS_KVP + (size_t)(hh_ * 8) * 4096 + (unsigned)(w * 4096 + tid * 8)), RLX_AGENT));
            phq[0] = *(const bf16x8*)(pqp + vlin); phq[1] = *(const bf16x8*)(pqp + (vlin + 1024)); );
        const unsigned vod = (unsigned)(hi * 2048 + (32 * w4 + r32) * 16);
        BAR(); asm volatile("" ::: "memory");
        const unsigned st0 = ks2;
        { const lds_ptr xp = lds + L_V + wid * 8192 + lane * 16;
#pragma unroll
          for (int c = 0; c < 2; ++c)
#pragma unroll
              for (int g = 0; g < 4; ++g) { f32x4 t;
#pragma unroll
                  for (int e = 0; e < 4; ++e) t[e] = G ? o[c][4 * g + e] : o[2 + c][4 * g + e];
                  *(LAS f32x4*)(xp + (c * 4 + g) * 1024) = t; }
          *(LAS f32x4*)(lds + st0 + wid * 1024 + lane * 16) = (f32x4){m, l, gate, 0.f};
          f32x2 s = ps[0];
#pragma unroll
          for (int w = 1; w < 8; ++w) s += ps[w];
          *(LAS f32x2*)(lds + st0 + 8192 + tid * 8) = s; }
        WAIT_L(0); BAR(); asm volatile("" ::: "memory");
        { const int ow = wid ^ 4;
          const f32x4 st = *(const LAS f32x4*)(lds + st0 + ow * 1024 + lane * 16);
          const float mn = fmaxf(m, st[0]), f0 = EX2(m - mn), f1 = EX2(st[0] - mn);
          const float gg = G ? st[2] : gate;
          const float inv = gg / (l * f0 + st[1] * f1); const float c0 = f0 * inv, c1 = f1 * inv;
          const lds_cptr xp = lds + L_V + ow * 8192 + lane * 16;
          const char* dst = A3 + (size_t)(bA * 576 + hh_ * 16 + G * 8) * 2048;
#pragma unroll
          for (int c = 0; c < 2; ++c) {
              float v[16];
#pragma unroll
              for (int g = 0; g < 4; ++g) { const f32x4 t = *(const LAS f32x4*)(xp + (c * 4 + g) * 1024);
#pragma unroll
                  for (int e = 0; e < 4; ++e) v[4 * g + e] = (G ? o[2 + c][4 * g + e] : o[c][4 * g + e]) * c0 + t[e] * c1; }
              { const bf16x8 t0 = PACK8(v, 0), t1 = PACK8(v, 8); ST_SC1(dst + c * 8192, vod, 0, t0); ST_SC1(dst + c * 8192 + 4096, vod, 0, t1); }
          }
          float kvv[2][8];
          const int col = r32 < 16 ? r32 : 16;
#pragma unroll
          for (int s = 0; s < 2; ++s)
#pragma unroll
              for (int j = 0; j < 8; ++j) { const int f = 16 * s + 8 * (j >> 2) + 4 * hi + (j & 3); const float v = *(const LAS float*)(lds + st0 + 8192 + (f * 32 + col) * 4); kvv[s][j] = r32 <= 16 ? v : 0.f; }
          f32x16 nm = MFMA32(PACK8(kvv[0], 0), phq[0], f32x16{});
          nm = MFMA32(PACK8(kvv[1], 0), phq[1], nm);
          const float den = bfround(bfround(lo32_bcast(nm[8])) + 1e-6f); const float sc = (1.f - gate) / den;
          float ov[8];
#pragma unroll
          for (int r = 0; r < 8; ++r) ov[r] = bfround(nm[r]) * sc;
          const bf16x8 t0 = PACK8(ov, 0);
          ST_SC1(A3 + (size_t)((G ? bB : bA) * 576 + 512 + 2 * hh_) * 2048, vod, 0, t0);
        }
        WAIT_L(0); BAR(); asm volatile("" ::: "memory");
    }
#undef KRD
#undef VRD
#undef PF
#undef DMA1
#undef ROT
#undef GOFF
#undef PREP_A
#undef PREP_B
#undef PREP_C
#undef PREP_D
#undef GAPA
#undef PVM
#undef RDB
#undef ROUND
#undef ROUND0
#undef DRAIN
    }
    {
        LANE_VARS();
        const int rt = 4 * (bid & 7) + (bid >> 6), ct = (bid >> 3) & 7;
        const char* gA = A3 + (size_t)((rt >> 1) * 576 + 2 * wid) * 2048 + (rt & 1) * 1024;
        const char* gW3 = ws + WS_W3 + (size_t)(ct * 288 + wid) * 2048;
        const unsigned wb = lds0 + wid * 20480;
        const unsigned* rdy = CNT + (64 + (rt >> 1)) * 64;
        auto issue = [&](int u, unsigned slot) {
            const char* ga = gA + (size_t)u * 32768; const char* gw = gW3 + (size_t)u * 16384;
            GLDS_IMM(ga, vlin, wb + slot, 0); GLDS_IMM(ga + 1024, vlin, wb + slot, 1024); GLDS_IMM(gw, vlin, wb + slot + 2048, 0); GLDS_IMM(gw, vlin, wb + slot + 2048, 1024); };
        unsigned seen, seenB; int hnext = 0;
        auto poll = [&]() { const unsigned long long w = __hip_atomic_load((const unsigned long long*)rdy, RLX_AGENT); seen = rfl((unsigned)w); seenB = rfl((unsigned)(w >> 32)); };
        auto next_head = [&]() -> int {
            unsigned spins = 0;
            while (hnext < 32 ? !((seen >> hnext) & 1u) : seenB != ~0u) { poll(); if (++spins > (1u << 20)) break; if (spins > 1) __builtin_amdgcn_s_sleep(2); }
            asm volatile("" ::: "memory");
            return hnext++; };
        poll();
        asm volatile("s_waitcnt vmcnt(0)\n\ts_barrier" ::: "memory");
        if (tid == 0) { __hip_atomic_fetch_or(CNT + (64 + bA) * 64, 1u << hh_, RLX_AGENT); __hip_atomic_fetch_or(CNT + (64 + bB) * 64, 1u << hh_, RLX_AGENT);
                        __hip_atomic_fetch_or(CNT + (64 + bA) * 64 + 1, 1u << hh_, RLX_AGENT); __hip_atomic_fetch_or(CNT + (64 + bB) * 64 + 1, 1u << hh_, RLX_AGENT); }
#pragma unroll
        for (int i = 0; i < 5; ++i) issue(next_head(), i * 4096u);
        f32x16 acc[2][2] = {};
        const unsigned ao = (unsigned)(wid * 20480 + hi * 1024 + r32 * 16);
        unsigned bo[2];
#pragma unroll
        for (int nb = 0; nb < 2; ++nb) bo[nb] = (unsigned)(wid * 20480 + 2048 + (4 * hi + q4) * 128 + 64 * (nb ^ ((q4 >> 1) & 1)) + 32 * blk + 8 * p4);
        bf16x8 af[2][2], bf[2][2];
#define K3RD(buf, sb) do { af[buf][0] = ldsr128((sb) + ao); bf[buf][0] = cat8(trrd((sb) + bo[0]), trrd((sb) + bo[0] + 1024)); \
        af[buf][1] = ldsr128((sb) + ao + 512); bf[buf][1] = cat8(trrd((sb) + bo[1]), trrd((sb) + bo[1] + 1024)); } while (0)
#define K3BODY(buf, i) do { \
        const unsigned snx = sc == 16384u ? 0u : sc + 4096u; \
        if ((i) + 1 < 36) K3RD(buf ^ 1, lds + snx); \
        SBAR(); acc[0][0] = MFMA32(bf[buf][0], af[buf][0], acc[0][0]); acc[1][0] = MFMA32(bf[buf][0], af[buf][1], acc[1][0]); SBAR(); \
        if ((i) + 5 < 36) issue(next_head(), sc); \
        SBAR(); acc[0][1] = MFMA32(bf[buf][1], af[buf][0], acc[0][1]); acc[1][1] = MFMA32(bf[buf][1], af[buf][1], acc[1][1]); SBAR(); \
        sc = snx; } while (0)
#define K3W(n) asm volatile("s_waitcnt vmcnt(" #n ") lgkmcnt(0)" ::: "memory")
        unsigned sc = 0u;
        WAIT_V(16);
        K3RD(0, lds);
        for (int i = 0; i < 32; i += 2) {
            if (G) __builtin_amdgcn_s_setprio(1); K3W(12); K3BODY(0, i);
            if (G) __builtin_amdgcn_s_setprio(0); K3W(12); K3BODY(1, i + 1); }
        K3W(8); K3BODY(0, 32); K3W(4); K3BODY(1, 33); K3W(0); K3BODY(0, 34); K3W(0); K3BODY(1, 35);
#undef K3RD
#undef K3BODY
#undef K3W
        BAR(); asm volatile("" ::: "memory");
#pragma unroll
        for (int mi = 0; mi < 2; ++mi)
#pragma unroll
            for (int nb = 0; nb < 2; ++nb)
#pragma unroll
                for (int g = 0; g < 4; ++g) { const int row = 32 * mi + r32, ch = 8 * nb + 2 * g + hi;
                    *(LAS f32x4*)(lds + wid * 16384 + row * 256 + 16 * (ch ^ (row & 15))) = (f32x4){acc[mi][nb][4 * g], acc[mi][nb][4 * g + 1], acc[mi][nb][4 * g + 2], acc[mi][nb][4 * g + 3]}; }
        WAIT_L(0); BAR(); asm volatile("" ::: "memory");
        { const int row = tid >> 3, c8 = tid & 7; f32x4 s0 = {}, s1 = {};
#pragma unroll
          for (int w = 0; w < 8; ++w) { s0 += *(const LAS f32x4*)(lds + w * 16384 + row * 256 + 16 * ((2 * c8) ^ (row & 15)));
                                        s1 += *(const LAS f32x4*)(lds + w * 16384 + row * 256 + 16 * ((2 * c8 + 1) ^ (row & 15))); }
          u32x4 w = {cvtpk(s0[0], s0[1]), cvtpk(s0[2], s0[3]), cvtpk(s1[0], s1[1]), cvtpk(s1[2], s1[3])};
          *(u32x4*)(out + (size_t)(rt * 64 + row) * 512 + ct * 64 + c8 * 8) = w; }
    }
    asm volatile(".Lcw_end:");
}

extern "C" void kernel_launch(void* const* d_in, const int* in_sizes, int n_in, void* d_out, int out_size, void* d_ws, size_t ws_size,
                              hipStream_t stream) {
    (void)in_sizes; (void)n_in; (void)out_size; (void)ws_size;
    char* ws = (char*)d_ws;
    (void)hipFuncSetAttribute((const void*)kf_all, hipFuncAttributeMaxDynamicSharedMemorySize, LDS_BYTES);
    kf_all<<<256, 512, LDS_BYTES, stream>>>((const u16*)d_in[0], (const u16*)d_in[1], (const u16*)d_in[2], (const u16*)d_in[3], (const u16*)d_in[4], (const u16*)d_in[5],
                                            (const u16*)d_in[7], (const u16*)d_in[8], (const u16*)d_in[9], (const u16*)d_in[10], ws, (u16*)d_out);
}
```
